```python
import torch

def _softmax(x, axis=-1):
    return torch.softmax(x, dim=axis)
def _gelu(x):
    return torch.nn.functional.gelu(x, approximate="tanh")
def _silu(x):
    return torch.nn.functional.silu(x)
def _sigmoid(x):
    return torch.sigmoid(x)
def _layernorm(x, axis=-1, eps=1e-5):
    m = x.mean(dim=axis, keepdim=True)
    v = x.var(dim=axis, keepdim=True, unbiased=False)
    return (x - m) / torch.sqrt(v + eps)
def _scatter_add(tgt, idx, src, axis=0):
    return tgt.clone().index_add_(axis, idx, src)
def _segment_sum(src, seg, n):
    out = torch.zeros((n,) + src.shape[1:], dtype=src.dtype)
    return out.index_add_(0, seg, src)
def _pad(x, pw):
    flat = tuple(v for pair in reversed(pw) for v in pair)
    return torch.nn.functional.pad(x, flat)

kernel_name = 'parallel_sum__linear_attn_block__dense_activation_relu__prompt'

def setup_inputs():

    _w = (lambda a: (lambda t: t.to(torch.bfloat16) if t.is_floating_point() else t)(torch.as_tensor(a)))
    def _A_setup():
        B, S, D = 1, 2048, 512
        return {
            "x": _w((lambda *s: torch.randn(*s, dtype=torch.bfloat16) * 0.05)(B, S, D)),
            "wqkv": _w((lambda *s: torch.randn(*s, dtype=torch.bfloat16) * 0.2)(D, 3 * D)),
            "wo": _w((lambda *s: torch.randn(*s, dtype=torch.bfloat16) * 0.2)(D, D)),
        }
    _A_in = _A_setup()

    def _B_setup():
        B, S, D, Dff = 1, 2048, 512, 1376
        return {
            "x": _w((lambda *s: torch.randn(*s, dtype=torch.bfloat16) * 0.05)(B, S, D)),
            "w1": _w((lambda *s: torch.randn(*s, dtype=torch.bfloat16) * 0.05)(D, Dff)),
            "w2": _w((lambda *s: torch.randn(*s, dtype=torch.bfloat16) * 0.05)(Dff, D)),
        }
    _B_in = _B_setup()

    return {
        "A_x": _A_in["x"],
        "A_wqkv": _A_in["wqkv"],
        "A_wo": _A_in["wo"],
        "B_x": _B_in["x"],
        "B_w1": _B_in["w1"],
        "B_w2": _B_in["w2"],
    }

def reference(A_x, A_wqkv, A_wo, B_x, B_w1, B_w2):
    def _A_ref(x, wqkv, wo):
        B, S, D = x.shape
        H = 32; Dh = D // H
        qkv = (lambda x, *d: x.permute(*d))((x @ wqkv).reshape(B, S, 3, H, Dh), 2, 0, 3, 1, 4)
        q, k, v = qkv[0], qkv[1], qkv[2]
        phi = lambda t: torch.where(t > 0, t + 1, torch.exp(t)) + 1e-6
        pq, pk = phi(q), phi(k)
        kv = torch.einsum("bhsd,bhse->bhde", pk, v)
        z = torch.einsum("bhsd->bhd", pk)
        num = torch.einsum("bhqd,bhde->bhqe", pq, kv)
        den = torch.einsum("bhqd,bhd->bhq", pq, z)[..., None] + 1e-6
        o = num / den
        return (lambda x, *d: x.permute(*d))(o, 0, 2, 1, 3).reshape(B, S, D) @ wo

    def _B_ref(x, w1, w2):
        h = torch.einsum("bsd,df->bsf", x, w1)
        zero = torch.zeros_like(h)
        h = torch.maximum(h, zero)
        return torch.einsum("bsf,fd->bsd", h, w2)

    x = A_x
    xn = _layernorm(x, axis=-1)
    return x + _A_ref(xn, A_wqkv, A_wo) + _B_ref(xn, B_w1, B_w2)
```

```cpp
#include <hip/hip_runtime.h>
#include <hip/hip_bf16.h>
typedef unsigned short u16;
__device__ __forceinline__ float bf2f(u16 v){ return __uint_as_float(((unsigned)v)<<16); }
__device__ __forceinline__ u16 f2bf(float f){ __hip_bfloat16 b = __float2bfloat16(f); return *reinterpret_cast<u16*>(&b); }
__device__ __forceinline__ float rbf(float f){ return bf2f(f2bf(f)); }

#define MIMIC 0
constexpr int S=2048, D=512, F=1376, H=32, DH=16;

__global__ void k_ln(const u16* x, float* xn){
  int row=blockIdx.x; int t=threadIdx.x;
  float v[8]; float s=0;
  for(int i=0;i<8;i++){ v[i]=bf2f(x[row*D+t*8+i]); s+=v[i]; }
  for(int o=32;o>0;o>>=1) s+=__shfl_xor(s,o);
  float m=s/D;
  float q=0; for(int i=0;i<8;i++){ float d=v[i]-m; q+=d*d; }
  for(int o=32;o>0;o>>=1) q+=__shfl_xor(q,o);
  float var=q/D;
  float r=rsqrtf(var+1e-5f);
  for(int i=0;i<8;i++) xn[row*D+t*8+i]=(v[i]-m)*r;
}
__global__ void k_gemm(const float* A, const u16* B, float* C, int N, int K, int mode){
  __shared__ float As[16][17]; __shared__ float Bs[16][17];
  int tx=threadIdx.x&15, ty=threadIdx.x>>4;
  int row=blockIdx.y*16+ty, col=blockIdx.x*16+tx;
  float acc=0;
  for(int k0=0;k0<K;k0+=16){
    As[ty][tx]=A[row*K+k0+tx];
    Bs[ty][tx]=bf2f(B[(k0+ty)*N+col]);
    __syncthreads();
    for(int k=0;k<16;k++) acc+=As[ty][k]*Bs[k][tx];
    __syncthreads();
  }
  if(mode==1) acc=fmaxf(acc,0.f);
  C[row*N+col]=acc;
}
__device__ __forceinline__ float phi(float t){
  return (t>0.f ? t+1.f : __expf(t)) + 1e-6f;
}
__global__ void k_kv(const float* qkv, float* kv, float* z){
  int h=blockIdx.x; int t=threadIdx.x;
  int d=t>>4,e=t&15; float acc=0, zz=0;
  for(int s=0;s<S;s++){ float pk=phi(qkv[s*1536+512+h*16+d]); acc+=pk*qkv[s*1536+1024+h*16+e]; zz+=pk; }
  kv[h*256+t]=acc; if(e==0) z[h*16+d]=zz;
}
__global__ void k_o(const float* qkv, const float* kv, const float* z, float* o){
  int s=blockIdx.x; int t=threadIdx.x;
  int h=t>>4,e=t&15; float num=0,den=0;
  for(int d=0;d<16;d++){ float pq=phi(qkv[s*1536+h*16+d]); num+=pq*kv[h*256+d*16+e]; den+=pq*z[h*16+d]; }
  o[s*D+t]=num/(den+1e-6f);
}
__global__ void k_fin(const u16* x, const float* a, const float* b, u16* out){
  int i=blockIdx.x*256+threadIdx.x;
  out[i]=f2bf(bf2f(x[i])+a[i]+b[i]);
}
extern "C" void kernel_launch(void* const* d_in, const int* in_sizes, int n_in, void* d_out, int out_size, void* d_ws, size_t ws_size, hipStream_t stream){
  const u16* x=(const u16*)d_in[0]; const u16* wqkv=(const u16*)d_in[1]; const u16* wo=(const u16*)d_in[2];
  const u16* w1=(const u16*)d_in[4]; const u16* w2=(const u16*)d_in[5];
  float* ws=(float*)d_ws;
  float* xn=ws; float* qkv=xn+S*D; float* hh=qkv+S*1536; float* kv=hh+S*F; float* z=kv+32*256; float* o=z+512; float* a=o+S*D; float* b=a+S*D;
  k_ln<<<S,64,0,stream>>>(x,xn);
  k_gemm<<<dim3(1536/16,S/16),256,0,stream>>>(xn,wqkv,qkv,1536,D,0);
  k_gemm<<<dim3(F/16,S/16),256,0,stream>>>(xn,w1,hh,F,D,1);
  k_kv<<<32,256,0,stream>>>(qkv,kv,z);
  k_o<<<S,512,0,stream>>>(qkv,kv,z,o);
  k_gemm<<<dim3(D/16,S/16),256,0,stream>>>(o,wo,a,D,D,0);
  k_gemm<<<dim3(D/16,S/16),256,0,stream>>>(hh,w2,b,D,F,0);
  k_fin<<<S*D/256,256,0,stream>>>(x,a,b,(u16*)d_out);
}
```

```cpp
#include <hip/hip_runtime.h>
#include <hip/hip_bf16.h>
#include <type_traits>

typedef unsigned u32;
typedef unsigned long long u64;
typedef __bf16 bf16x8 __attribute__((ext_vector_type(8)));
typedef __bf16 bf16x2 __attribute__((ext_vector_type(2)));
typedef short s16x4 __attribute__((ext_vector_type(4)));
typedef short s16x2 __attribute__((ext_vector_type(2)));
typedef float f32x16 __attribute__((ext_vector_type(16)));
typedef float f32x4 __attribute__((ext_vector_type(4)));
typedef float f32x2 __attribute__((ext_vector_type(2)));
typedef u32 u32x4 __attribute__((ext_vector_type(4)));
typedef u32 u32x2 __attribute__((ext_vector_type(2)));
#define LAS __attribute__((address_space(3)))
#define GAS __attribute__((address_space(1)))
#define DEV __device__ __forceinline__
#define RLX_AGENT __ATOMIC_RELAXED, __HIP_MEMORY_SCOPE_AGENT
#define LDSP(T, a) ((LAS T*)(size_t)(a))

#define ROT1 1
#define NT1 0
#define NT2 0

constexpr u32 WS_EPOCH = 0;
constexpr u32 WS_STATS = 4096;
constexpr u32 WS_FLAGS = WS_STATS + 16384;
constexpr u32 WS_KVP = WS_FLAGS + 32 * 48 * 128;
constexpr u32 KVH_B = 17408;
constexpr u32 WS_A = 1u << 20;
constexpr u32 NSTEP = 118;
constexpr u32 WS_END = WS_A + 64 * NSTEP * 1024;
constexpr u32 WS_STAMP = 16u << 20;
static_assert(WS_KVP + 32 * KVH_B <= WS_A, "ws map");
constexpr u32 OOB = 0x40000000u;

constexpr u32 L_SCR = 131072;
constexpr u32 L_CORRA = L_SCR;
constexpr u32 L_CORRB = L_SCR + 512;
constexpr u32 L_MS = L_SCR + 1536;
constexpr u32 L_RL = L_SCR + 2048;
constexpr u32 L_SYNC = L_SCR + 2560;
constexpr u32 L_GRAN = L_SCR + 3072;
constexpr u32 L_KVF = L_SCR + 4096;
constexpr u32 L_XCH = L_SCR + 8192;
constexpr u32 L_TOTAL = 163840;

constexpr u32 CODE_LINES = 184;
constexpr u32 SPIN_CAP = 1u << 18;
constexpr float LOG2E = 1.4426950408889634f, LN2 = 0.6931471805599453f;

DEV void sfence() { __builtin_amdgcn_sched_barrier(0); }
template <int N> DEV void wait_vm() { asm volatile("s_waitcnt vmcnt(%0)" ::"i"(N) : "memory"); }
DEV void wait_lgkm() { sfence(); __builtin_amdgcn_s_waitcnt(0xC07F); asm volatile("" ::: "memory"); sfence(); }
DEV void barrier() { sfence(); __builtin_amdgcn_s_waitcnt(0xC07F); asm volatile("s_barrier" ::: "memory"); sfence(); }

DEV u32x4 make_srd(const void* p, u32 bytes) {
  u64 a = (u64)p;
  return (u32x4){(u32)a, (u32)(a >> 32) & 0xffffu, bytes, 0x00020000u};
}
template <int POL> DEV void dma(u32x4 srd, u32 voff, u32 soff, u32 ldsaddr) {
  if (POL == 1)
    asm volatile("s_mov_b32 m0, %3\n\ts_nop 0\n\tbuffer_load_dwordx4 %0, %1, %2 offen nt lds" ::"v"(voff), "s"(srd), "s"(soff), "s"(ldsaddr) : "memory");
  else if (POL == 2)
    asm volatile("s_mov_b32 m0, %3\n\ts_nop 0\n\tbuffer_load_dwordx4 %0, %1, %2 offen sc1 lds" ::"v"(voff), "s"(srd), "s"(soff), "s"(ldsaddr) : "memory");
  else
    asm volatile("s_mov_b32 m0, %3\n\ts_nop 0\n\tbuffer_load_dwordx4 %0, %1, %2 offen lds" ::"v"(voff), "s"(srd), "s"(soff), "s"(ldsaddr) : "memory");
}
template <int POL> DEV void dma4(u32x4 srd, u32x4 v, u32 soff, u32 ldsaddr) {
  if (POL == 1)
    asm volatile("s_mov_b32 m0, %6\n\ts_nop 0\n\tbuffer_load_dwordx4 %0, %4, %5 offen nt lds\n\tbuffer_load_dwordx4 %1, %4, %5 offen offset:1024 nt lds\n\t"
                 "buffer_load_dwordx4 %2, %4, %5 offen offset:2048 nt lds\n\tbuffer_load_dwordx4 %3, %4, %5 offen offset:3072 nt lds"
                 ::"v"(v.x), "v"(v.y), "v"(v.z), "v"(v.w), "s"(srd), "s"(soff), "s"(ldsaddr) : "memory");
  else
    asm volatile("s_mov_b32 m0, %6\n\ts_nop 0\n\tbuffer_load_dwordx4 %0, %4, %5 offen lds\n\tbuffer_load_dwordx4 %1, %4, %5 offen offset:1024 lds\n\t"
                 "buffer_load_dwordx4 %2, %4, %5 offen offset:2048 lds\n\tbuffer_load_dwordx4 %3, %4, %5 offen offset:3072 lds"
                 ::"v"(v.x), "v"(v.y), "v"(v.z), "v"(v.w), "s"(srd), "s"(soff), "s"(ldsaddr) : "memory");
}
DEV u32x4 steps4(u32 v, u32 step) { step -= 1024; return (u32x4){v, v + step, v + 2 * step, v + 3 * step}; }
DEV void store_one(u32x4 v, u32x4 srd, u32 soff) {
  asm volatile("s_mov_b64 exec, 1\n\tbuffer_store_dwordx4 %0, off, %1, %2 sc1\n\ts_mov_b64 exec, -1\n\ts_nop 0" ::"v"(v), "s"(srd), "s"(soff) : "memory");
}
DEV void store_dev(u32x4 v, u32x4 srd, u32 voff, u32 soff) {
  asm volatile("buffer_store_dwordx4 %0, %1, %2, %3 offen sc1\n\ts_nop 1" ::"v"(v), "v"(voff), "s"(srd), "s"(soff) : "memory");
}
#define LDSO(T, a, o) ((LAS T*)(LDSP(char, a) + (o)))
DEV bf16x8 lds_b128(u32 a, int o = 0) { return *LDSO(bf16x8, a, o); }
template <int D> DEV bf16x8 lds_tr(u32 a, int o) {
  s16x4 lo = __builtin_amdgcn_ds_read_tr16_b64_v4i16(LDSO(s16x4, a, o));
  s16x4 hi = __builtin_amdgcn_ds_read_tr16_b64_v4i16(LDSO(s16x4, a, o + D));
  return __builtin_bit_cast(bf16x8, __builtin_shufflevector(lo, hi, 0, 1, 2, 3, 4, 5, 6, 7));
}
DEV f32x16 mfma32(bf16x8 a, bf16x8 b, f32x16 c) { return __builtin_amdgcn_mfma_f32_32x32x16_bf16(a, b, c, 0, 0, 0); }
DEV f32x4 mfma16(bf16x8 a, bf16x8 b, f32x4 c) { return __builtin_amdgcn_mfma_f32_16x16x32_bf16(a, b, c, 0, 0, 0); }
DEV bf16x8 as_bf(u32x4 v) { return __builtin_bit_cast(bf16x8, v); }
DEV u32 pack2(float a, float b) { return __builtin_bit_cast(u32, __builtin_convertvector((f32x2){a, b}, bf16x2)); }
DEV u32 relu2(u32 v) { return __builtin_bit_cast(u32, __builtin_elementwise_max(__builtin_bit_cast(s16x2, v), (s16x2){0, 0})); }
DEV float lo_f(u32 v) { return __uint_as_float(v << 16); }
DEV float hi_f(u32 v) { return __uint_as_float(v & 0xffff0000u); }
DEV float rbf(float f) { return lo_f(pack2(f, 0.f)); }
DEV u32 split_hi_lo(float c) { float h = rbf(c); return pack2(h, c - h); }
DEV float mul1(float a, float b) { float r = a * b; asm("" : "+v"(r)); return r; }
template <int CTRL> DEV float dpp_add(float v) {
  return v + __uint_as_float(__builtin_amdgcn_update_dpp(0u, __float_as_uint(v), CTRL, 0xf, 0xf, true));
}
DEV float swap16_add(float v) {
  auto r = __builtin_amdgcn_permlane16_swap(__float_as_uint(v), __float_as_uint(v), false, false);
  return __uint_as_float(r[0]) + __uint_as_float(r[1]);
}
DEV float swap32_add(float v) {
  auto r = __builtin_amdgcn_permlane32_swap(__float_as_uint(v), __float_as_uint(v), false, false);
  return __uint_as_float(r[0]) + __uint_as_float(r[1]);
}
DEV float sum32(float v) {
  v = dpp_add<0xB1>(v); v = dpp_add<0x4E>(v); v = dpp_add<0x141>(v); v = dpp_add<0x140>(v);
  return swap16_add(v);
}
DEV u32x4 phi8(const float (&t2)[8]) {
  float e[8], m[8];
#pragma unroll
  for (int i = 0; i < 8; ++i) e[i] = __builtin_amdgcn_fmed3f(__builtin_amdgcn_exp2f(t2[i]), 0.f, 1.f);
  sfence();
#pragma unroll
  for (int i = 0; i < 8; ++i) asm("v_max_f32 %0, %1, %2" : "=v"(m[i]) : "s"(1e-6f * LOG2E), "v"(t2[i]));
  sfence();
#pragma unroll
  for (int i = 0; i < 8; ++i) { e[i] = __builtin_fmaf(m[i], LN2, e[i]); asm("" : "+v"(e[i])); }
  sfence();
  return (u32x4){pack2(e[0], e[1]), pack2(e[2], e[3]), pack2(e[4], e[5]), pack2(e[6], e[7])};
}
DEV void lds_signal(u32 addr) {
  asm volatile("s_mov_b64 exec, 1\n\tds_add_u32 %0, %1\n\ts_mov_b64 exec, -1" ::"v"(addr), "v"(1u) : "memory");
}
DEV void lds_wait(u32 addr, u32 waves) {
#pragma unroll 1
  for (u32 i = 0; i < SPIN_CAP; ++i) {
    u32 v;
    asm volatile("ds_read_b32 %0, %1\n\ts_waitcnt lgkmcnt(0)" : "=v"(v) : "v"(addr) : "memory");
    if ((u32)__builtin_amdgcn_readfirstlane(v) >= waves) break;
    __builtin_amdgcn_s_sleep(1);
  }
}
DEV bool try4(const char* p, u64 tag) {
    u64 f0, f1, f2, f3;
    asm volatile("s_load_dwordx2 %0, %4, 0 glc\n\ts_load_dwordx2 %1, %4, 128 glc\n\ts_load_dwordx2 %2, %4, 256 glc\n\t"
                 "s_load_dwordx2 %3, %4, 384 glc\n\ts_waitcnt lgkmcnt(0)"
                 : "=&s"(f0), "=&s"(f1), "=&s"(f2), "=&s"(f3) : "s"(p) : "memory");
    return f0 == tag && f1 == tag && f2 == tag && f3 == tag;
}
DEV void poll4(const char* p, u64 tag) {
#pragma unroll 1
  for (u32 i = 0; i < SPIN_CAP; ++i)
    if (try4(p, tag)) break;
}

#define STAMP(i)
#undef FSTAMP
#define FSTAMP(w, i)

template <bool XT>
DEV void phase_a_compute(f32x16 (&acc)[2][2], bf16x8 (&xf)[4][2], bf16x8 (&wf)[4][2], f32x4 (&rl)[8], u32 lane, u32 mh, u32 nh) {
  const u32 hh = lane >> 5, q = (lane & 15) >> 2;
  u32 xa = (mh * 64 + (lane & 31)) * 128 + ((hh ^ ((lane >> 1) & 7)) << 4);
  u32 wa0 = 16384 + (8 * hh + q) * 256 + (lane & 16) * 2 + (lane & 3) * 8 + ((XT ? (nh ^ q) : ((nh - q) & 3)) << 6);
  u32 wa1 = wa0 ^ 128;
  const u32 selv = (((lane >> 4) ^ (lane >> 2)) & 1) ? 0u : 0x3f803f80u;
  const bf16x8 sel = as_bf((u32x4){selv, selv, selv, selv});
  const u32 ca = L_CORRA + (nh * 32 + (lane & 31)) * 4, ma = L_MS + (mh * 64 + (lane & 31)) * 4;
  f32x4 cs = {};
  auto ldx = [&](int s, int h) { xf[s][h] = lds_b128(xa ^ (s << 5), h * 4096); sfence(); };
  auto ldw = [&](int s, int h) { wf[s][h] = lds_tr<1024>(h ? wa1 : wa0, s * 4096); sfence(); };
  auto m1 = [&](int s, int i) {
    const int t = (i + 1) >> 1 & 1, u = i >> 1;
    acc[t][u] = XT ? mfma32(xf[s][t], wf[s][u], acc[t][u]) : mfma32(wf[s][u], xf[s][t], acc[t][u]);
    sfence();
  };
#pragma unroll 1
  for (u32 st = 0; st < 8; ++st) {
    m1(2, 0); ldw(0, 0); m1(2, 1); ldx(0, 0); ldx(0, 1); m1(2, 2); ldw(0, 1); m1(2, 3); ldw(1, 0);
    m1(3, 0); ldx(1, 0); ldx(1, 1); m1(3, 1); ldw(1, 1); m1(3, 2); ldw(2, 0); m1(3, 3); ldx(2, 0); ldx(2, 1);
    m1(0, 0); ldw(2, 1); m1(0, 1); ldw(3, 0); m1(0, 2); ldx(3, 0); ldx(3, 1); m1(0, 3); ldw(3, 1);
    m1(1, 0);
    xa = (xa + 32768) & 131071; wa0 = (wa0 + 32768) & 131071; wa1 = (wa1 + 32768) & 131071;
    asm volatile("" : "+v"(xa), "+v"(wa0), "+v"(wa1));
    sfence();
    m1(1, 1); m1(1, 2); m1(1, 3);
    if (mh) {
#pragma unroll
      for (int s = 0; s < 4; ++s) cs = mfma16(sel, wf[s][1], cs);
    } else {
#pragma unroll
      for (int s = 0; s < 4; ++s) cs = mfma16(sel, wf[s][0], cs);
    }
    if (st == 7) *LDSP(u32, ca + mh * 256) = split_hi_lo(cs[0]);
    barrier();
  }
  __builtin_amdgcn_s_setprio(2);
  m1(2, 0);
  u32 c0 = *LDSP(u32, ca), c1 = *LDSO(u32, ca, 256);
  u32 n0 = *LDSP(u32, ma), n1 = *LDSO(u32, ma, 128);
  sfence();
  m1(2, 3);
  if (XT) {
    u32 ra = L_RL + (mh * 64 + hh * 4) * 4;
    asm volatile("" : "+v"(ra));
#pragma unroll
    for (int i = 0; i < 8; ++i) rl[i] = *LDSO(f32x4, ra, i * 32);
  }
  sfence();
  m1(3, 0); m1(3, 3);
  n0 = (n0 & 0xffffu) ^ 0x8000u; n1 = (n1 & 0xffffu) ^ 0x8000u;
  xf[0][0] = as_bf((u32x4){hh ? 0u : (n0 | (n0 << 16)), 0u, 0u, 0u});
  wf[0][0] = as_bf((u32x4){c0, c0, c0, c0});
  asm volatile("" : "+v"(xf[0][0]), "+v"(wf[0][0]));
  sfence();
  m1(2, 1);
  xf[0][1] = as_bf((u32x4){hh ? 0u : (n1 | (n1 << 16)), 0u, 0u, 0u});
  wf[0][1] = as_bf((u32x4){c1, c1, c1, c1});
  asm volatile("" : "+v"(xf[0][1]), "+v"(wf[0][1]));
  sfence();
  m1(0, 0); m1(0, 3); m1(2, 2); m1(3, 1); m1(3, 2); m1(0, 1); m1(0, 2);
}

__global__ __launch_bounds__(512, 2) void fused_block(const char* __restrict__ x, const char* __restrict__ wqkv, const char* __restrict__ w1,
                                                      char* ws, const char* __restrict__ w2, const char* __restrict__ wo, char* __restrict__ out) {
  __shared__ __attribute__((aligned(16))) char lds_all[L_TOTAL];
  u64 pc;
  asm volatile("s_getpc_b64 %0" : "=s"(pc) : "s"((u32)(size_t)lds_all));
  STAMP(0);
  asm volatile("" ::"s"(x), "s"(wqkv), "s"(w1), "s"(ws), "s"(w2), "s"(wo), "s"(out));
  const u32 b = blockIdx.x, xcd = b & 7, r = b >> 3;
  const u32 lane = threadIdx.x & 63, w = __builtin_amdgcn_readfirstlane(threadIdx.x >> 6);
  const u32 rt = (xcd & 1) * 8 + (r & 7), c4 = (xcd >> 1) * 4 + (r >> 3), g = c4 >> 1;
  const bool xt = (c4 & 1) == 0;
  const u32 hh = lane >> 5, q = (lane & 15) >> 2, kq = w & 3;
  const __amdgpu_buffer_rsrc_t rws = __builtin_amdgcn_make_buffer_rsrc(ws, 0, WS_END, 0x00020000);
  const u32x4 srd_ws = make_srd(ws, WS_END);
  u64 tag;
  const u32 v128 = ((lane & 7) ^ ((lane >> 2) & 4)) << 4;
  const u32 trl = q * 128 + (lane & 16) * 2 + (lane & 3) * 8, trs = (q & 2) << 5;
  auto read_epoch = [&]() {
    u64 e; u32 d0, d1, d2, d3;
    const u64 cp = (pc & ~127ull) + min(r * 8 + w * 2, CODE_LINES - 2) * 128;
    asm volatile("s_load_dwordx2 %0, %5, 0\n\ts_load_dword %1, %6, 0\n\ts_load_dword %2, %6, 64\n\ts_load_dword %3, %6, 128\n\t"
                 "s_load_dword %4, %6, 192\n\ts_waitcnt lgkmcnt(0)"
                 : "=&s"(e), "=&s"(d0), "=&s"(d1), "=&s"(d2), "=&s"(d3) : "s"(ws), "s"(cp) : "memory");
    tag = e + 0x9E3779B97F4A7C15ull;
  };

  const u32 crt = xcd * 4 + (r & 3), ct = r >> 2;
  const u32 tok = crt * 64 + w * 8 + (lane >> 3);
  u32 st;
  u32x4 xr;
  const u32 reg2 = (w ^ 4) * 16384;
  const u32 ra0 = reg2 + hh * 512 + trl + trs, ra1 = ra0 ^ 64;
  bf16x8 w2f[8][2];
  bool ready = false;
  auto w2_chunk = [&](auto G0_, int half, int i) {
    constexpr bool G0 = G0_;
    const u32x4 vw2 = steps4((lane >> 3) * 1024 + v128, 8192);
    if (G0) dma4<NT2>(make_srd(w2, 1376 * 1024), vw2, (16 * kq + 8 * half + 2 * i) * 16384 + ct * 128, reg2 + i * 4096);
    else if (half == 0 && i < 3) dma4<NT2>(make_srd(w2, 1376 * 1024), vw2, (64 + 6 * kq + 2 * i) * 16384 + ct * 128, reg2 + i * 4096);
    else dma4<NT2>(make_srd(wo, 512 * 1024), vw2, (8 * kq + 2 * i) * 16384 + ct * 128, reg2 + i * 4096);
  };
  auto w2_second = [&](auto G0_, auto WAIT_) {
    constexpr bool G0 = G0_, WAIT = WAIT_;
#pragma unroll
    for (int i = 0; i < (G0 ? 4 : 3); ++i) {
      if (i == (G0 ? 3 : 2)) ready = try4(ws + WS_FLAGS + (crt * 48 + (G0 ? 0 : 16) + kq * 4) * 128, tag);
      if (WAIT) wait_vm<12>();
#pragma unroll
      for (int s = 2 * i; s < 2 * i + 2; ++s) { w2f[s][0] = lds_tr<1024>(ra0, s * 2048); w2f[s][1] = lds_tr<1024>(ra1, s * 2048); }
      wait_lgkm();
      w2_chunk(G0_, 1, i);
    }
  };
  auto publish_fetch = [&](auto G0_, u32 flag_off) {
    w2_chunk(G0_, 0, 0);
    wait_vm<4>();
    store_one((u32x4){(u32)tag, (u32)(tag >> 32), 0u, 0u}, srd_ws, flag_off);
    __builtin_amdgcn_s_setprio(0);
    STAMP(4);
    w2_chunk(G0_, 0, 1); w2_chunk(G0_, 0, 2); w2_chunk(G0_, 0, 3);
    w2_second(G0_, std::true_type{});
    STAMP(5);
  };
  auto second = [&](auto G0_) {
  constexpr bool G0 = G0_;
  const u32 sb0 = G0 ? 16 * kq : 64 + 6 * kq, n0 = G0 ? 8 : (kq < 3 ? 6 : 4);
  const u32 sb1 = G0 ? 16 * kq + 8 : 86 + 8 * kq;
  STAMP(7);
  {
    const char* fp = ws + WS_FLAGS + (crt * 48 + (G0 ? 0 : 16) + kq * 4) * 128;
    if (!ready) poll4(fp, tag);
    STAMP(8);
    const u32 a0 = WS_A + (crt * 2 * NSTEP + sb0) * 1024, a1 = WS_A + (crt * 2 * NSTEP + sb1) * 1024;
    u32x4 af[16][2];
    auto lda = [&](int s) {
      const u32 so = s < 8 ? ((u32)s < n0 ? a0 + s * 1024 : OOB) : a1 + (s - 8) * 1024;
      af[s][0] = __builtin_amdgcn_raw_buffer_load_b128(rws, lane * 16, so, 16);
      af[s][1] = __builtin_amdgcn_raw_buffer_load_b128(rws, lane * 16 + NSTEP * 1024, so, 16);
      sfence();
    };
    auto ldres = [&]() {
      st = __hip_atomic_load((GAS u32*)(ws + WS_STATS + tok * 8), RLX_AGENT);
      xr = *(const GAS u32x4*)(x + tok * 1024 + ct * 128 + (lane & 7) * 16);
      sfence();
    };
    f32x16 acc[2][2] = {};
    auto ldw = [&](int s) { w2f[s][0] = lds_tr<1024>(ra0, s * 2048); w2f[s][1] = lds_tr<1024>(ra1, s * 2048); sfence(); };
    auto mm = [&](int s) {
#pragma unroll
      for (int t = 0; t < 2; ++t) {
        acc[t][0] = mfma32(w2f[s & 7][0], as_bf(af[s][t]), acc[t][0]);
        acc[t][1] = mfma32(w2f[s & 7][1], as_bf(af[s][t]), acc[t][1]);
      }
      sfence();
    };
    if constexpr (G0) {
#pragma unroll
      for (int s = 0; s < 12; ++s) lda(s);
      STAMP(9);
#pragma unroll
      for (int s = 0; s < 4; ++s) { mm(s); lda(12 + s); ldw(s); }
      ldres();
#pragma unroll
      for (int s = 4; s < 8; ++s) { mm(s); ldw(s); }
    } else {
#pragma unroll
      for (int s = 0; s < 6; ++s) lda(s);
      ldres();
#pragma unroll
      for (int s = 0; s < 6; ++s) { mm(s); ldw(s); }
      ldw(6); ldw(7);
      poll4(fp + 16 * 128, tag);
      STAMP(9);
#pragma unroll
      for (int s = 8; s < 16; ++s) lda(s);
    }
#pragma unroll
    for (int s = 8; s < 16; ++s) mm(s);
    if constexpr (G0) __builtin_amdgcn_s_waitcnt(0x0F70);
    STAMP(10);
    const u32 da = reg2 + (lane & 31) * 256 + ((hh ^ (lane & 7)) << 4);
#pragma unroll
    for (int t = 0; t < 2; ++t)
#pragma unroll
      for (int c = 0; c < 2; ++c)
#pragma unroll
        for (int r4 = 0; r4 < 4; ++r4)
          *LDSO(f32x4, da ^ (r4 << 5), t * 8192 + c * 128) =
              (f32x4){acc[t][c][4 * r4], acc[t][c][4 * r4 + 1], acc[t][c][4 * r4 + 2], acc[t][c][4 * r4 + 3]};
  }
  };
  if (w >= 4) {
    const u32 j = kq;
    const u32 rot = ROT1 == 0 ? 0 : ROT1 == 1 ? (r & 7) : (r & 7) + 2 * (r >> 3);
    const u32x4 srd_x = make_srd(x, 2048 * 1024);
    const u32 vx = (lane >> 3) * 1024 + (((lane & 7) ^ (lane >> 4)) << 4);
    const u32x4 vx4 = {vx, (vx ^ 64) + 7168, vx + 14336, (vx ^ 64) + 21504};
    auto issue_x = [&](int st) { dma4<NT1>(srd_x, vx4, (rt * 128 + 32 * j) * 1024 + ((st + rot) & 7) * 128, (st & 3) * 32768 + j * 4096); };
    issue_x(0);
    sfence();
    const u32 wst = xt ? 3072 : 2752;
    const u32x4 srd_w = make_srd(xt ? wqkv : w1, 512 * wst);
    const u32 ym = xt ? 0u : ~0u, l4 = lane >> 4;
    const u32 blk = ((q ^ l4) + (((q & l4) << 1) & ym)) & 3;
    const u32 vwa = l4 * wst + (lane & 3) * 16 + (blk & 1) * 64 + (blk >> 1) * (xt ? 1024 : 128) + (xt ? 1024 + g * 128 : g * 256);
    const u32x4 vwa4 = steps4(vwa, 4 * wst);
    auto issue_w = [&](int st) { dma4<NT1>(srd_w, vwa4, (((st + rot) & 7) * 64 + 16 * j) * wst, (st & 3) * 32768 + 16384 + j * 4096); };
    issue_w(0);
    sfence();
    const u32 nl = (g < 6 && !(g & 1)) ? 2 : 1, lstep = 64 + (g >> 1) * 6 + ((g & 1) ? (g < 6 ? 4 : 2) : 0);
    const u32 vwb = (lane >> 3) * wst + (v128 & ((xt || nl == 2) ? 127 : 48)) + (xt ? g * 128 : lstep * 32);
    auto issue = [&](int st, int part) {
      if (st < 8) { if (part) issue_w(st); else issue_x(st); }
      else dma4<NT1>(srd_w, steps4(vwb, 8 * wst), ((((st - 8) * 4 + j + rot) & 7) * 64 + 32 * part) * wst, (st - 8) * 32768 + j * 8192 + part * 4096);
    };
    issue(1, 0); issue(1, 1);
    const u32 xa = (j * 32 + (lane & 31)) * 128 + ((hh ^ ((lane >> 1) & 7)) << 4);
    const u32 selv = (((lane >> 4) ^ (lane >> 2)) & 1) ? 0u : 0x3f803f80u;
    const bf16x8 sel = as_bf((u32x4){selv, selv, selv, selv});
    bf16x8 xf[32];
    u32 ms;
#pragma unroll
    for (int st = 0; st < 8; ++st) {
      wait_vm<8>();
      barrier();
      if (st == 0) STAMP(1);
      const u32 slot = (st & 3) * 32768;
      if (st == 4) dma<2>(srd_ws, lane * 16, j == 0 ? WS_STATS + rt * 1024 : OOB, j == 0 ? L_GRAN : L_XCH);
      issue(st + 2, 0);
      sfence();
      u32x4 gr;
      u32x2 tg;
      if (st == 6) { gr = *LDSP(u32x4, L_GRAN + (j * 16 + ((lane & 31) >> 1)) * 16); tg = *LDSP(u32x2, L_SYNC + 16); }
#pragma unroll
      for (int s = 0; s < 4; ++s) xf[st * 4 + s] = lds_b128((xa ^ (s << 5)) + (slot & 65536), slot & 32768);
      sfence();
      issue(st + 2, 1);
      sfence();
      if (st == 6) {
        tag = ((u64)(u32)__builtin_amdgcn_readfirstlane(tg.y) << 32) | (u32)__builtin_amdgcn_readfirstlane(tg.x);
#pragma unroll 1
        for (u32 i = 0; i < SPIN_CAP; ++i) {
          if (__all(gr.y == (u32)tag && gr.w == (u32)(tag >> 32))) break;
          asm volatile("buffer_load_dwordx4 %0, %1, %2, %3 offen sc1\n\ts_waitcnt vmcnt(0)"
                       : "=&v"(gr) : "v"((j * 16 + ((lane & 31) >> 1)) * 16), "s"(srd_ws), "s"(WS_STATS + rt * 1024) : "memory");
        }
        ms = (lane & 1) ? gr.z : gr.x;
        *LDSP(u32, L_MS + (j * 32 + (lane & 31)) * 4) = ms;
        *LDSP(float, L_RL + (j * 32 + (lane & 31)) * 4) = LOG2E * __builtin_amdgcn_rcpf(hi_f(ms));
      }
      sfence();
    }
    wait_vm<8>();
    __builtin_amdgcn_s_setprio(1);
    barrier();
    STAMP(2);
    const u32 wb0 = hh * 1024 + trl + trs, wb1 = wb0 ^ 64;
    const u32 wc0 = wb0 + j * 8192, wc1 = wb1 + j * 8192;
    bf16x8 wf[4][2], cw[3];
    f32x16 acc[2] = {};
    f32x4 c0 = {}, c1 = {};
    u32 cnt = 0;
    auto ldw = [&](int s, int u) { wf[s & 3][u] = lds_tr<512>(u ? wb1 : wb0, s * 2048); sfence(); };
#pragma unroll
    for (int s = 0; s < 3; ++s) { ldw(s, 0); ldw(s, 1); }
#pragma unroll
    for (int s = 0; s < 32; ++s) {
      const int cr = s < 8 ? s : (s >= 14 && s < 22 ? s - 6 : -1), cm = (s >= 2 && s < 10) ? s - 2 : (s >= 16 && s < 24 ? s - 8 : -1);
      acc[0] = mfma32(wf[s & 3][0], xf[s], acc[0]); sfence();
      if (s == 12 && cnt < 4) lds_wait(L_SYNC, 4);
      if (s + 3 < 32) ldw(s + 3, 0);
      if (cr >= 0) { cw[cr % 3] = lds_tr<512>((cr & 1) ? wc1 : wc0, (cr >> 3) * 32768 + ((cr >> 1) & 3) * 2048); sfence(); }
      acc[1] = mfma32(wf[s & 3][1], xf[s], acc[1]); sfence();
      if (s + 3 < 32) ldw(s + 3, 1);
      if (cm >= 0) { if (cm & 1) c1 = mfma16(sel, cw[cm % 3], c1); else c0 = mfma16(sel, cw[cm % 3], c0); sfence(); }
      if (s == 8) { wait_vm<0>(); lds_signal(L_SYNC); }
      if (s == 10) cnt = __hip_atomic_load(LDSP(u32, L_SYNC), __ATOMIC_RELAXED, __HIP_MEMORY_SCOPE_WORKGROUP);
      if (s == 11) cnt = __builtin_amdgcn_readfirstlane(cnt);
      if (s == 25) *LDSP(u32, L_CORRB + lane * 16 + j * 4) = split_hi_lo(hh ? c1[0] : c0[0]);
      if (s == 28) lds_signal(L_SYNC);
      sfence();
    }
    lds_wait(L_SYNC, 8);
    {
      u32 n = (ms & 0xffffu) ^ 0x8000u;
      n = hh ? 0u : (n | (n << 16));
      const bf16x8 mn = as_bf((u32x4){n, n, n, n});
      acc[0] = mfma32(lds_b128(L_CORRB + (lane & 31) * 16), mn, acc[0]);
      acc[1] = mfma32(lds_b128(L_CORRB + (lane & 31) * 16, 512), mn, acc[1]);
    }
    STAMP(3);
    if (!xt) {
      __builtin_amdgcn_s_setprio(3);
      const u32 arow = WS_A + ((rt * 4 + j) * NSTEP + lstep) * 1024;
#pragma unroll
      for (int u = 0; u < 2; ++u)
#pragma unroll
        for (int s = 0; s < 2; ++s) {
          u32x4 o;
#pragma unroll
          for (int i = 0; i < 4; ++i) o[i] = relu2(pack2(acc[u][8 * s + 2 * i], acc[u][8 * s + 2 * i + 1]));
          store_dev(o, srd_ws, lane * 16, (u32)u < nl ? arow + (u * 2 + s) * 1024 : OOB);
        }
      publish_fetch(std::false_type{}, WS_FLAGS + ((rt * 2 + (j >> 1)) * 48 + 16 + g * 2 + (j & 1)) * 128);
    } else {
      __builtin_amdgcn_s_setprio(0);
      u32x4 pq[4];
      const float rl = LOG2E * __builtin_amdgcn_rcpf(hi_f(ms));
#pragma unroll
      for (int h = 0; h < 4; ++h) {
        float t2[8];
#pragma unroll
        for (int i = 0; i < 8; ++i) t2[i] = mul1(acc[h >> 1][8 * (h & 1) + i], rl);
        sfence();
        pq[h] = phi8(t2);
        w2_chunk(std::false_type{}, 0, h);
      }
      STAMP(4);
      w2_second(std::false_type{}, std::true_type{});
      STAMP(5);
      __builtin_amdgcn_s_setprio(3);
      lds_wait(L_SYNC + 4, 4);
      STAMP(6);
      const float sc = LN2 * hi_f(ms);
      const u32 ka = L_KVF + ((lane & 16) ? 512 + hh * 16 : hh * 256 + (lane & 15) * 16);
      bf16x8 kf[4];
      f32x16 d[4];
#pragma unroll
      for (int h = 0; h < 4; ++h) kf[h] = lds_b128(ka, h * 1024);
      sfence();
#pragma unroll
      for (int h = 0; h < 4; ++h) d[h] = mfma32(kf[h], as_bf(pq[h]), (f32x16){});
      sfence();
#pragma unroll
      for (int h = 0; h < 4; ++h) {
        const float f = sc * __builtin_amdgcn_rcpf(d[h][8] + 1e-6f);
        u32x4 o;
#pragma unroll
        for (int i = 0; i < 4; ++i) o[i] = pack2(mul1(d[h][2 * i], f), mul1(d[h][2 * i + 1], f));
        store_dev(o, srd_ws, lane * 16, WS_A + ((rt * 4 + kq) * NSTEP + 86 + g * 4 + h) * 1024);
      }
      wait_vm<0>();
      store_one((u32x4){(u32)tag, (u32)(tag >> 32), 0u, 0u}, srd_ws, WS_FLAGS + ((rt * 2 + (kq >> 1)) * 48 + 32 + g * 2 + (kq & 1)) * 128);
      __builtin_amdgcn_s_setprio(0);
    }
    second(std::false_type{});
  } else {
    auto first = [&](auto XT_) {
    constexpr bool XT = XT_;
    const u32 mh = w >> 1, nh = w & 1;
    f32x16 acc[2][2] = {};
    bf16x8 xf[4][2] = {}, wf[4][2] = {};
    {
      const u32 row = rt * 128 + c4 * 8 + 2 * w;
      u32x4 a0, a1;
      asm volatile("global_load_dwordx4 %0, %2, %3 ; path %4\n\tglobal_load_dwordx4 %1, %2, %3 offset:512"
                   : "=&v"(a0), "=&v"(a1) : "v"((row + hh) * 1024 + (lane & 31) * 16), "s"(x), "i"((int)XT) : "memory");
      sfence();
      *LDSP(u32, L_SYNC + (lane & 3) * 4) = 0u;
      asm volatile("" : "+v"(acc[0][0]), "+v"(acc[0][1]), "+v"(acc[1][0]), "+v"(acc[1][1]));
      asm volatile("" : "+v"(xf[2][0]), "+v"(xf[2][1]), "+v"(wf[2][0]), "+v"(wf[2][1]), "+v"(xf[3][0]), "+v"(xf[3][1]), "+v"(wf[3][0]), "+v"(wf[3][1]));
      read_epoch();
      *LDSP(u32x2, L_SYNC + 16) = (u32x2){(u32)tag, (u32)(tag >> 32)};
      asm volatile("s_waitcnt vmcnt(0)" : "+v"(a0), "+v"(a1));
      sfence();
      float v[16];
#pragma unroll
      for (int i = 0; i < 4; ++i) { v[2 * i] = lo_f(a0[i]); v[2 * i + 1] = hi_f(a0[i]); v[8 + 2 * i] = lo_f(a1[i]); v[9 + 2 * i] = hi_f(a1[i]); }
      float s[4] = {};
#pragma unroll
      for (int i = 0; i < 16; ++i) s[i & 3] += v[i];
      const float mean = sum32((s[0] + s[1]) + (s[2] + s[3])) * (1.f / 512.f);
      float qq[4] = {};
#pragma unroll
      for (int i = 0; i < 16; ++i) { float d = v[i] - mean; qq[i & 3] = __builtin_fmaf(d, d, qq[i & 3]); }
      const float var = sum32((qq[0] + qq[1]) + (qq[2] + qq[3])) * (1.f / 512.f);
      const u32 p = pack2(mean, __builtin_amdgcn_sqrtf(rbf(rbf(var) + 1e-5f)));
      const u32 p1 = __builtin_amdgcn_readlane(p, 32);
      u32x4 gr = {p, (u32)tag, p1, (u32)(tag >> 32)};
      asm volatile("" : "+v"(gr));
      STAMP(1);
      store_one(gr, srd_ws, WS_STATS + row * 8);
      __builtin_amdgcn_s_setprio(1);
      barrier();
    }
    f32x4 rl[8];
    if constexpr (!XT) {
      phase_a_compute<false>(acc, xf, wf, rl, lane, mh, nh);
      STAMP(2);
#pragma unroll
      for (int k = 0; k < 4; ++k) {
        const int t = k >> 1, u = k & 1;
#pragma unroll
        for (int s = 0; s < 2; ++s) {
          u32x4 o;
#pragma unroll
          for (int i = 0; i < 4; ++i) o[i] = relu2(pack2(acc[t][u][8 * s + 2 * i], acc[t][u][8 * s + 2 * i + 1]));
          store_dev(o, srd_ws, lane * 16, WS_A + ((rt * 4 + mh * 2 + t) * NSTEP + g * 8 + u * 4 + nh * 2 + s) * 1024);
        }
      }
      STAMP(3);
      publish_fetch(std::true_type{}, WS_FLAGS + ((rt * 2 + mh) * 48 + g * 2 + nh) * 128);
    } else {
      phase_a_compute<true>(acc, xf, wf, rl, lane, mh, nh);
      STAMP(2);
      f32x16 kv = {}, zz = {};
      const bf16x8 ones = as_bf((u32x4){0x3f803f80u, 0x3f803f80u, 0x3f803f80u, 0x3f803f80u});
#pragma unroll
      for (int k = 0; k < 4; ++k) {
        const int t = k >> 1, s = k & 1;
        w2_chunk(std::true_type{}, 0, k);
        float t2[8], v2[8];
#pragma unroll
        for (int i = 0; i < 8; ++i) t2[i] = mul1(acc[t][0][8 * s + i], rl[2 * k + (i >> 2)][i & 3]);
#pragma unroll
        for (int i = 0; i < 8; ++i) v2[i] = mul1(acc[t][1][8 * s + i], rl[2 * k + (i >> 2)][i & 3]);
        sfence();
        const u32x4 pv = {pack2(v2[0], v2[1]), pack2(v2[2], v2[3]), pack2(v2[4], v2[5]), pack2(v2[6], v2[7])};
        const u32x4 pk = phi8(t2);
        kv = mfma32(as_bf(pk), as_bf(pv), kv);
        zz = mfma32(as_bf(pk), ones, zz);
        sfence();
      }
      const bool iskv = ((lane >> 4) & 1) == mh;
      f32x4 keep[2], send[2];
      if (mh) {
#pragma unroll
        for (int i = 0; i < 8; ++i) { send[i >> 2][i & 3] = iskv ? zz[i] : kv[i]; keep[i >> 2][i & 3] = iskv ? kv[8 + i] : zz[8 + i]; }
      } else {
#pragma unroll
        for (int i = 0; i < 8; ++i) { send[i >> 2][i & 3] = iskv ? zz[8 + i] : kv[8 + i]; keep[i >> 2][i & 3] = iskv ? kv[i] : zz[i]; }
      }
      *LDSP(f32x4, L_XCH + w * 2048 + lane * 16) = send[0];
      *LDSO(f32x4, L_XCH + w * 2048 + lane * 16, 1024) = send[1];
      lds_signal(L_SYNC + 8 + nh * 4);
      const u32 head = g * 4 + nh * 2 + mh;
      const u32 o0 = iskv ? rt * 1024 + hh * 256 + (lane & 15) * 16 : 16384 + rt * 64 + hh * 16, o1 = o0 + (iskv ? 512 : 32);
      lds_wait(L_SYNC + 8 + nh * 4, 2);
      keep[0] += *LDSP(f32x4, L_XCH + (w ^ 2) * 2048 + lane * 16);
      keep[1] += *LDSO(f32x4, L_XCH + (w ^ 2) * 2048 + lane * 16, 1024);
      store_dev((u32x4){pack2(keep[0][0], keep[0][1]), (u32)tag, pack2(keep[0][2], keep[0][3]), (u32)(tag >> 32)}, srd_ws, o0, WS_KVP + head * KVH_B);
      store_dev((u32x4){pack2(keep[1][0], keep[1][1]), (u32)tag, pack2(keep[1][2], keep[1][3]), (u32)(tag >> 32)}, srd_ws, o1, WS_KVP + head * KVH_B);
      __builtin_amdgcn_s_setprio(0);
      STAMP(3);
      wait_vm<2>();
      w2_second(std::true_type{}, std::false_type{});
      STAMP(5);
      __builtin_amdgcn_s_setprio(3);
      const u32 hl = (w & 1) * 2 + (w >> 1), kvb = WS_KVP + (g * 4 + hl) * KVH_B;
      u32x4 pa[17];
#pragma unroll 1
      for (u32 it = 0; it < SPIN_CAP; ++it) {
#pragma unroll
        for (int i = 0; i < 17; ++i) pa[i] = __builtin_amdgcn_raw_buffer_load_b128(rws, lane * 16, kvb + i * 1024, 16);
        sfence();
        u32 bad = 0;
#pragma unroll
        for (int i = 0; i < 17; ++i) bad |= (pa[i].y ^ (u32)tag) | (pa[i].w ^ (u32)(tag >> 32));
        if (__all(bad == 0)) break;
#pragma unroll 1
        for (; it < SPIN_CAP; ++it) {
          u32x4 z;
          asm volatile("buffer_load_dwordx4 %0, %1, %2, %3 offen sc1\n\ts_waitcnt vmcnt(0)" : "=&v"(z) : "v"(lane * 16), "s"(srd_ws), "s"(kvb + 16384) : "memory");
          if (__all(z.y == (u32)tag && z.w == (u32)(tag >> 32))) break;
        }
      }
      STAMP(6);
      float z[4] = {lo_f(pa[16].x), hi_f(pa[16].x), lo_f(pa[16].z), hi_f(pa[16].z)}, sm[4] = {};
#pragma unroll
      for (int i = 0; i < 4; ++i) z[i] = dpp_add<0x128>(z[i]);
#pragma unroll
      for (int i = 0; i < 4; ++i) z[i] = dpp_add<0x124>(z[i]);
#pragma unroll
      for (int i = 0; i < 4; ++i) z[i] = swap16_add(z[i]);
#pragma unroll
      for (int i = 0; i < 4; ++i) z[i] = swap32_add(z[i]);
#pragma unroll
      for (int i = 0; i < 16; ++i) { sm[0] += lo_f(pa[i].x); sm[1] += hi_f(pa[i].x); sm[2] += lo_f(pa[i].z); sm[3] += hi_f(pa[i].z); }
      *LDSP(u32x2, L_KVF + hl * 1024 + 512 + (lane & 1) * 16 + (lane & 2) * 4) = (u32x2){pack2(z[0], z[1]), pack2(z[2], z[3])};
      *LDSP(u32x2, L_KVF + hl * 1024 + (lane & 31) * 16 + hh * 8) = (u32x2){pack2(sm[0], sm[1]), pack2(sm[2], sm[3])};
      lds_signal(L_SYNC + 4);
      __builtin_amdgcn_s_setprio(0);
    }
    };
    if (xt) first(std::true_type{}); else first(std::false_type{});
    second(std::true_type{});
  }
  {
    STAMP(11);
    barrier();
    STAMP(12);
    const u32 fa = (w * 8 + (lane >> 3)) * 256 + ((((lane & 7) * 2) ^ (lane >> 3)) << 4);
    f32x4 p0[8], p1[8];
#pragma unroll
    for (int p = 0; p < 8; ++p) { p0[p] = *LDSO(f32x4, fa + (p >> 2) * 65536, (p & 3) * 16384); p1[p] = *LDSO(f32x4, (fa ^ 16) + (p >> 2) * 65536, (p & 3) * 16384); }
    sfence();
    const f32x4 s0 = ((p0[0] + p0[1]) + (p0[2] + p0[3])) + ((p0[4] + p0[5]) + (p0[6] + p0[7]));
    const f32x4 s1 = ((p1[0] + p1[1]) + (p1[2] + p1[3])) + ((p1[4] + p1[5]) + (p1[6] + p1[7]));
    const float rs = __builtin_amdgcn_rcpf(hi_f(st));
    u32x4 o;
    o[0] = pack2(__builtin_fmaf(s0[0], rs, lo_f(xr[0])), __builtin_fmaf(s0[1], rs, hi_f(xr[0])));
    o[1] = pack2(__builtin_fmaf(s0[2], rs, lo_f(xr[1])), __builtin_fmaf(s0[3], rs, hi_f(xr[1])));
    o[2] = pack2(__builtin_fmaf(s1[0], rs, lo_f(xr[2])), __builtin_fmaf(s1[1], rs, hi_f(xr[2])));
    o[3] = pack2(__builtin_fmaf(s1[2], rs, lo_f(xr[3])), __builtin_fmaf(s1[3], rs, hi_f(xr[3])));
    *(GAS u32x4*)(out + tok * 1024 + ct * 128 + (lane & 7) * 16) = o;
    if (b == 0 && w == 0) store_one((u32x4){(u32)tag, (u32)(tag >> 32), 0u, 0u}, srd_ws, WS_EPOCH);
    STAMP(13);
  }
}

extern "C" void kernel_launch(void* const* d_in, const int* in_sizes, int n_in, void* d_out, int out_size, void* d_ws, size_t ws_size,
                              hipStream_t stream) {
  fused_block<<<256, 512, 0, stream>>>((const char*)d_in[0], (const char*)d_in[1], (const char*)d_in[4], (char*)d_ws, (const char*)d_in[5],
                                       (const char*)d_in[2], (char*)d_out);
}
```

```cpp
#define PROBE 99
#define PGRP 0
#define PTYPE 0
#include <hip/hip_runtime.h>
#include <hip/hip_bf16.h>
#include <type_traits>

typedef unsigned u32;
typedef unsigned long long u64;
typedef __bf16 bf16x8 __attribute__((ext_vector_type(8)));
typedef __bf16 bf16x2 __attribute__((ext_vector_type(2)));
typedef short s16x4 __attribute__((ext_vector_type(4)));
typedef short s16x2 __attribute__((ext_vector_type(2)));
typedef float f32x16 __attribute__((ext_vector_type(16)));
typedef float f32x4 __attribute__((ext_vector_type(4)));
typedef float f32x2 __attribute__((ext_vector_type(2)));
typedef u32 u32x4 __attribute__((ext_vector_type(4)));
typedef u32 u32x2 __attribute__((ext_vector_type(2)));
#define LAS __attribute__((address_space(3)))
#define GAS __attribute__((address_space(1)))
#define DEV __device__ __forceinline__
#define RLX_AGENT __ATOMIC_RELAXED, __HIP_MEMORY_SCOPE_AGENT
#define LDSP(T, a) ((LAS T*)(size_t)(a))

#define ROT1 1
#define NT1 0
#define NT2 0

constexpr u32 WS_EPOCH = 0;
constexpr u32 WS_STATS = 4096;
constexpr u32 WS_FLAGS = WS_STATS + 16384;
constexpr u32 WS_KVP = WS_FLAGS + 32 * 48 * 128;
constexpr u32 KVH_B = 17408;
constexpr u32 WS_A = 1u << 20;
constexpr u32 NSTEP = 118;
constexpr u32 WS_END = WS_A + 64 * NSTEP * 1024;
constexpr u32 WS_STAMP = 16u << 20;
static_assert(WS_KVP + 32 * KVH_B <= WS_A, "ws map");
constexpr u32 OOB = 0x40000000u;

constexpr u32 L_SCR = 131072;
constexpr u32 L_CORRA = L_SCR;
constexpr u32 L_CORRB = L_SCR + 512;
constexpr u32 L_MS = L_SCR + 1536;
constexpr u32 L_RL = L_SCR + 2048;
constexpr u32 L_SYNC = L_SCR + 2560;
constexpr u32 L_GRAN = L_SCR + 3072;
constexpr u32 L_KVF = L_SCR + 4096;
constexpr u32 L_XCH = L_SCR + 8192;
constexpr u32 L_TOTAL = 163840;

constexpr u32 CODE_LINES = 184;
constexpr u32 SPIN_CAP = 1u << 18;
constexpr float LOG2E = 1.4426950408889634f, LN2 = 0.6931471805599453f;

DEV void sfence() { __builtin_amdgcn_sched_barrier(0); }
template <int N> DEV void wait_vm() { asm volatile("s_waitcnt vmcnt(%0)" ::"i"(N) : "memory"); }
DEV void wait_lgkm() { sfence(); __builtin_amdgcn_s_waitcnt(0xC07F); asm volatile("" ::: "memory"); sfence(); }
DEV void barrier() { sfence(); __builtin_amdgcn_s_waitcnt(0xC07F); asm volatile("s_barrier" ::: "memory"); sfence(); }

DEV u32x4 make_srd(const void* p, u32 bytes) {
  u64 a = (u64)p;
  return (u32x4){(u32)a, (u32)(a >> 32) & 0xffffu, bytes, 0x00020000u};
}
template <int POL> DEV void dma(u32x4 srd, u32 voff, u32 soff, u32 ldsaddr) {
  if (POL == 1)
    asm volatile("s_mov_b32 m0, %3\n\ts_nop 0\n\tbuffer_load_dwordx4 %0, %1, %2 offen nt lds" ::"v"(voff), "s"(srd), "s"(soff), "s"(ldsaddr) : "memory");
  else if (POL == 2)
    asm volatile("s_mov_b32 m0, %3\n\ts_nop 0\n\tbuffer_load_dwordx4 %0, %1, %2 offen sc1 lds" ::"v"(voff), "s"(srd), "s"(soff), "s"(ldsaddr) : "memory");
  else
    asm volatile("s_mov_b32 m0, %3\n\ts_nop 0\n\tbuffer_load_dwordx4 %0, %1, %2 offen lds" ::"v"(voff), "s"(srd), "s"(soff), "s"(ldsaddr) : "memory");
}
template <int POL> DEV void dma4(u32x4 srd, u32x4 v, u32 soff, u32 ldsaddr) {
  if (POL == 1)
    asm volatile("s_mov_b32 m0, %6\n\ts_nop 0\n\tbuffer_load_dwordx4 %0, %4, %5 offen nt lds\n\tbuffer_load_dwordx4 %1, %4, %5 offen offset:1024 nt lds\n\t"
                 "buffer_load_dwordx4 %2, %4, %5 offen offset:2048 nt lds\n\tbuffer_load_dwordx4 %3, %4, %5 offen offset:3072 nt lds"
                 ::"v"(v.x), "v"(v.y), "v"(v.z), "v"(v.w), "s"(srd), "s"(soff), "s"(ldsaddr) : "memory");
  else
    asm volatile("s_mov_b32 m0, %6\n\ts_nop 0\n\tbuffer_load_dwordx4 %0, %4, %5 offen lds\n\tbuffer_load_dwordx4 %1, %4, %5 offen offset:1024 lds\n\t"
                 "buffer_load_dwordx4 %2, %4, %5 offen offset:2048 lds\n\tbuffer_load_dwordx4 %3, %4, %5 offen offset:3072 lds"
                 ::"v"(v.x), "v"(v.y), "v"(v.z), "v"(v.w), "s"(srd), "s"(soff), "s"(ldsaddr) : "memory");
}
DEV u32x4 steps4(u32 v, u32 step) { step -= 1024; return (u32x4){v, v + step, v + 2 * step, v + 3 * step}; }
DEV void store_one(u32x4 v, u32x4 srd, u32 soff) {
  asm volatile("s_mov_b64 exec, 1\n\tbuffer_store_dwordx4 %0, off, %1, %2 sc1\n\ts_mov_b64 exec, -1\n\ts_nop 0" ::"v"(v), "s"(srd), "s"(soff) : "memory");
}
DEV void store_dev(u32x4 v, u32x4 srd, u32 voff, u32 soff) {
  asm volatile("buffer_store_dwordx4 %0, %1, %2, %3 offen sc1\n\ts_nop 1" ::"v"(v), "v"(voff), "s"(srd), "s"(soff) : "memory");
}
#define LDSO(T, a, o) ((LAS T*)(LDSP(char, a) + (o)))
DEV bf16x8 lds_b128(u32 a, int o = 0) { return *LDSO(bf16x8, a, o); }
template <int D> DEV bf16x8 lds_tr(u32 a, int o) {
  s16x4 lo = __builtin_amdgcn_ds_read_tr16_b64_v4i16(LDSO(s16x4, a, o));
  s16x4 hi = __builtin_amdgcn_ds_read_tr16_b64_v4i16(LDSO(s16x4, a, o + D));
  return __builtin_bit_cast(bf16x8, __builtin_shufflevector(lo, hi, 0, 1, 2, 3, 4, 5, 6, 7));
}
DEV f32x16 mfma32(bf16x8 a, bf16x8 b, f32x16 c) { return __builtin_amdgcn_mfma_f32_32x32x16_bf16(a, b, c, 0, 0, 0); }
DEV f32x4 mfma16(bf16x8 a, bf16x8 b, f32x4 c) { return __builtin_amdgcn_mfma_f32_16x16x32_bf16(a, b, c, 0, 0, 0); }
DEV bf16x8 as_bf(u32x4 v) { return __builtin_bit_cast(bf16x8, v); }
DEV u32 pack2(float a, float b) { return __builtin_bit_cast(u32, __builtin_convertvector((f32x2){a, b}, bf16x2)); }
DEV u32 relu2(u32 v) { return __builtin_bit_cast(u32, __builtin_elementwise_max(__builtin_bit_cast(s16x2, v), (s16x2){0, 0})); }
DEV float lo_f(u32 v) { return __uint_as_float(v << 16); }
DEV float hi_f(u32 v) { return __uint_as_float(v & 0xffff0000u); }
DEV float rbf(float f) { return lo_f(pack2(f, 0.f)); }
DEV u32 split_hi_lo(float c) { float h = rbf(c); return pack2(h, c - h); }
DEV float mul1(float a, float b) { float r = a * b; asm("" : "+v"(r)); return r; }
template <int CTRL> DEV float dpp_add(float v) {
  return v + __uint_as_float(__builtin_amdgcn_update_dpp(0u, __float_as_uint(v), CTRL, 0xf, 0xf, true));
}
DEV float swap16_add(float v) {
  auto r = __builtin_amdgcn_permlane16_swap(__float_as_uint(v), __float_as_uint(v), false, false);
  return __uint_as_float(r[0]) + __uint_as_float(r[1]);
}
DEV float swap32_add(float v) {
  auto r = __builtin_amdgcn_permlane32_swap(__float_as_uint(v), __float_as_uint(v), false, false);
  return __uint_as_float(r[0]) + __uint_as_float(r[1]);
}
DEV float sum32(float v) {
  v = dpp_add<0xB1>(v); v = dpp_add<0x4E>(v); v = dpp_add<0x141>(v); v = dpp_add<0x140>(v);
  return swap16_add(v);
}
DEV u32x4 phi8(const float (&t2)[8]) {
  float e[8], m[8];
#pragma unroll
  for (int i = 0; i < 8; ++i) e[i] = __builtin_amdgcn_fmed3f(__builtin_amdgcn_exp2f(t2[i]), 0.f, 1.f);
  sfence();
#pragma unroll
  for (int i = 0; i < 8; ++i) asm("v_max_f32 %0, %1, %2" : "=v"(m[i]) : "s"(1e-6f * LOG2E), "v"(t2[i]));
  sfence();
#pragma unroll
  for (int i = 0; i < 8; ++i) { e[i] = __builtin_fmaf(m[i], LN2, e[i]); asm("" : "+v"(e[i])); }
  sfence();
  return (u32x4){pack2(e[0], e[1]), pack2(e[2], e[3]), pack2(e[4], e[5]), pack2(e[6], e[7])};
}
DEV void lds_signal(u32 addr) {
  asm volatile("s_mov_b64 exec, 1\n\tds_add_u32 %0, %1\n\ts_mov_b64 exec, -1" ::"v"(addr), "v"(1u) : "memory");
}
DEV void lds_wait(u32 addr, u32 waves) {
#pragma unroll 1
  for (u32 i = 0; i < SPIN_CAP; ++i) {
    u32 v;
    asm volatile("ds_read_b32 %0, %1\n\ts_waitcnt lgkmcnt(0)" : "=v"(v) : "v"(addr) : "memory");
    if ((u32)__builtin_amdgcn_readfirstlane(v) >= waves) break;
    __builtin_amdgcn_s_sleep(1);
  }
}
DEV bool try4(const char* p, u64 tag) {
    u64 f0, f1, f2, f3;
    asm volatile("s_load_dwordx2 %0, %4, 0 glc\n\ts_load_dwordx2 %1, %4, 128 glc\n\ts_load_dwordx2 %2, %4, 256 glc\n\t"
                 "s_load_dwordx2 %3, %4, 384 glc\n\ts_waitcnt lgkmcnt(0)"
                 : "=&s"(f0), "=&s"(f1), "=&s"(f2), "=&s"(f3) : "s"(p) : "memory");
    return f0 == tag && f1 == tag && f2 == tag && f3 == tag;
}
DEV void poll4(const char* p, u64 tag) {
#pragma unroll 1
  for (u32 i = 0; i < SPIN_CAP; ++i)
    if (try4(p, tag)) break;
}

#define STAMP(i) do { if ((i) == 0 || (i) == PROBE) { sfence(); ptime[(i) ? 1 : 0] = (u32)__builtin_amdgcn_s_memrealtime(); __builtin_amdgcn_s_waitcnt(0xC07F); sfence(); } } while (0)
#undef FSTAMP
#define FSTAMP(w, i)

template <bool XT>
DEV void phase_a_compute(f32x16 (&acc)[2][2], bf16x8 (&xf)[4][2], bf16x8 (&wf)[4][2], f32x4 (&rl)[8], u32 lane, u32 mh, u32 nh) {
  const u32 hh = lane >> 5, q = (lane & 15) >> 2;
  u32 xa = (mh * 64 + (lane & 31)) * 128 + ((hh ^ ((lane >> 1) & 7)) << 4);
  u32 wa0 = 16384 + (8 * hh + q) * 256 + (lane & 16) * 2 + (lane & 3) * 8 + ((XT ? (nh ^ q) : ((nh - q) & 3)) << 6);
  u32 wa1 = wa0 ^ 128;
  const u32 selv = (((lane >> 4) ^ (lane >> 2)) & 1) ? 0u : 0x3f803f80u;
  const bf16x8 sel = as_bf((u32x4){selv, selv, selv, selv});
  const u32 ca = L_CORRA + (nh * 32 + (lane & 31)) * 4, ma = L_MS + (mh * 64 + (lane & 31)) * 4;
  f32x4 cs = {};
  auto ldx = [&](int s, int h) { xf[s][h] = lds_b128(xa ^ (s << 5), h * 4096); sfence(); };
  auto ldw = [&](int s, int h) { wf[s][h] = lds_tr<1024>(h ? wa1 : wa0, s * 4096); sfence(); };
  auto m1 = [&](int s, int i) {
    const int t = (i + 1) >> 1 & 1, u = i >> 1;
    acc[t][u] = XT ? mfma32(xf[s][t], wf[s][u], acc[t][u]) : mfma32(wf[s][u], xf[s][t], acc[t][u]);
    sfence();
  };
#pragma unroll 1
  for (u32 st = 0; st < 8; ++st) {
    m1(2, 0); ldw(0, 0); m1(2, 1); ldx(0, 0); ldx(0, 1); m1(2, 2); ldw(0, 1); m1(2, 3); ldw(1, 0);
    m1(3, 0); ldx(1, 0); ldx(1, 1); m1(3, 1); ldw(1, 1); m1(3, 2); ldw(2, 0); m1(3, 3); ldx(2, 0); ldx(2, 1);
    m1(0, 0); ldw(2, 1); m1(0, 1); ldw(3, 0); m1(0, 2); ldx(3, 0); ldx(3, 1); m1(0, 3); ldw(3, 1);
    m1(1, 0);
    xa = (xa + 32768) & 131071; wa0 = (wa0 + 32768) & 131071; wa1 = (wa1 + 32768) & 131071;
    asm volatile("" : "+v"(xa), "+v"(wa0), "+v"(wa1));
    sfence();
    m1(1, 1); m1(1, 2); m1(1, 3);
    if (mh) {
#pragma unroll
      for (int s = 0; s < 4; ++s) cs = mfma16(sel, wf[s][1], cs);
    } else {
#pragma unroll
      for (int s = 0; s < 4; ++s) cs = mfma16(sel, wf[s][0], cs);
    }
    if (st == 7) *LDSP(u32, ca + mh * 256) = split_hi_lo(cs[0]);
    barrier();
  }
  __builtin_amdgcn_s_setprio(2);
  m1(2, 0);
  u32 c0 = *LDSP(u32, ca), c1 = *LDSO(u32, ca, 256);
  u32 n0 = *LDSP(u32, ma), n1 = *LDSO(u32, ma, 128);
  sfence();
  m1(2, 3);
  if (XT) {
    u32 ra = L_RL + (mh * 64 + hh * 4) * 4;
    asm volatile("" : "+v"(ra));
#pragma unroll
    for (int i = 0; i < 8; ++i) rl[i] = *LDSO(f32x4, ra, i * 32);
  }
  sfence();
  m1(3, 0); m1(3, 3);
  n0 = (n0 & 0xffffu) ^ 0x8000u; n1 = (n1 & 0xffffu) ^ 0x8000u;
  xf[0][0] = as_bf((u32x4){hh ? 0u : (n0 | (n0 << 16)), 0u, 0u, 0u});
  wf[0][0] = as_bf((u32x4){c0, c0, c0, c0});
  asm volatile("" : "+v"(xf[0][0]), "+v"(wf[0][0]));
  sfence();
  m1(2, 1);
  xf[0][1] = as_bf((u32x4){hh ? 0u : (n1 | (n1 << 16)), 0u, 0u, 0u});
  wf[0][1] = as_bf((u32x4){c1, c1, c1, c1});
  asm volatile("" : "+v"(xf[0][1]), "+v"(wf[0][1]));
  sfence();
  m1(0, 0); m1(0, 3); m1(2, 2); m1(3, 1); m1(3, 2); m1(0, 1); m1(0, 2);
}

__global__ __launch_bounds__(512, 2) void fused_block(const char* __restrict__ x, const char* __restrict__ wqkv, const char* __restrict__ w1,
                                                      char* ws, const char* __restrict__ w2, const char* __restrict__ wo, char* __restrict__ out) {
  __shared__ __attribute__((aligned(16))) char lds_all[L_TOTAL];
  u32 ptime[2] = {};
  u64 pc;
  asm volatile("s_getpc_b64 %0" : "=s"(pc) : "s"((u32)(size_t)lds_all));
  STAMP(0);
  asm volatile("" ::"s"(x), "s"(wqkv), "s"(w1), "s"(ws), "s"(w2), "s"(wo), "s"(out));
  const u32 b = blockIdx.x, xcd = b & 7, r = b >> 3;
  const u32 lane = threadIdx.x & 63, w = __builtin_amdgcn_readfirstlane(threadIdx.x >> 6);
  const u32 rt = (xcd & 1) * 8 + (r & 7), c4 = (xcd >> 1) * 4 + (r >> 3), g = c4 >> 1;
  const bool xt = (c4 & 1) == 0;
  const u32 hh = lane >> 5, q = (lane & 15) >> 2, kq = w & 3;
  const __amdgpu_buffer_rsrc_t rws = __builtin_amdgcn_make_buffer_rsrc(ws, 0, WS_END, 0x00020000);
  const u32x4 srd_ws = make_srd(ws, WS_END);
  u64 tag;
  const u32 v128 = ((lane & 7) ^ ((lane >> 2) & 4)) << 4;
  const u32 trl = q * 128 + (lane & 16) * 2 + (lane & 3) * 8, trs = (q & 2) << 5;
  auto read_epoch = [&]() {
    u64 e; u32 d0, d1, d2, d3;
    const u64 cp = (pc & ~127ull) + min(r * 8 + w * 2, CODE_LINES - 2) * 128;
    asm volatile("s_load_dwordx2 %0, %5, 0\n\ts_load_dword %1, %6, 0\n\ts_load_dword %2, %6, 64\n\ts_load_dword %3, %6, 128\n\t"
                 "s_load_dword %4, %6, 192\n\ts_waitcnt lgkmcnt(0)"
                 : "=&s"(e), "=&s"(d0), "=&s"(d1), "=&s"(d2), "=&s"(d3) : "s"(ws), "s"(cp) : "memory");
    tag = e + 0x9E3779B97F4A7C15ull;
  };

  const u32 crt = xcd * 4 + (r & 3), ct = r >> 2;
  const u32 tok = crt * 64 + w * 8 + (lane >> 3);
  u32 st;
  u32x4 xr;
  const u32 reg2 = (w ^ 4) * 16384;
  const u32 ra0 = reg2 + hh * 512 + trl + trs, ra1 = ra0 ^ 64;
  bf16x8 w2f[8][2];
  bool ready = false;
  auto w2_chunk = [&](auto G0_, int half, int i) {
    constexpr bool G0 = G0_;
    const u32x4 vw2 = steps4((lane >> 3) * 1024 + v128, 8192);
    if (G0) dma4<NT2>(make_srd(w2, 1376 * 1024), vw2, (16 * kq + 8 * half + 2 * i) * 16384 + ct * 128, reg2 + i * 4096);
    else if (half == 0 && i < 3) dma4<NT2>(make_srd(w2, 1376 * 1024), vw2, (64 + 6 * kq + 2 * i) * 16384 + ct * 128, reg2 + i * 4096);
    else dma4<NT2>(make_srd(wo, 512 * 1024), vw2, (8 * kq + 2 * i) * 16384 + ct * 128, reg2 + i * 4096);
  };
  auto w2_second = [&](auto G0_, auto WAIT_) {
    constexpr bool G0 = G0_, WAIT = WAIT_;
#pragma unroll
    for (int i = 0; i < (G0 ? 4 : 3); ++i) {
      if (i == (G0 ? 3 : 2)) ready = try4(ws + WS_FLAGS + (crt * 48 + (G0 ? 0 : 16) + kq * 4) * 128, tag);
      if (WAIT) wait_vm<12>();
#pragma unroll
      for (int s = 2 * i; s < 2 * i + 2; ++s) { w2f[s][0] = lds_tr<1024>(ra0, s * 2048); w2f[s][1] = lds_tr<1024>(ra1, s * 2048); }
      wait_lgkm();
      w2_chunk(G0_, 1, i);
    }
  };
  auto publish_fetch = [&](auto G0_, u32 flag_off) {
    w2_chunk(G0_, 0, 0);
    wait_vm<4>();
    store_one((u32x4){(u32)tag, (u32)(tag >> 32), 0u, 0u}, srd_ws, flag_off);
    __builtin_amdgcn_s_setprio(0);
    STAMP(4);
    w2_chunk(G0_, 0, 1); w2_chunk(G0_, 0, 2); w2_chunk(G0_, 0, 3);
    w2_second(G0_, std::true_type{});
    STAMP(5);
  };
  auto second = [&](auto G0_) {
  constexpr bool G0 = G0_;
  const u32 sb0 = G0 ? 16 * kq : 64 + 6 * kq, n0 = G0 ? 8 : (kq < 3 ? 6 : 4);
  const u32 sb1 = G0 ? 16 * kq + 8 : 86 + 8 * kq;
  STAMP(7);
  {
    const char* fp = ws + WS_FLAGS + (crt * 48 + (G0 ? 0 : 16) + kq * 4) * 128;
    if (!ready) poll4(fp, tag);
    STAMP(8);
    const u32 a0 = WS_A + (crt * 2 * NSTEP + sb0) * 1024, a1 = WS_A + (crt * 2 * NSTEP + sb1) * 1024;
    u32x4 af[16][2];
    auto lda = [&](int s) {
      const u32 so = s < 8 ? ((u32)s < n0 ? a0 + s * 1024 : OOB) : a1 + (s - 8) * 1024;
      af[s][0] = __builtin_amdgcn_raw_buffer_load_b128(rws, lane * 16, so, 16);
      af[s][1] = __builtin_amdgcn_raw_buffer_load_b128(rws, lane * 16 + NSTEP * 1024, so, 16);
      sfence();
    };
    auto ldres = [&]() {
      st = __hip_atomic_load((GAS u32*)(ws + WS_STATS + tok * 8), RLX_AGENT);
      xr = *(const GAS u32x4*)(x + tok * 1024 + ct * 128 + (lane & 7) * 16);
      sfence();
    };
    f32x16 acc[2][2] = {};
    auto ldw = [&](int s) { w2f[s][0] = lds_tr<1024>(ra0, s * 2048); w2f[s][1] = lds_tr<1024>(ra1, s * 2048); sfence(); };
    auto mm = [&](int s) {
#pragma unroll
      for (int t = 0; t < 2; ++t) {
        acc[t][0] = mfma32(w2f[s & 7][0], as_bf(af[s][t]), acc[t][0]);
        acc[t][1] = mfma32(w2f[s & 7][1], as_bf(af[s][t]), acc[t][1]);
      }
      sfence();
    };
    if constexpr (G0) {
#pragma unroll
      for (int s = 0; s < 12; ++s) lda(s);
      STAMP(9);
#pragma unroll
      for (int s = 0; s < 4; ++s) { mm(s); lda(12 + s); ldw(s); }
      ldres();
#pragma unroll
      for (int s = 4; s < 8; ++s) { mm(s); ldw(s); }
    } else {
#pragma unroll
      for (int s = 0; s < 6; ++s) lda(s);
      ldres();
#pragma unroll
      for (int s = 0; s < 6; ++s) { mm(s); ldw(s); }
      ldw(6); ldw(7);
      poll4(fp + 16 * 128, tag);
      STAMP(9);
#pragma unroll
      for (int s = 8; s < 16; ++s) lda(s);
    }
#pragma unroll
    for (int s = 8; s < 16; ++s) mm(s);
    if constexpr (G0) __builtin_amdgcn_s_waitcnt(0x0F70);
    STAMP(10);
    const u32 da = reg2 + (lane & 31) * 256 + ((hh ^ (lane & 7)) << 4);
#pragma unroll
    for (int t = 0; t < 2; ++t)
#pragma unroll
      for (int c = 0; c < 2; ++c)
#pragma unroll
        for (int r4 = 0; r4 < 4; ++r4)
          *LDSO(f32x4, da ^ (r4 << 5), t * 8192 + c * 128) =
              (f32x4){acc[t][c][4 * r4], acc[t][c][4 * r4 + 1], acc[t][c][4 * r4 + 2], acc[t][c][4 * r4 + 3]};
  }
  };
  if (w >= 4) {
    const u32 j = kq;
    const u32 rot = ROT1 == 0 ? 0 : ROT1 == 1 ? (r & 7) : (r & 7) + 2 * (r >> 3);
    const u32x4 srd_x = make_srd(x, 2048 * 1024);
    const u32 vx = (lane >> 3) * 1024 + (((lane & 7) ^ (lane >> 4)) << 4);
    const u32x4 vx4 = {vx, (vx ^ 64) + 7168, vx + 14336, (vx ^ 64) + 21504};
    auto issue_x = [&](int st) { dma4<NT1>(srd_x, vx4, (rt * 128 + 32 * j) * 1024 + ((st + rot) & 7) * 128, (st & 3) * 32768 + j * 4096); };
    issue_x(0);
    sfence();
    const u32 wst = xt ? 3072 : 2752;
    const u32x4 srd_w = make_srd(xt ? wqkv : w1, 512 * wst);
    const u32 ym = xt ? 0u : ~0u, l4 = lane >> 4;
    const u32 blk = ((q ^ l4) + (((q & l4) << 1) & ym)) & 3;
    const u32 vwa = l4 * wst + (lane & 3) * 16 + (blk & 1) * 64 + (blk >> 1) * (xt ? 1024 : 128) + (xt ? 1024 + g * 128 : g * 256);
    const u32x4 vwa4 = steps4(vwa, 4 * wst);
    auto issue_w = [&](int st) { dma4<NT1>(srd_w, vwa4, (((st + rot) & 7) * 64 + 16 * j) * wst, (st & 3) * 32768 + 16384 + j * 4096); };
    issue_w(0);
    sfence();
    const u32 nl = (g < 6 && !(g & 1)) ? 2 : 1, lstep = 64 + (g >> 1) * 6 + ((g & 1) ? (g < 6 ? 4 : 2) : 0);
    const u32 vwb = (lane >> 3) * wst + (v128 & ((xt || nl == 2) ? 127 : 48)) + (xt ? g * 128 : lstep * 32);
    auto issue = [&](int st, int part) {
      if (st < 8) { if (part) issue_w(st); else issue_x(st); }
      else dma4<NT1>(srd_w, steps4(vwb, 8 * wst), ((((st - 8) * 4 + j + rot) & 7) * 64 + 32 * part) * wst, (st - 8) * 32768 + j * 8192 + part * 4096);
    };
    issue(1, 0); issue(1, 1);
    const u32 xa = (j * 32 + (lane & 31)) * 128 + ((hh ^ ((lane >> 1) & 7)) << 4);
    const u32 selv = (((lane >> 4) ^ (lane >> 2)) & 1) ? 0u : 0x3f803f80u;
    const bf16x8 sel = as_bf((u32x4){selv, selv, selv, selv});
    bf16x8 xf[32];
    u32 ms;
#pragma unroll
    for (int st = 0; st < 8; ++st) {
      wait_vm<8>();
      barrier();
      if (st == 0) STAMP(1);
      const u32 slot = (st & 3) * 32768;
      if (st == 4) dma<2>(srd_ws, lane * 16, j == 0 ? WS_STATS + rt * 1024 : OOB, j == 0 ? L_GRAN : L_XCH);
      issue(st + 2, 0);
      sfence();
      u32x4 gr;
      u32x2 tg;
      if (st == 6) { gr = *LDSP(u32x4, L_GRAN + (j * 16 + ((lane & 31) >> 1)) * 16); tg = *LDSP(u32x2, L_SYNC + 16); }
#pragma unroll
      for (int s = 0; s < 4; ++s) xf[st * 4 + s] = lds_b128((xa ^ (s << 5)) + (slot & 65536), slot & 32768);
      sfence();
      issue(st + 2, 1);
      sfence();
      if (st == 6) {
        tag = ((u64)(u32)__builtin_amdgcn_readfirstlane(tg.y) << 32) | (u32)__builtin_amdgcn_readfirstlane(tg.x);
#pragma unroll 1
        for (u32 i = 0; i < SPIN_CAP; ++i) {
          if (__all(gr.y == (u32)tag && gr.w == (u32)(tag >> 32))) break;
          asm volatile("buffer_load_dwordx4 %0, %1, %2, %3 offen sc1\n\ts_waitcnt vmcnt(0)"
                       : "=&v"(gr) : "v"((j * 16 + ((lane & 31) >> 1)) * 16), "s"(srd_ws), "s"(WS_STATS + rt * 1024) : "memory");
        }
        ms = (lane & 1) ? gr.z : gr.x;
        *LDSP(u32, L_MS + (j * 32 + (lane & 31)) * 4) = ms;
        *LDSP(float, L_RL + (j * 32 + (lane & 31)) * 4) = LOG2E * __builtin_amdgcn_rcpf(hi_f(ms));
      }
      sfence();
    }
    wait_vm<8>();
    __builtin_amdgcn_s_setprio(1);
    barrier();
    STAMP(2);
    const u32 wb0 = hh * 1024 + trl + trs, wb1 = wb0 ^ 64;
    const u32 wc0 = wb0 + j * 8192, wc1 = wb1 + j * 8192;
    bf16x8 wf[4][2], cw[3];
    f32x16 acc[2] = {};
    f32x4 c0 = {}, c1 = {};
    u32 cnt = 0;
    auto ldw = [&](int s, int u) { wf[s & 3][u] = lds_tr<512>(u ? wb1 : wb0, s * 2048); sfence(); };
#pragma unroll
    for (int s = 0; s < 3; ++s) { ldw(s, 0); ldw(s, 1); }
#pragma unroll
    for (int s = 0; s < 32; ++s) {
      const int cr = s < 8 ? s : (s >= 14 && s < 22 ? s - 6 : -1), cm = (s >= 2 && s < 10) ? s - 2 : (s >= 16 && s < 24 ? s - 8 : -1);
      acc[0] = mfma32(wf[s & 3][0], xf[s], acc[0]); sfence();
      if (s == 12 && cnt < 4) lds_wait(L_SYNC, 4);
      if (s + 3 < 32) ldw(s + 3, 0);
      if (cr >= 0) { cw[cr % 3] = lds_tr<512>((cr & 1) ? wc1 : wc0, (cr >> 3) * 32768 + ((cr >> 1) & 3) * 2048); sfence(); }
      acc[1] = mfma32(wf[s & 3][1], xf[s], acc[1]); sfence();
      if (s + 3 < 32) ldw(s + 3, 1);
      if (cm >= 0) { if (cm & 1) c1 = mfma16(sel, cw[cm % 3], c1); else c0 = mfma16(sel, cw[cm % 3], c0); sfence(); }
      if (s == 8) { wait_vm<0>(); lds_signal(L_SYNC); }
      if (s == 10) cnt = __hip_atomic_load(LDSP(u32, L_SYNC), __ATOMIC_RELAXED, __HIP_MEMORY_SCOPE_WORKGROUP);
      if (s == 11) cnt = __builtin_amdgcn_readfirstlane(cnt);
      if (s == 25) *LDSP(u32, L_CORRB + lane * 16 + j * 4) = split_hi_lo(hh ? c1[0] : c0[0]);
      if (s == 28) lds_signal(L_SYNC);
      sfence();
    }
    lds_wait(L_SYNC, 8);
    {
      u32 n = (ms & 0xffffu) ^ 0x8000u;
      n = hh ? 0u : (n | (n << 16));
      const bf16x8 mn = as_bf((u32x4){n, n, n, n});
      acc[0] = mfma32(lds_b128(L_CORRB + (lane & 31) * 16), mn, acc[0]);
      acc[1] = mfma32(lds_b128(L_CORRB + (lane & 31) * 16, 512), mn, acc[1]);
    }
    STAMP(3);
    if (!xt) {
      __builtin_amdgcn_s_setprio(3);
      const u32 arow = WS_A + ((rt * 4 + j) * NSTEP + lstep) * 1024;
#pragma unroll
      for (int u = 0; u < 2; ++u)
#pragma unroll
        for (int s = 0; s < 2; ++s) {
          u32x4 o;
#pragma unroll
          for (int i = 0; i < 4; ++i) o[i] = relu2(pack2(acc[u][8 * s + 2 * i], acc[u][8 * s + 2 * i + 1]));
          store_dev(o, srd_ws, lane * 16, (u32)u < nl ? arow + (u * 2 + s) * 1024 : OOB);
        }
      publish_fetch(std::false_type{}, WS_FLAGS + ((rt * 2 + (j >> 1)) * 48 + 16 + g * 2 + (j & 1)) * 128);
    } else {
      __builtin_amdgcn_s_setprio(0);
      u32x4 pq[4];
      const float rl = LOG2E * __builtin_amdgcn_rcpf(hi_f(ms));
#pragma unroll
      for (int h = 0; h < 4; ++h) {
        float t2[8];
#pragma unroll
        for (int i = 0; i < 8; ++i) t2[i] = mul1(acc[h >> 1][8 * (h & 1) + i], rl);
        sfence();
        pq[h] = phi8(t2);
        w2_chunk(std::false_type{}, 0, h);
      }
      STAMP(4);
      w2_second(std::false_type{}, std::true_type{});
      STAMP(5);
      __builtin_amdgcn_s_setprio(3);
      lds_wait(L_SYNC + 4, 4);
      STAMP(6);
      const float sc = LN2 * hi_f(ms);
      const u32 ka = L_KVF + ((lane & 16) ? 512 + hh * 16 : hh * 256 + (lane & 15) * 16);
      bf16x8 kf[4];
      f32x16 d[4];
#pragma unroll
      for (int h = 0; h < 4; ++h) kf[h] = lds_b128(ka, h * 1024);
      sfence();
#pragma unroll
      for (int h = 0; h < 4; ++h) d[h] = mfma32(kf[h], as_bf(pq[h]), (f32x16){});
      sfence();
#pragma unroll
      for (int h = 0; h < 4; ++h) {
        const float f = sc * __builtin_amdgcn_rcpf(d[h][8] + 1e-6f);
        u32x4 o;
#pragma unroll
        for (int i = 0; i < 4; ++i) o[i] = pack2(mul1(d[h][2 * i], f), mul1(d[h][2 * i + 1], f));
        store_dev(o, srd_ws, lane * 16, WS_A + ((rt * 4 + kq) * NSTEP + 86 + g * 4 + h) * 1024);
      }
      wait_vm<0>();
      store_one((u32x4){(u32)tag, (u32)(tag >> 32), 0u, 0u}, srd_ws, WS_FLAGS + ((rt * 2 + (kq >> 1)) * 48 + 32 + g * 2 + (kq & 1)) * 128);
      __builtin_amdgcn_s_setprio(0);
    }
    second(std::false_type{});
  } else {
    auto first = [&](auto XT_) {
    constexpr bool XT = XT_;
    const u32 mh = w >> 1, nh = w & 1;
    f32x16 acc[2][2] = {};
    bf16x8 xf[4][2] = {}, wf[4][2] = {};
    {
      const u32 row = rt * 128 + c4 * 8 + 2 * w;
      u32x4 a0, a1;
      asm volatile("global_load_dwordx4 %0, %2, %3 ; path %4\n\tglobal_load_dwordx4 %1, %2, %3 offset:512"
                   : "=&v"(a0), "=&v"(a1) : "v"((row + hh) * 1024 + (lane & 31) * 16), "s"(x), "i"((int)XT) : "memory");
      sfence();
      *LDSP(u32, L_SYNC + (lane & 3) * 4) = 0u;
      asm volatile("" : "+v"(acc[0][0]), "+v"(acc[0][1]), "+v"(acc[1][0]), "+v"(acc[1][1]));
      asm volatile("" : "+v"(xf[2][0]), "+v"(xf[2][1]), "+v"(wf[2][0]), "+v"(wf[2][1]), "+v"(xf[3][0]), "+v"(xf[3][1]), "+v"(wf[3][0]), "+v"(wf[3][1]));
      read_epoch();
      *LDSP(u32x2, L_SYNC + 16) = (u32x2){(u32)tag, (u32)(tag >> 32)};
      asm volatile("s_waitcnt vmcnt(0)" : "+v"(a0), "+v"(a1));
      sfence();
      float v[16];
#pragma unroll
      for (int i = 0; i < 4; ++i) { v[2 * i] = lo_f(a0[i]); v[2 * i + 1] = hi_f(a0[i]); v[8 + 2 * i] = lo_f(a1[i]); v[9 + 2 * i] = hi_f(a1[i]); }
      float s[4] = {};
#pragma unroll
      for (int i = 0; i < 16; ++i) s[i & 3] += v[i];
      const float mean = sum32((s[0] + s[1]) + (s[2] + s[3])) * (1.f / 512.f);
      float qq[4] = {};
#pragma unroll
      for (int i = 0; i < 16; ++i) { float d = v[i] - mean; qq[i & 3] = __builtin_fmaf(d, d, qq[i & 3]); }
      const float var = sum32((qq[0] + qq[1]) + (qq[2] + qq[3])) * (1.f / 512.f);
      const u32 p = pack2(mean, __builtin_amdgcn_sqrtf(rbf(rbf(var) + 1e-5f)));
      const u32 p1 = __builtin_amdgcn_readlane(p, 32);
      u32x4 gr = {p, (u32)tag, p1, (u32)(tag >> 32)};
      asm volatile("" : "+v"(gr));
      STAMP(1);
      store_one(gr, srd_ws, WS_STATS + row * 8);
      __builtin_amdgcn_s_setprio(1);
      barrier();
    }
    f32x4 rl[8];
    if constexpr (!XT) {
      phase_a_compute<false>(acc, xf, wf, rl, lane, mh, nh);
      STAMP(2);
#pragma unroll
      for (int k = 0; k < 4; ++k) {
        const int t = k >> 1, u = k & 1;
#pragma unroll
        for (int s = 0; s < 2; ++s) {
          u32x4 o;
#pragma unroll
          for (int i = 0; i < 4; ++i) o[i] = relu2(pack2(acc[t][u][8 * s + 2 * i], acc[t][u][8 * s + 2 * i + 1]));
          store_dev(o, srd_ws, lane * 16, WS_A + ((rt * 4 + mh * 2 + t) * NSTEP + g * 8 + u * 4 + nh * 2 + s) * 1024);
        }
      }
      STAMP(3);
      publish_fetch(std::true_type{}, WS_FLAGS + ((rt * 2 + mh) * 48 + g * 2 + nh) * 128);
    } else {
      phase_a_compute<true>(acc, xf, wf, rl, lane, mh, nh);
      STAMP(2);
      f32x16 kv = {}, zz = {};
      const bf16x8 ones = as_bf((u32x4){0x3f803f80u, 0x3f803f80u, 0x3f803f80u, 0x3f803f80u});
#pragma unroll
      for (int k = 0; k < 4; ++k) {
        const int t = k >> 1, s = k & 1;
        w2_chunk(std::true_type{}, 0, k);
        float t2[8], v2[8];
#pragma unroll
        for (int i = 0; i < 8; ++i) t2[i] = mul1(acc[t][0][8 * s + i], rl[2 * k + (i >> 2)][i & 3]);
#pragma unroll
        for (int i = 0; i < 8; ++i) v2[i] = mul1(acc[t][1][8 * s + i], rl[2 * k + (i >> 2)][i & 3]);
        sfence();
        const u32x4 pv = {pack2(v2[0], v2[1]), pack2(v2[2], v2[3]), pack2(v2[4], v2[5]), pack2(v2[6], v2[7])};
        const u32x4 pk = phi8(t2);
        kv = mfma32(as_bf(pk), as_bf(pv), kv);
        zz = mfma32(as_bf(pk), ones, zz);
        sfence();
      }
      const bool iskv = ((lane >> 4) & 1) == mh;
      f32x4 keep[2], send[2];
      if (mh) {
#pragma unroll
        for (int i = 0; i < 8; ++i) { send[i >> 2][i & 3] = iskv ? zz[i] : kv[i]; keep[i >> 2][i & 3] = iskv ? kv[8 + i] : zz[8 + i]; }
      } else {
#pragma unroll
        for (int i = 0; i < 8; ++i) { send[i >> 2][i & 3] = iskv ? zz[8 + i] : kv[8 + i]; keep[i >> 2][i & 3] = iskv ? kv[i] : zz[i]; }
      }
      *LDSP(f32x4, L_XCH + w * 2048 + lane * 16) = send[0];
      *LDSO(f32x4, L_XCH + w * 2048 + lane * 16, 1024) = send[1];
      lds_signal(L_SYNC + 8 + nh * 4);
      const u32 head = g * 4 + nh * 2 + mh;
      const u32 o0 = iskv ? rt * 1024 + hh * 256 + (lane & 15) * 16 : 16384 + rt * 64 + hh * 16, o1 = o0 + (iskv ? 512 : 32);
      lds_wait(L_SYNC + 8 + nh * 4, 2);
      keep[0] += *LDSP(f32x4, L_XCH + (w ^ 2) * 2048 + lane * 16);
      keep[1] += *LDSO(f32x4, L_XCH + (w ^ 2) * 2048 + lane * 16, 1024);
      store_dev((u32x4){pack2(keep[0][0], keep[0][1]), (u32)tag, pack2(keep[0][2], keep[0][3]), (u32)(tag >> 32)}, srd_ws, o0, WS_KVP + head * KVH_B);
      store_dev((u32x4){pack2(keep[1][0], keep[1][1]), (u32)tag, pack2(keep[1][2], keep[1][3]), (u32)(tag >> 32)}, srd_ws, o1, WS_KVP + head * KVH_B);
      __builtin_amdgcn_s_setprio(0);
      STAMP(3);
      wait_vm<2>();
      w2_second(std::true_type{}, std::false_type{});
      STAMP(5);
      __builtin_amdgcn_s_setprio(3);
      const u32 hl = (w & 1) * 2 + (w >> 1), kvb = WS_KVP + (g * 4 + hl) * KVH_B;
      u32x4 pa[17];
#pragma unroll 1
      for (u32 it = 0; it < SPIN_CAP; ++it) {
#pragma unroll
        for (int i = 0; i < 17; ++i) pa[i] = __builtin_amdgcn_raw_buffer_load_b128(rws, lane * 16, kvb + i * 1024, 16);
        sfence();
        u32 bad = 0;
#pragma unroll
        for (int i = 0; i < 17; ++i) bad |= (pa[i].y ^ (u32)tag) | (pa[i].w ^ (u32)(tag >> 32));
        if (__all(bad == 0)) break;
#pragma unroll 1
        for (; it < SPIN_CAP; ++it) {
          u32x4 z;
          asm volatile("buffer_load_dwordx4 %0, %1, %2, %3 offen sc1\n\ts_waitcnt vmcnt(0)" : "=&v"(z) : "v"(lane * 16), "s"(srd_ws), "s"(kvb + 16384) : "memory");
          if (__all(z.y == (u32)tag && z.w == (u32)(tag >> 32))) break;
        }
      }
      STAMP(6);
      float z[4] = {lo_f(pa[16].x), hi_f(pa[16].x), lo_f(pa[16].z), hi_f(pa[16].z)}, sm[4] = {};
#pragma unroll
      for (int i = 0; i < 4; ++i) z[i] = dpp_add<0x128>(z[i]);
#pragma unroll
      for (int i = 0; i < 4; ++i) z[i] = dpp_add<0x124>(z[i]);
#pragma unroll
      for (int i = 0; i < 4; ++i) z[i] = swap16_add(z[i]);
#pragma unroll
      for (int i = 0; i < 4; ++i) z[i] = swap32_add(z[i]);
#pragma unroll
      for (int i = 0; i < 16; ++i) { sm[0] += lo_f(pa[i].x); sm[1] += hi_f(pa[i].x); sm[2] += lo_f(pa[i].z); sm[3] += hi_f(pa[i].z); }
      *LDSP(u32x2, L_KVF + hl * 1024 + 512 + (lane & 1) * 16 + (lane & 2) * 4) = (u32x2){pack2(z[0], z[1]), pack2(z[2], z[3])};
      *LDSP(u32x2, L_KVF + hl * 1024 + (lane & 31) * 16 + hh * 8) = (u32x2){pack2(sm[0], sm[1]), pack2(sm[2], sm[3])};
      lds_signal(L_SYNC + 4);
      __builtin_amdgcn_s_setprio(0);
    }
    };
    if (xt) first(std::true_type{}); else first(std::false_type{});
    second(std::true_type{});
  }
  {
    STAMP(11);
    barrier();
    STAMP(12);
    const u32 fa = (w * 8 + (lane >> 3)) * 256 + ((((lane & 7) * 2) ^ (lane >> 3)) << 4);
    f32x4 p0[8], p1[8];
#pragma unroll
    for (int p = 0; p < 8; ++p) { p0[p] = *LDSO(f32x4, fa + (p >> 2) * 65536, (p & 3) * 16384); p1[p] = *LDSO(f32x4, (fa ^ 16) + (p >> 2) * 65536, (p & 3) * 16384); }
    sfence();
    const f32x4 s0 = ((p0[0] + p0[1]) + (p0[2] + p0[3])) + ((p0[4] + p0[5]) + (p0[6] + p0[7]));
    const f32x4 s1 = ((p1[0] + p1[1]) + (p1[2] + p1[3])) + ((p1[4] + p1[5]) + (p1[6] + p1[7]));
    const float rs = __builtin_amdgcn_rcpf(hi_f(st));
    u32x4 o;
    o[0] = pack2(__builtin_fmaf(s0[0], rs, lo_f(xr[0])), __builtin_fmaf(s0[1], rs, hi_f(xr[0])));
    o[1] = pack2(__builtin_fmaf(s0[2], rs, lo_f(xr[1])), __builtin_fmaf(s0[3], rs, hi_f(xr[1])));
    o[2] = pack2(__builtin_fmaf(s1[0], rs, lo_f(xr[2])), __builtin_fmaf(s1[1], rs, hi_f(xr[2])));
    o[3] = pack2(__builtin_fmaf(s1[2], rs, lo_f(xr[3])), __builtin_fmaf(s1[3], rs, hi_f(xr[3])));
    *(GAS u32x4*)(out + tok * 1024 + ct * 128 + (lane & 7) * 16) = o;
    if (b == 0 && w == 0) store_one((u32x4){(u32)tag, (u32)(tag >> 32), 0u, 0u}, srd_ws, WS_EPOCH);
    STAMP(13);
    { u32 lim = 4000; if (ptime[1] && (w >> 2) == PGRP && (c4 & 1) == PTYPE) lim += 10 * (ptime[1] - ptime[0]);
      while ((u32)__builtin_amdgcn_s_memrealtime() - ptime[0] < lim) __builtin_amdgcn_s_sleep(2); }
  }
}

extern "C" void kernel_launch(void* const* d_in, const int* in_sizes, int n_in, void* d_out, int out_size, void* d_ws, size_t ws_size,
                              hipStream_t stream) {
  fused_block<<<256, 512, 0, stream>>>((const char*)d_in[0], (const char*)d_in[1], (const char*)d_in[4], (char*)d_ws, (const char*)d_in[5],
                                       (const char*)d_in[2], (char*)d_out);
}
```

```cpp
#define PROBE 10
#define PGRP 1
#define PTYPE 0
#include <hip/hip_runtime.h>
#include <hip/hip_bf16.h>
#include <type_traits>

typedef unsigned u32;
typedef unsigned long long u64;
typedef __bf16 bf16x8 __attribute__((ext_vector_type(8)));
typedef __bf16 bf16x2 __attribute__((ext_vector_type(2)));
typedef short s16x4 __attribute__((ext_vector_type(4)));
typedef short s16x2 __attribute__((ext_vector_type(2)));
typedef float f32x16 __attribute__((ext_vector_type(16)));
typedef float f32x4 __attribute__((ext_vector_type(4)));
typedef float f32x2 __attribute__((ext_vector_type(2)));
typedef u32 u32x4 __attribute__((ext_vector_type(4)));
typedef u32 u32x2 __attribute__((ext_vector_type(2)));
#define LAS __attribute__((address_space(3)))
#define GAS __attribute__((address_space(1)))
#define DEV __device__ __forceinline__
#define RLX_AGENT __ATOMIC_RELAXED, __HIP_MEMORY_SCOPE_AGENT
#define LDSP(T, a) ((LAS T*)(size_t)(a))

#define ROT1 1
#define PF 0
#define QWIN 3
#define AWIN 5
#define DLY 0
#define SLP 2
#define PSC "sc1"
#define SL 10
#define SO 16
#define YPRIO 0
#define PRIO2 1

constexpr u32 WS_EPOCH = 0;
constexpr u32 WS_STATS = 4096;
constexpr u32 WS_FLAGS = WS_STATS + 16384;
constexpr u32 WS_XFL = WS_FLAGS + 32 * 48 * 128;
constexpr u32 WS_KVF = WS_XFL + 1024 * 128;
constexpr u32 WS_KVZ = WS_KVF + 32768;
constexpr u32 WS_KVP = WS_KVZ + 2048;
constexpr u32 KVH_B = 34816;
constexpr u32 WS_A = 2u << 20;
constexpr u32 NSTEP = 118;
constexpr u32 WS_X = WS_A + 64 * NSTEP * 1024;
constexpr u32 WS_END = WS_X + 4096 * 2048;
constexpr u32 WS_STAMP = 32u << 20;
static_assert(WS_KVP + 32 * KVH_B <= WS_A, "ws map");
constexpr u32 OOB = 0x40000000u;

constexpr u32 L_WE = 65536;
constexpr u32 L_SCR = 131072;
constexpr u32 L_CORRA = L_SCR;
constexpr u32 L_MN = L_SCR + 2048;
constexpr u32 L_CORRB = L_SCR + 4096;
constexpr u32 L_GRAN = L_SCR + 5120;
constexpr u32 L_RL = L_SCR + 6144;
constexpr u32 L_SYNC = L_SCR + 6656;
constexpr u32 L_XCH = L_SCR + 8192;
constexpr u32 L_KVF = L_SCR + 16384;
constexpr u32 L_FINE = L_SCR + 24576;
constexpr u32 L_TOTAL = 163840;

constexpr u32 CODE_LINES = 176;
constexpr u32 SPIN_CAP = 1u << 18;
constexpr float LOG2E = 1.4426950408889634f, LN2 = 0.6931471805599453f;

DEV void sfence() { __builtin_amdgcn_sched_barrier(0); }
template <int N> DEV void wait_vm() { asm volatile("s_waitcnt vmcnt(%0)" ::"i"(N) : "memory"); }
DEV void wait_lgkm() { sfence(); __builtin_amdgcn_s_waitcnt(0xC07F); asm volatile("" ::: "memory"); sfence(); }
DEV void barrier() { sfence(); __builtin_amdgcn_s_waitcnt(0xC07F); asm volatile("s_barrier" ::: "memory"); sfence(); }

DEV u32x4 make_srd(const void* p, u32 bytes) {
  u64 a = (u64)p;
  return (u32x4){(u32)a, (u32)(a >> 32) & 0xffffu, bytes, 0x00020000u};
}
template <int POL> DEV void dma(u32x4 srd, u32 voff, u32 soff, u32 ldsaddr) {
  if (POL == 1)
    asm volatile("s_mov_b32 m0, %3\n\ts_nop 0\n\tbuffer_load_dwordx4 %0, %1, %2 offen nt lds" ::"v"(voff), "s"(srd), "s"(soff), "s"(ldsaddr) : "memory");
  else if (POL == 2)
    asm volatile("s_mov_b32 m0, %3\n\ts_nop 0\n\tbuffer_load_dwordx4 %0, %1, %2 offen sc1 lds" ::"v"(voff), "s"(srd), "s"(soff), "s"(ldsaddr) : "memory");
  else
    asm volatile("s_mov_b32 m0, %3\n\ts_nop 0\n\tbuffer_load_dwordx4 %0, %1, %2 offen lds" ::"v"(voff), "s"(srd), "s"(soff), "s"(ldsaddr) : "memory");
}
template <int POL> DEV void dma4(u32x4 srd, u32x4 v, u32 soff, u32 ldsaddr) {
  if (POL == 1)
    asm volatile("s_mov_b32 m0, %6\n\ts_nop 0\n\tbuffer_load_dwordx4 %0, %4, %5 offen nt lds\n\tbuffer_load_dwordx4 %1, %4, %5 offen offset:1024 nt lds\n\t"
                 "buffer_load_dwordx4 %2, %4, %5 offen offset:2048 nt lds\n\tbuffer_load_dwordx4 %3, %4, %5 offen offset:3072 nt lds"
                 ::"v"(v.x), "v"(v.y), "v"(v.z), "v"(v.w), "s"(srd), "s"(soff), "s"(ldsaddr) : "memory");
  else
    asm volatile("s_mov_b32 m0, %6\n\ts_nop 0\n\tbuffer_load_dwordx4 %0, %4, %5 offen lds\n\tbuffer_load_dwordx4 %1, %4, %5 offen offset:1024 lds\n\t"
                 "buffer_load_dwordx4 %2, %4, %5 offen offset:2048 lds\n\tbuffer_load_dwordx4 %3, %4, %5 offen offset:3072 lds"
                 ::"v"(v.x), "v"(v.y), "v"(v.z), "v"(v.w), "s"(srd), "s"(soff), "s"(ldsaddr) : "memory");
}
DEV void touch64(u32x4 srd, u32 voff, u32 soff, u32 ldsaddr) {
  asm volatile("s_mov_b32 m0, %3\n\ts_nop 0\n\tbuffer_load_dword %0, %1, %2 offen lds" ::"v"(voff), "s"(srd), "s"(soff), "s"(ldsaddr) : "memory");
}
DEV u32x4 steps4(u32 v, u32 step) { step -= 1024; return (u32x4){v, v + step, v + 2 * step, v + 3 * step}; }
DEV void store_one(u32x4 v, u32x4 srd, u32 soff) {
  asm volatile("s_mov_b64 exec, 1\n\tbuffer_store_dwordx4 %0, off, %1, %2 sc1\n\ts_mov_b64 exec, -1\n\ts_nop 0" ::"v"(v), "s"(srd), "s"(soff) : "memory");
}
DEV void store_dev(u32x4 v, u32x4 srd, u32 voff, u32 soff) {
  asm volatile("buffer_store_dwordx4 %0, %1, %2, %3 offen sc1\n\ts_nop 1" ::"v"(v), "v"(voff), "s"(srd), "s"(soff) : "memory");
}
#define LDSO(T, a, o) ((LAS T*)(LDSP(char, a) + (o)))
DEV bf16x8 lds_b128(u32 a, int o = 0) { return *LDSO(bf16x8, a, o); }
template <int D> DEV bf16x8 lds_tr(u32 a, int o) {
  s16x4 lo = __builtin_amdgcn_ds_read_tr16_b64_v4i16(LDSO(s16x4, a, o));
  s16x4 hi = __builtin_amdgcn_ds_read_tr16_b64_v4i16(LDSO(s16x4, a, o + D));
  return __builtin_bit_cast(bf16x8, __builtin_shufflevector(lo, hi, 0, 1, 2, 3, 4, 5, 6, 7));
}
DEV f32x16 mfma32(bf16x8 a, bf16x8 b, f32x16 c) { return __builtin_amdgcn_mfma_f32_32x32x16_bf16(a, b, c, 0, 0, 0); }
DEV f32x4 mfma16(bf16x8 a, bf16x8 b, f32x4 c) { return __builtin_amdgcn_mfma_f32_16x16x32_bf16(a, b, c, 0, 0, 0); }
DEV bf16x8 as_bf(u32x4 v) { return __builtin_bit_cast(bf16x8, v); }
DEV u32 pack2(float a, float b) { return __builtin_bit_cast(u32, __builtin_convertvector((f32x2){a, b}, bf16x2)); }
DEV u32 relu2(u32 v) { return __builtin_bit_cast(u32, __builtin_elementwise_max(__builtin_bit_cast(s16x2, v), (s16x2){0, 0})); }
DEV float lo_f(u32 v) { return __uint_as_float(v << 16); }
DEV float hi_f(u32 v) { return __uint_as_float(v & 0xffff0000u); }
DEV float rbf(float f) { return lo_f(pack2(f, 0.f)); }
DEV u32 split_hi_lo(float c) { float h = rbf(c); return pack2(h, c - h); }
DEV float mul1(float a, float b) { float r = a * b; asm("" : "+v"(r)); return r; }
template <int CTRL> DEV float dpp_add(float v) {
  return v + __uint_as_float(__builtin_amdgcn_update_dpp(0u, __float_as_uint(v), CTRL, 0xf, 0xf, true));
}
DEV float swap16_add(float v) {
  auto r = __builtin_amdgcn_permlane16_swap(__float_as_uint(v), __float_as_uint(v), false, false);
  return __uint_as_float(r[0]) + __uint_as_float(r[1]);
}
DEV float swap32_add(float v) {
  auto r = __builtin_amdgcn_permlane32_swap(__float_as_uint(v), __float_as_uint(v), false, false);
  return __uint_as_float(r[0]) + __uint_as_float(r[1]);
}
DEV float sum32(float v) {
  v = dpp_add<0xB1>(v); v = dpp_add<0x4E>(v); v = dpp_add<0x141>(v); v = dpp_add<0x140>(v);
  return swap16_add(v);
}
DEV u32x4 phi8(const float (&t2)[8]) {
  float e[8], m[8];
#pragma unroll
  for (int i = 0; i < 8; ++i) e[i] = __builtin_amdgcn_fmed3f(__builtin_amdgcn_exp2f(t2[i]), 0.f, 1.f);
  sfence();
#pragma unroll
  for (int i = 0; i < 8; ++i) asm("v_max_f32 %0, %1, %2" : "=v"(m[i]) : "s"(1e-6f * LOG2E), "v"(t2[i]));
  sfence();
#pragma unroll
  for (int i = 0; i < 8; ++i) { e[i] = __builtin_fmaf(m[i], LN2, e[i]); asm("" : "+v"(e[i])); }
  sfence();
  return (u32x4){pack2(e[0], e[1]), pack2(e[2], e[3]), pack2(e[4], e[5]), pack2(e[6], e[7])};
}
DEV void lds_signal(u32 addr) {
  asm volatile("s_mov_b64 exec, 1\n\tds_add_u32 %0, %1\n\ts_mov_b64 exec, -1" ::"v"(addr), "v"(1u) : "memory");
}
DEV void lds_wait(u32 addr, u32 waves) {
  u32 v, s, n = SPIN_CAP;
  asm volatile("1:\n\tds_read_b32 %0, %3\n\ts_waitcnt lgkmcnt(0)\n\tv_readfirstlane_b32 %1, %0\n\ts_sub_u32 %2, %2, 1\n\ts_cbranch_scc1 2f\n\t"
               "s_cmp_lt_u32 %1, %4\n\ts_cbranch_scc1 1b\n2:"
               : "=&v"(v), "=&s"(s), "+s"(n) : "v"(addr), "s"(waves) : "memory", "scc");
}
DEV u64 poll4(const char* p, u64 tag, u32 n) {
  u64 f0, f1, f2, f3;
  asm volatile("1:\n\ts_load_dwordx2 %0, %5, 0 glc\n\ts_load_dwordx2 %1, %5, 128 glc\n\ts_load_dwordx2 %2, %5, 256 glc\n\t"
               "s_load_dwordx2 %3, %5, 384 glc\n\ts_waitcnt lgkmcnt(0)\n\t"
               "s_xor_b64 %0, %0, %6\n\ts_xor_b64 %1, %1, %6\n\ts_xor_b64 %2, %2, %6\n\ts_xor_b64 %3, %3, %6\n\t"
               "s_or_b64 %0, %0, %1\n\ts_or_b64 %2, %2, %3\n\ts_or_b64 %0, %0, %2\n\ts_cbranch_scc0 2f\n\t"
               "s_sub_u32 %4, %4, 1\n\ts_cbranch_scc0 1b\n2:"
               : "=&s"(f0), "=&s"(f1), "=&s"(f2), "=&s"(f3), "+s"(n) : "s"(p), "s"(tag) : "memory", "scc");
  return f0;
}
template <int D> DEV void poll2(const char* p, u64 tag, u64 mask) {
  u64 f0, f1; u32 n = SPIN_CAP;
  asm volatile("1:\n\ts_load_dwordx2 %0, %3, 0 glc\n\ts_load_dwordx2 %1, %3, %6 glc\n\ts_waitcnt lgkmcnt(0)\n\t"
               "s_xor_b64 %0, %0, %4\n\ts_xor_b64 %1, %1, %4\n\ts_or_b64 %0, %0, %1\n\ts_and_b64 %0, %0, %5\n\ts_cbranch_scc0 2f\n\t"
               "s_sub_u32 %2, %2, 1\n\ts_cbranch_scc0 1b\n2:"
               : "=&s"(f0), "=&s"(f1), "+s"(n) : "s"(p), "s"(tag), "s"(mask), "i"(D) : "memory", "scc");
}
DEV u32x4 load_dev_wait(u32x4 srd, u32 voff, u32 soff) {
  u32x4 v;
  asm volatile("buffer_load_dwordx4 %0, %1, %2, %3 offen sc1\n\ts_waitcnt vmcnt(0)" : "=&v"(v) : "v"(voff), "s"(srd), "s"(soff) : "memory");
  return v;
}
template <int N, int I = 0, class F> DEV void static_for(F&& f) {
  if constexpr (I < N) { f(std::integral_constant<int, I>{}); static_for<N, I + 1>(f); }
}
template <int T, int K> DEV void need(u32x4& v) {
  constexpr int issued = 16 + 4 * ((T < 6 ? T : 6) / 2 + 1) + (T >= SL ? 6 : 0) + (T >= SO ? 8 : 0), idx = K < 16 ? K : 16 + K;
  asm volatile("s_waitcnt vmcnt(%1)" : "+v"(v) : "i"(issued - idx - 1));
}
template <class T> DEV void keep(T& v) { asm volatile("" : "+v"(v)); }
template <class T> DEV T opaque_v(T v) { asm volatile("" : "+v"(v)); return v; }
template <class T> DEV T opaque_s(T v) { asm volatile("" : "+s"(v)); return v; }

#define STAMP(i) do { if ((i) == 0 || (i) == PROBE) { sfence(); stamp[(i) ? 1 : 0] = (u32)__builtin_amdgcn_s_memrealtime(); __builtin_amdgcn_s_waitcnt(0xC07F); sfence(); } } while (0)
#define FSTAMP(w, i)

struct Ptrs { const char *x, *wqkv, *w1; char* ws; const char *w2, *wo; char* out; };

DEV void group1(const Ptrs& p, u32 b, u32 lane, u32 w, u32* stamp) {
  const u32 xcd = b & 7, r = b >> 3, j = w & 3;
  const u32 rt = (xcd & 1) * 8 + (r & 7);
  const u32 rot = ROT1 == 0 ? 0 : ROT1 == 1 ? (r & 7) : (r & 7) + 2 * (r >> 3);
  const u32x4 srd_x = make_srd(p.x, 2048 * 1024);
  const u32 vx = (lane >> 3) * 1024 + (((lane & 7) ^ (lane >> 4)) << 4);
  const u32x4 vx4 = {vx, (vx ^ 64) + 7168, vx + 14336, (vx ^ 64) + 21504};
  auto issue_x = [&](int st) { dma4<0>(srd_x, vx4, (rt * 128 + 32 * j) * 1024 + ((st + rot) & 7) * 128, (st & 3) * 32768 + j * 4096); };
  issue_x(0);
  STAMP(10);
  sfence();
  const u32 c4 = (xcd >> 1) * 4 + (r >> 3), g = c4 >> 1;
  const bool xt = (c4 & 1) == 0;
  const u32 hh = lane >> 5, q = (lane & 15) >> 2;
  const u32 wst = xt ? 3072 : 2752;
  const u32x4 srd_w = make_srd(xt ? p.wqkv : p.w1, 512 * wst);
  const u32 ym = xt ? 0u : ~0u, l4 = lane >> 4;
  const u32 blk = ((q ^ l4) + (((q & l4) << 1) & ym)) & 3;
  const u32x4 vwa4 = steps4(l4 * wst + (lane & 3) * 16 + (blk & 1) * 64 + (blk >> 1) * (xt ? 1024 : 128) + (xt ? 1024 + g * 128 : g * 256), 4 * wst);
  auto issue_w = [&](int st) { dma4<0>(srd_w, vwa4, (((st + rot) & 7) * 64 + 16 * j) * wst, (st & 3) * 32768 + 16384 + j * 4096); };
  issue_w(0);
  const u32 row = rt * 128 + c4 * 8 + 2 * j;
  u32x4 a0, a1;
  asm volatile("global_load_dwordx4 %0, %2, %3\n\tglobal_load_dwordx4 %1, %2, %3 offset:512"
               : "=&v"(a0), "=&v"(a1) : "v"((row + hh) * 1024 + (lane & 31) * 16), "s"(p.x) : "memory");
  STAMP(11);
  issue_x(1); issue_w(1);
  sfence();
  u64 tag;
  asm volatile("s_load_dwordx2 %0, %1, 0\n\ts_waitcnt lgkmcnt(0)" : "=&s"(tag) : "s"(p.ws) : "memory");
  tag += 0x9E3779B97F4A7C15ull;
  const u32 nl = (xt || (g < 6 && !(g & 1))) ? 2 : 1, lstep = xt ? 86 + g * 4 : 64 + (g >> 1) * 6 + ((g & 1) ? (g < 6 ? 4 : 2) : 0);
  const u32x4 vwb4 = steps4((lane >> 3) * wst + ((((lane & 7) ^ ((lane >> 2) & 4)) << 4) & (nl == 2 ? 127 : 48)) + (xt ? g * 128 : lstep * 32), 8 * wst);
  auto issue = [&](int st, int part) {
    if (st < 8) { if (part) issue_w(st); else issue_x(st); }
    else dma4<0>(srd_w, vwb4, ((((st - 8) * 4 + j + rot) & 7) * 64 + 32 * part) * wst, (st - 8) * 32768 + j * 8192 + part * 4096);
  };
  const u32x4 srd_ws = make_srd(p.ws, WS_END);
  const u32 xa = (j * 32 + (lane & 31)) * 128 + ((hh ^ ((lane >> 1) & 7)) << 4);
  const u32 selv = (((lane >> 4) ^ (lane >> 2)) & 1) ? 0u : 0x3f803f80u;
  const bf16x8 sel = as_bf((u32x4){selv, selv, selv, selv});
  const u32 wa = 16384 + (8 * hh + q) * 256 + (lane & 16) * 2 + (lane & 3) * 8 + ((xt ? (j ^ q) : ((j - q) & 3)) << 6);
  f32x4 cs = {};
  bf16x8 xf[32];
  u32 mn;
  float rl;
#pragma unroll
  for (int st = 0; st < 8; ++st) {
    if (st == 1) wait_vm<9>(); else wait_vm<8>();
    barrier();
    if (st == 0) STAMP(1);
    const u32 slot = (st & 3) * 32768;
    if (st == 5) dma<2>(srd_ws, lane * 16, j == 0 ? WS_STATS + rt * 1024 : OOB, j == 0 ? L_GRAN : L_XCH);
    issue(st + 2, 0);
    sfence();
    u32x4 gr;
    if (st == 7) gr = *LDSP(u32x4, L_GRAN + (j * 16 + ((lane & 31) >> 1)) * 16);
    bf16x8 cw[4];
#pragma unroll
    for (int s = 0; s < 4; ++s) cw[s] = lds_tr<1024>(wa + (slot & 65536), (slot & 32768) + s * 4096);
#pragma unroll
    for (int s = 0; s < 4; ++s) xf[st * 4 + s] = lds_b128((xa ^ (s << 5)) + (slot & 65536), slot & 32768);
    sfence();
    issue(st + 2, 1);
    sfence();
    if (st == 0) {
      asm volatile("" : "+v"(a0), "+v"(a1));
    float v[16];
#pragma unroll
    for (int i = 0; i < 4; ++i) { v[2 * i] = lo_f(a0[i]); v[2 * i + 1] = hi_f(a0[i]); v[8 + 2 * i] = lo_f(a1[i]); v[9 + 2 * i] = hi_f(a1[i]); }
    float s[4] = {};
#pragma unroll
    for (int i = 0; i < 16; ++i) s[i & 3] += v[i];
    const float mean = sum32((s[0] + s[1]) + (s[2] + s[3])) * (1.f / 512.f);
    float qq[4] = {};
#pragma unroll
    for (int i = 0; i < 16; ++i) { float d = v[i] - mean; qq[i & 3] = __builtin_fmaf(d, d, qq[i & 3]); }
    const float var = sum32((qq[0] + qq[1]) + (qq[2] + qq[3])) * (1.f / 512.f);
    const u32 pm = pack2(mean, __builtin_amdgcn_sqrtf(rbf(rbf(var) + 1e-5f)));
    const u32 p1 = __builtin_amdgcn_readlane(pm, 32);
    store_one(opaque_v((u32x4){pm, (u32)tag, p1, (u32)(tag >> 32)}), srd_ws, WS_STATS + row * 8);
      *LDSP(u32x2, L_SYNC + 40) = (u32x2){(u32)tag, (u32)(tag >> 32)};
    }
    if (st == 7) {
#pragma unroll 1
      for (u32 i = 0; i < SPIN_CAP; ++i) {
        if (__all(gr.y == (u32)tag && gr.w == (u32)(tag >> 32))) break;
        gr = load_dev_wait(srd_ws, (j * 16 + ((lane & 31) >> 1)) * 16, WS_STATS + rt * 1024);
      }
      const u32 ms = (lane & 1) ? gr.z : gr.x;
      mn = ((ms & 0xffffu) ^ 0x8000u) * 0x10001u;
      rl = LOG2E * __builtin_amdgcn_rcpf(hi_f(ms));
      *LDSP(u32x4, L_MN + (j * 32 + (lane & 31)) * 16) = (u32x4){mn, mn, mn, mn};
      *LDSP(float, L_RL + (j * 32 + (lane & 31)) * 4) = rl;
      mn = hh ? 0u : mn;
    }
    sfence();
#pragma unroll
    for (int s = 0; s < 4; ++s) cs = mfma16(sel, cw[s], cs);
    asm volatile("" : "+v"(cs));
    sfence();
  }
  *LDSP(u32, L_CORRA + (j * 32 + (lane & 31)) * 16) = split_hi_lo(cs[0]);
  const u32 kq = c4 >> 2, cb = (c4 & 3) * 256;
  const u32x4 vw2 = steps4(l4 * 1024 + (lane & 3) * 16 + ((q ^ l4) << 6), 4096);
  const u32 wb0 = opaque_v(hh * 1024 + q * 128 + (lane & 16) * 2 + (lane & 3) * 8 + ((q & 2) << 5)), wb1 = opaque_v(wb0 ^ 64);
  const u32 wc0 = opaque_v(wb0 + j * 8192), wc1 = opaque_v(wb1 + j * 8192);
  wait_vm<8>();
  __builtin_amdgcn_s_setprio(1);
  barrier();
  STAMP(2);
  dma4<0>(make_srd(p.w2, 1376 * 1024), vw2, (16 * kq + j) * 16384 + cb, L_WE + j * 4096);
  dma4<0>(make_srd(p.w2, 1376 * 1024), vw2, (16 * kq + 4 + j) * 16384 + cb, L_WE + (4 + j) * 4096);
  f32x16 acc[2] = {};
  {
    bf16x8 wf[4][2], cw[3];
    f32x4 c0 = {}, c1 = {};
    u32 cnt = 0;
    auto ldw = [&](int s, int u) { wf[s & 3][u] = lds_tr<512>(u ? wb1 : wb0, s * 2048); sfence(); };
#pragma unroll
    for (int s = 0; s < 3; ++s) { ldw(s, 0); ldw(s, 1); }
#pragma unroll
    for (int s = 0; s < 32; ++s) {
      const int cr = s < 8 ? s : (s >= 14 && s < 22 ? s - 6 : -1), cm = (s >= 2 && s < 10) ? s - 2 : (s >= 16 && s < 24 ? s - 8 : -1);
      acc[0] = mfma32(wf[s & 3][0], xf[s], acc[0]); sfence();
      if (s == 12 && cnt < 4) lds_wait(L_SYNC, 4);
      if (s == 31 && cnt < 8) lds_wait(L_SYNC, 8);
      if (s + 3 < 32) ldw(s + 3, 0);
      if (s == 31) { wf[0][0] = lds_b128(L_CORRB + (lane & 31) * 16); wf[0][1] = lds_b128(L_CORRB + (lane & 31) * 16, 512); sfence(); }
      if (cr >= 0) { cw[cr % 3] = lds_tr<512>((cr & 1) ? wc1 : wc0, (cr >> 3) * 32768 + ((cr >> 1) & 3) * 2048); sfence(); }
      acc[1] = mfma32(wf[s & 3][1], xf[s], acc[1]); sfence();
      if (s + 3 < 32) ldw(s + 3, 1);
      if (cm >= 0) { if (cm & 1) c1 = mfma16(sel, cw[cm % 3], c1); else c0 = mfma16(sel, cw[cm % 3], c0); sfence(); }
      if (s == 7) { wait_vm<8>(); lds_signal(L_SYNC); }
      if (s == 9 || s == 29) cnt = __hip_atomic_load(LDSP(u32, L_SYNC), __ATOMIC_RELAXED, __HIP_MEMORY_SCOPE_WORKGROUP);
      if (s == 11 || s == 30) cnt = __builtin_amdgcn_readfirstlane(cnt);
      if (s == 25) *LDSP(u32, L_CORRB + lane * 16 + j * 4) = split_hi_lo(hh ? c1[0] : c0[0]);
      if (s == 28) { wait_vm<0>(); lds_signal(L_SYNC); }
      sfence();
    }
    const bf16x8 mf = as_bf((u32x4){mn, mn, mn, mn});
    acc[0] = mfma32(wf[0][0], mf, acc[0]);
    acc[1] = mfma32(wf[0][1], mf, acc[1]);
  }
  STAMP(3);
  __builtin_amdgcn_s_setprio(3);
  {
    const u32 arow = WS_A + ((rt * 4 + j) * NSTEP + lstep) * 1024;
    if (!xt) {
#pragma unroll
      for (int h = 0; h < 4; ++h) {
        u32x4 o;
#pragma unroll
        for (int i = 0; i < 4; ++i) o[i] = relu2(pack2(acc[h >> 1][8 * (h & 1) + 2 * i], acc[h >> 1][8 * (h & 1) + 2 * i + 1]));
        store_dev(o, srd_ws, lane * 16, (u32)(h >> 1) < nl ? arow + h * 1024 : OOB);
      }
    } else {
#pragma unroll
      for (int h = 0; h < 4; ++h) {
        float t2[8];
#pragma unroll
        for (int i = 0; i < 8; ++i) t2[i] = mul1(acc[h >> 1][8 * (h & 1) + i], rl);
        sfence();
        store_dev(phi8(t2), srd_ws, lane * 16, arow + h * 1024);
      }
    }
  }
  STAMP(4);
  const u32 hd = g * 4 + j, kvb = WS_KVP + hd * KVH_B;
  const bool zown = rt == (hd & 15);
  const u32 sa = xt ? OOB : kvb + rt * 2048, sz = (!xt && zown) ? kvb + 32768 : OOB;
  u32x4 a, a2, z, z2;
#define SLICE_LOADS "buffer_load_dwordx4 %0, %4, %5, %6 offen sc1\n\tbuffer_load_dwordx4 %1, %4, %5, %6 offen offset:1024 sc1\n\t" \
                    "buffer_load_dwordx4 %2, %4, %5, %7 offen sc1\n\tbuffer_load_dwordx4 %3, %4, %5, %7 offen offset:1024 sc1"
  asm volatile(SLICE_LOADS : "=&v"(a), "=&v"(a2), "=&v"(z), "=&v"(z2) : "v"(lane * 16), "s"(srd_ws), "s"(sa), "s"(sz) : "memory");
  wait_vm<4>();
  store_one((u32x4){(u32)tag, (u32)(tag >> 32), 0u, 0u}, srd_ws, WS_FLAGS + ((rt * 2 + (j >> 1)) * 48 + (xt ? 32 : 16) + g * 2 + (j & 1)) * 128);
  asm volatile("s_waitcnt vmcnt(1)" : "+v"(a), "+v"(a2), "+v"(z), "+v"(z2));
  STAMP(5);
  if (!xt) {
#pragma unroll 1
    for (u32 i = 0; i < 64; ++i) {
      u32 bad = (a.y ^ (u32)tag) | (a.w ^ (u32)(tag >> 32)), bad2 = (a2.y ^ (u32)tag) | (a2.w ^ (u32)(tag >> 32));
      if (zown) { bad |= (z.y ^ (u32)tag) | (z.w ^ (u32)(tag >> 32)); bad2 |= (z2.y ^ (u32)tag) | (z2.w ^ (u32)(tag >> 32)); }
      const u64 m = __ballot(bad != 0), m2 = __ballot(bad2 != 0);
      if ((m | m2) == 0) break;
      poll2<0>(p.ws + sa + (m ? __builtin_ctzll(m) : 64 + __builtin_ctzll(m2)) * 16, tag << 32, 0xffffffff00000000ull);
      asm volatile(SLICE_LOADS "\n\ts_waitcnt vmcnt(0)" : "=&v"(a), "=&v"(a2), "=&v"(z), "=&v"(z2) : "v"(lane * 16), "s"(srd_ws), "s"(sa), "s"(sz) : "memory");
    }
    float v[8] = {lo_f(a.x) + lo_f(a2.x), hi_f(a.x) + hi_f(a2.x), lo_f(a.z) + lo_f(a2.z), hi_f(a.z) + hi_f(a2.z),
                  lo_f(z.x) + lo_f(z2.x), hi_f(z.x) + hi_f(z2.x), lo_f(z.z) + lo_f(z2.z), hi_f(z.z) + hi_f(z2.z)};
#pragma unroll
    for (int i = 0; i < 8; ++i) v[i] = dpp_add<0x128>(v[i]);
#pragma unroll
    for (int i = 0; i < 8; ++i) v[i] = dpp_add<0x124>(v[i]);
#pragma unroll
    for (int i = 0; i < 4; ++i) {
      auto sw = __builtin_amdgcn_permlane16_swap(__float_as_uint(v[i]), __float_as_uint(v[4 + i]), false, false);
      v[i] = swap32_add(__uint_as_float(sw[0]) + __uint_as_float(sw[1]));
    }
    const u32 fo = lane < 4 ? WS_KVF + hd * 1024 + rt * 64 + lane * 16 : ((lane >> 2) == 4 && zown ? WS_KVZ + hd * 64 + (lane & 3) * 16 : OOB);
    store_dev((u32x4){pack2(v[0], v[1]), (u32)tag, pack2(v[2], v[3]), (u32)(tag >> 32)}, srd_ws, fo, 0);
  }
  __builtin_amdgcn_s_setprio(0);
  STAMP(6);
  const char* fq = p.ws + WS_FLAGS + ((rt * 2 + (j >> 1)) * 48 + 16 + kq * 4 + (j & 1)) * 128;
  const u32 rdy = L_SYNC + 48 + j * 4;
  u32 now = 0;
#pragma unroll 1
  for (u32 i = 0; i < SPIN_CAP && now == 0; ++i) {
    u64 f0, f1, f2, f3;
    asm volatile("s_load_dwordx2 %0, %4, 0 glc\n\ts_load_dwordx2 %1, %4, 256 glc\n\ts_load_dwordx2 %2, %4, 2048 glc\n\t"
                 "s_load_dwordx2 %3, %4, 2304 glc\n\ts_waitcnt lgkmcnt(0)"
                 : "=&s"(f0), "=&s"(f1), "=&s"(f2), "=&s"(f3) : "s"(fq) : "memory");
    now = ((f0 ^ tag) | (f1 ^ tag)) ? 0 : (((f2 ^ tag) | (f3 ^ tag)) ? 1 : 2);
  }
  *LDSP(u32, rdy) = now;
  __builtin_amdgcn_s_setprio(3);
  STAMP(7);
  u32x4 k0, k1, z0, z1;
#define KV_LOADS "buffer_load_dwordx4 %0, %4, %6, %7 offen sc1\n\tbuffer_load_dwordx4 %1, %4, %6, %7 offen offset:1024 sc1\n\t" \
                 "buffer_load_dwordx4 %2, %5, %6, %8 offen sc1\n\tbuffer_load_dwordx4 %3, %5, %6, %8 offen offset:64 sc1"
#define KV_OPS : "=&v"(k0), "=&v"(k1), "=&v"(z0), "=&v"(z1) \
               : "v"(lane * 16), "v"(l4 * 16), "s"(srd_ws), "s"(WS_KVF + (kq * 8 + j * 2) * 1024), "s"(WS_KVZ + (kq * 8 + j * 2) * 64) : "memory"
  asm volatile(KV_LOADS KV_OPS);
  poll2<256>(fq + 2048, tag, ~0ull);
  *LDSP(u32, rdy) = 2u;
  asm volatile("s_waitcnt vmcnt(0)" : "+v"(k0), "+v"(k1), "+v"(z0), "+v"(z1));
  STAMP(8);
#pragma unroll 1
  for (u32 i = 0; i < SPIN_CAP; ++i) {
    const u32 bad = ((k0.y ^ (u32)tag) | (k1.y ^ (u32)tag) | (z0.y ^ (u32)tag) | (z1.y ^ (u32)tag)) |
                    ((k0.w ^ (u32)(tag >> 32)) | (k1.w ^ (u32)(tag >> 32)) | (z0.w ^ (u32)(tag >> 32)) | (z1.w ^ (u32)(tag >> 32)));
    if (__all(bad == 0)) break;
    __builtin_amdgcn_s_sleep(4);
    asm volatile(KV_LOADS "\n\ts_waitcnt vmcnt(0)" KV_OPS);
  }
  STAMP(9);
  {
    auto a0 = __builtin_amdgcn_permlane16_swap(k0.x, z0.x, false, false), a1 = __builtin_amdgcn_permlane16_swap(k0.z, z0.z, false, false);
    auto b0 = __builtin_amdgcn_permlane16_swap(k1.x, z1.x, false, false), b1 = __builtin_amdgcn_permlane16_swap(k1.z, z1.z, false, false);
    *LDSP(u32x4, L_KVF + j * 2048 + lane * 16) = (u32x4){a0[0], a1[0], a0[1], a1[1]};
    *LDSO(u32x4, L_KVF + j * 2048 + lane * 16, 1024) = (u32x4){b0[0], b1[0], b0[1], b1[1]};
    lds_signal(L_SYNC + 36);
  }
  STAMP(13);
}

template <bool XT> DEV void group0_first(const Ptrs& p, u32 b, u32 lane, u32 w, u64& tag, u32* stamp) {
  const u32 xcd = b & 7, r = b >> 3, rt = (xcd & 1) * 8 + (r & 7), c4 = (xcd >> 1) * 4 + (r >> 3), g = c4 >> 1;
  const u32 mh = w >> 1, nh = w & 1, hh = lane >> 5, q = (lane & 15) >> 2;
  f32x16 acc[2][2] = {};
  bf16x8 xf[4][2] = {}, wf[4][2] = {};
  u32 xa = (mh * 64 + (lane & 31)) * 128 + ((hh ^ ((lane >> 1) & 7)) << 4);
  u32 wa = 16384 + (8 * hh + q) * 256 + (lane & 16) * 2 + (lane & 3) * 8 + ((XT ? (nh ^ q) : ((nh - q) & 3)) << 6), wb = wa ^ 128;
  u32 ca = L_CORRA + (nh * 32 + (lane & 31)) * 16;
  const u32x4 srd_ws = make_srd(p.ws, WS_END);
  u32x4 vw2 = steps4((lane >> 4) * 1024 + (lane & 3) * 16 + ((q ^ (lane >> 4)) << 6), 4096);
  auto wchunk = [&](int i) { dma4<0>(make_srd(p.w2, 1376 * 1024), vw2, (16 * (c4 >> 2) + 4 * i + w) * 16384 + (c4 & 3) * 256, L_WE + (4 * i + w) * 4096); };
  u32 ma0 = hh ? L_SYNC + 16 : L_MN + (mh * 64 + (lane & 31)) * 16, ma1 = hh ? L_SYNC + 16 : ma0 + 512;
  *LDSP(u32, L_SYNC + (lane & 15) * 4) = 0u;
  *LDSP(u32x4, L_CORRA + (w * 32 + (lane & 31)) * 16) = (u32x4){0u, 0u, 0u, 0u};
  asm volatile("" : "+v"(acc[0][0]), "+v"(acc[0][1]), "+v"(acc[1][0]), "+v"(acc[1][1]), "+v"(vw2));
  asm volatile("" : "+v"(xf[2][0]), "+v"(xf[2][1]), "+v"(wf[2][0]), "+v"(wf[2][1]), "+v"(xf[3][0]), "+v"(xf[3][1]), "+v"(wf[3][0]), "+v"(wf[3][1]),
               "+v"(xa), "+v"(wa), "+v"(wb), "+v"(ca), "+v"(ma0), "+v"(ma1));
  STAMP(1);
  __builtin_amdgcn_s_setprio(1);
  auto ldx = [&](int s, int h) { xf[s][h] = lds_b128(xa ^ (s << 5), h * 4096); sfence(); };
  auto ldw = [&](int s, int h) { wf[s][h] = lds_tr<1024>(h ? wb : wa, s * 4096); sfence(); };
  auto m1 = [&](int s, int i) {
    const int t = (i + 1) >> 1 & 1, u = i >> 1;
    acc[t][u] = XT ? mfma32(xf[s][t], wf[s][u], acc[t][u]) : mfma32(wf[s][u], xf[s][t], acc[t][u]);
    sfence();
  };
  barrier();
#pragma unroll 1
  for (u32 st = 0; st < 8; ++st) {
    m1(2, 0); ldw(0, 0); m1(2, 1); ldx(0, 0); ldx(0, 1); m1(2, 2); ldw(0, 1); m1(2, 3); ldw(1, 0);
    m1(3, 0); ldx(1, 0); ldx(1, 1); m1(3, 1); ldw(1, 1); m1(3, 2); ldw(2, 0); m1(3, 3); ldx(2, 0); ldx(2, 1);
    m1(0, 0); ldw(2, 1); m1(0, 1); ldw(3, 0); m1(0, 2); ldx(3, 0); ldx(3, 1); m1(0, 3); ldw(3, 1);
    m1(1, 0);
    xa = (xa + 32768) & 131071; wa = (wa + 32768) & 131071; wb = (wb + 32768) & 131071;
    asm volatile("" : "+v"(xa), "+v"(wa), "+v"(wb));
    sfence();
    m1(1, 1);
    m1(1, 2); m1(1, 3);
    barrier();
  }
  __builtin_amdgcn_s_setprio(2);
  f32x4 rl[8];
  m1(2, 0);
  const u32x2 tg = *LDSP(u32x2, L_SYNC + 40);
  wf[0][0] = lds_b128(ca); xf[0][0] = lds_b128(ma0); wf[0][1] = lds_b128(ca, 1024); xf[0][1] = lds_b128(ma1);
  sfence();
  m1(2, 3);
  if (XT) {
    const u32 ra = opaque_v(L_RL + (mh * 64 + hh * 4) * 4);
#pragma unroll
    for (int i = 0; i < 8; ++i) rl[i] = *LDSO(f32x4, ra, i * 32);
    sfence();
  }
  m1(3, 0);
  tag = ((u64)(u32)__builtin_amdgcn_readfirstlane(tg.y) << 32) | (u32)__builtin_amdgcn_readfirstlane(tg.x);
  sfence();
  m1(3, 3); m1(0, 0); m1(0, 3); m1(2, 1); m1(2, 2); m1(3, 1); m1(3, 2); m1(0, 1); m1(0, 2);
  STAMP(2);
  if constexpr (!XT) {
    __builtin_amdgcn_s_setprio(YPRIO);
#pragma unroll
    for (int k = 0; k < 8; ++k) {
      const int t = k >> 2, u = (k >> 1) & 1, s = k & 1;
      u32x4 o;
#pragma unroll
      for (int i = 0; i < 4; ++i) o[i] = relu2(pack2(acc[t][u][8 * s + 2 * i], acc[t][u][8 * s + 2 * i + 1]));
      store_dev(o, srd_ws, lane * 16, WS_A + ((rt * 4 + mh * 2 + t) * NSTEP + g * 8 + u * 4 + nh * 2 + s) * 1024);
    }
    STAMP(3);
    wchunk(2);
    wait_vm<4>();
    store_one((u32x4){(u32)tag, (u32)(tag >> 32), 0u, 0u}, srd_ws, WS_FLAGS + ((rt * 2 + mh) * 48 + g * 2 + nh) * 128);
    __builtin_amdgcn_s_setprio(0);
    STAMP(4);
    wchunk(3);
  } else {
    f32x16 kv = {}, zz = {};
    const bf16x8 ones = as_bf((u32x4){0x3f803f80u, 0x3f803f80u, 0x3f803f80u, 0x3f803f80u});
    const bool up = lane & 16;
    const u32 kvb = WS_KVP + (g * 4 + nh * 2) * KVH_B + (rt * 2 + mh) * 64;
    const u32 o0 = opaque_v((up ? KVH_B : 0) + (hh * 8 + q) * 2048 + (lane & 3) * 16);
    const u32 oz = opaque_v((lane & 14) ? OOB : (up ? KVH_B : 0) + 32768 + hh * 32 + (lane & 1) * 16);
#pragma unroll
    for (int k = 0; k < 4; ++k) {
      const int t = k >> 1, s = k & 1;
      float t2[8], v2[8];
#pragma unroll
      for (int i = 0; i < 8; ++i) t2[i] = mul1(acc[t][0][8 * s + i], rl[2 * k + (i >> 2)][i & 3]);
#pragma unroll
      for (int i = 0; i < 8; ++i) v2[i] = mul1(acc[t][1][8 * s + i], rl[2 * k + (i >> 2)][i & 3]);
      sfence();
      const u32x4 pv = {pack2(v2[0], v2[1]), pack2(v2[2], v2[3]), pack2(v2[4], v2[5]), pack2(v2[6], v2[7])};
      const u32x4 pk = phi8(t2);
      kv = mfma32(as_bf(pk), as_bf(pv), kv);
      zz = mfma32(as_bf(pk), ones, zz);
      sfence();
    }
    u32 ks[4], zs[4];
#pragma unroll
    for (int i = 0; i < 4; ++i) { const u32 lo = pack2(kv[2 * i], kv[2 * i + 1]), hi = pack2(kv[8 + 2 * i], kv[9 + 2 * i]); ks[i] = up ? hi : lo; }
    store_dev((u32x4){ks[0], (u32)tag, ks[1], (u32)(tag >> 32)}, srd_ws, o0, kvb);
    store_dev((u32x4){ks[2], (u32)tag, ks[3], (u32)(tag >> 32)}, srd_ws, o0, kvb + 8192);
#pragma unroll
    for (int i = 0; i < 4; ++i) { const u32 lo = pack2(zz[2 * i], zz[2 * i + 1]), hi = pack2(zz[8 + 2 * i], zz[9 + 2 * i]); zs[i] = up ? hi : lo; }
    const bool od = lane & 1;
    store_dev((u32x4){od ? zs[2] : zs[0], (u32)tag, od ? zs[3] : zs[1], (u32)(tag >> 32)}, srd_ws, oz, kvb);
    STAMP(3);
    __builtin_amdgcn_s_setprio(0);
    wchunk(2); wchunk(3);
  }
}

DEV void group0(const Ptrs& p, u32 b, u32 lane, u32 w, u32* stamp) {
  u64 tag;
  if (b & 64) group0_first<false>(p, b, lane, w, tag, stamp); else group0_first<true>(p, b, lane, w, tag, stamp);
  STAMP(5);
  b = opaque_s(b); lane = opaque_v(lane);
  const u32 rt = (b & 1) * 8 + ((b >> 3) & 7), kq = (b >> 1) & 3, ct = b >> 6, hh = lane >> 5, q = (lane & 15) >> 2;
  const __amdgpu_buffer_rsrc_t rws = __builtin_amdgcn_make_buffer_rsrc(p.ws, 0, WS_END, 0x00020000);
  const u32x4 srd_ws = make_srd(p.ws, WS_END);
  const char* fp = p.ws + WS_FLAGS + ((rt * 2 + (w >> 1)) * 48 + kq * 4) * 128;
  const u32 ab = WS_A + (rt * 4 + w) * NSTEP * 1024;
  u32 wl[4], we[4];
#pragma unroll
  for (int i = 0; i < 4; ++i) {
    wl[i] = opaque_v((4 * hh + q) * 256 + (lane & 16) * 2 + (lane & 3) * 8 + ((i ^ kq ^ q) << 6));
    we[i] = opaque_v(wl[i] + L_WE);
  }
  const float rlv = *LDSP(float, L_RL + (w * 32 + (lane & 31)) * 4);
  const u32 tok = rt * 128 + w * 32 + (lane & 31), oo = tok * 1024 + ct * 256 + kq * 64 + hh * 16;
  u32x4 xr0, xr1;
  const u32 rdy = opaque_v(L_SYNC + 48 + w * 4), cl = opaque_v(L_SYNC + 32);
  u32x4 af[30];
  const u32 l16 = opaque_v(lane * 16);
  auto lda = [&](int s) {
    const u32 so = s < 16 ? ab + (16 * kq + s) * 1024 : s < 22 ? ((s < 20 || kq < 3) ? ab + (48 + 6 * kq + s) * 1024 : OOB) : ab + (64 + 8 * kq + s) * 1024;
    asm volatile("buffer_load_dwordx4 %0, %1, %2, %3 offen sc1" : "=v"(af[s]) : "v"(l16), "s"(srd_ws), "s"(so) : "memory");
  };
  bf16x8 wf[3][4], kf[8];
  f32x16 acc[4] = {}, d[2];
  auto ldw = [&](int s, int i) { wf[s % 3][i] = lds_tr<2048>(s < 16 ? we[i] : wl[i], (s & 15) * 4096); sfence(); };
  auto peek = [&](u32 a, int o) { return __hip_atomic_load(LDSO(u32, a, o), __ATOMIC_RELAXED, __HIP_MEMORY_SCOPE_WORKGROUP); };
  auto pin = [&](f32x16& v) { asm volatile("" : "+v"(v)); sfence(); };
  const float sc = __builtin_amdgcn_rcpf(rlv);
  const u32x4 vw2 = steps4((lane >> 4) * 1024 + (lane & 3) * 16 + ((q ^ (lane >> 4)) << 6), 4096);
  u32 c0 = 0, c1 = 0;
  poll4(fp, tag, SPIN_CAP);
  STAMP(6);
#pragma unroll
  for (int s = 0; s < 8; ++s) lda(s);
  lds_wait(L_SYNC, 8);
#pragma unroll
  for (int i = 0; i < 6; ++i) ldw(i / 3, 1 + i % 3);
  wait_vm<8>();
  lds_signal(L_SYNC + 4);
#pragma unroll
  for (int s = 8; s < 16; ++s) lda(s);
  STAMP(7);
  static_for<30>([&](auto S_) {
    constexpr int s = S_;
    if (s == 0) dma4<0>(make_srd(p.w2, 1376 * 1024), vw2, (64 + 6 * kq + w) * 16384 + ct * 256, w * 4096);
    if (s == 2) dma4<0>(make_srd(p.w2, 1376 * 1024), vw2, w < 2 ? (68 + 6 * kq + w) * 16384 + ct * 256 : OOB, (w < 2 ? 4 + w : 14) * 4096);
    if (s == 4) dma4<0>(make_srd(p.wo, 512 * 1024), vw2, (8 * kq + w) * 16384 + ct * 256, (6 + w) * 4096);
    if (s == 6) dma4<0>(make_srd(p.wo, 512 * 1024), vw2, (8 * kq + 4 + w) * 16384 + ct * 256, (10 + w) * 4096);
    if (s == SL - 2 || s == SO - 2) { c0 = peek(rdy, 0); sfence(); }
    if (s == 4) { c1 = peek(cl, -28); sfence(); }
    if (s == 6 && (u32)__builtin_amdgcn_readfirstlane(c1) < 4) lds_wait(L_SYNC + 4, 4);
    if (s == SL) {
      if ((u32)__builtin_amdgcn_readfirstlane(c0) < 1) lds_wait(rdy, 1);
      STAMP(8);
#pragma unroll
      for (int k = 16; k < 22; ++k) lda(k);
    }
    if (s == 12) { wait_vm<6>(); lds_signal(cl); }
    if (s == 14) lds_wait(cl, 4);
    if (s == 17) { c1 = peek(cl, 4); sfence(); }
    if (s == SO) {
      if ((u32)__builtin_amdgcn_readfirstlane(c0) < 2) lds_wait(rdy, 2);
      STAMP(9);
#pragma unroll
      for (int k = 22; k < 30; ++k) lda(k);
    }
    if (s == 19) {
      if ((u32)__builtin_amdgcn_readfirstlane(c1) < 4) lds_wait(cl + 4, 4);
#pragma unroll
      for (int h = 0; h < 4; ++h) kf[h] = lds_b128(L_KVF + lane * 16, h * 1024);
      sfence();
    }
    if constexpr (s >= 21 && s < 25) { kf[s - 17] = lds_b128(L_KVF + lane * 16, (s - 17) * 1024); sfence(); }
    constexpr int h = s - 21;
    constexpr bool at = h >= 0 && h < 8;
    float f, m[8];
    if constexpr (s < 22) need<s, s>(af[s]);
    if constexpr (h >= -1 && h < 7) { need<s, s + 2>(af[s + 2]); d[(h + 1) & 1] = mfma32(kf[h + 1], as_bf(af[s + 2]), (f32x16){}); pin(d[(h + 1) & 1]); }
    if constexpr (at) { f = mul1(sc, __builtin_amdgcn_rcpf(d[h & 1][8] + 1e-6f)); sfence(); }
    acc[1] = mfma32(wf[s % 3][1], as_bf(af[s]), acc[1]); pin(acc[1]);
    if (s + 2 < 30) ldw(s + 2, 1);
    if constexpr (at) { for (int i = 0; i < 4; ++i) m[i] = mul1(d[h & 1][i], f); sfence(); }
    acc[2] = mfma32(wf[s % 3][2], as_bf(af[s]), acc[2]); pin(acc[2]);
    if (s + 2 < 30) ldw(s + 2, 2);
    if constexpr (at) { for (int i = 4; i < 8; ++i) m[i] = mul1(d[h & 1][i], f); keep(d[h & 1]); sfence(); }
    acc[3] = mfma32(wf[s % 3][3], as_bf(af[s]), acc[3]); pin(acc[3]);
    if (s + 2 < 30) ldw(s + 2, 3);
    if constexpr (at) { af[s + 1] = (u32x4){pack2(m[0], m[1]), pack2(m[2], m[3]), pack2(m[4], m[5]), pack2(m[6], m[7])}; keep(af[s + 1]); sfence(); }
  });
  STAMP(10);
  bf16x8 w0[6];
  auto ld0 = [&](int s) { w0[s % 6] = lds_tr<2048>(s < 16 ? we[0] : wl[0], (s & 15) * 4096); sfence(); };
#pragma unroll
  for (int s = 0; s < 5; ++s) ld0(s);
  const u32 xb = WS_X + ((rt * 4 + ct) * 4 + w) * 32768;
#pragma unroll
  for (int i = 1; i < 4; ++i)
#pragma unroll
    for (int k = 0; k < 2; ++k)
      store_dev((u32x4){pack2(acc[i][8 * k], acc[i][8 * k + 1]), pack2(acc[i][8 * k + 2], acc[i][8 * k + 3]), pack2(acc[i][8 * k + 4], acc[i][8 * k + 5]),
                        pack2(acc[i][8 * k + 6], acc[i][8 * k + 7])}, srd_ws, lane * 16, xb + ((kq ^ i) * 4 + kq) * 2048 + k * 1024);
  xr0 = *(const GAS u32x4*)(p.x + oo); xr1 = *(const GAS u32x4*)(p.x + oo + 32);
  sfence();
#pragma unroll
  for (int s = 0; s < 30; ++s) {
    acc[0] = mfma32(w0[s % 6], as_bf(af[s]), acc[0]); pin(acc[0]);
    if (s + 5 < 30) ld0(s + 5);
  }
  wait_vm<2>();
  const u32 xf = WS_XFL + ((rt * 4 + ct) * 4 + w) * 512;
  store_one((u32x4){(u32)tag, (u32)(tag >> 32), 0u, 0u}, srd_ws, xf + kq * 128);
  STAMP(11);
  poll4(p.ws + xf, tag, SPIN_CAP);
  STAMP(12);
  u32x4 pp[3][2];
#pragma unroll
  for (int i = 0; i < 3; ++i) {
    pp[i][0] = __builtin_amdgcn_raw_buffer_load_b128(rws, lane * 16, xb + (kq * 4 + (kq ^ (i + 1))) * 2048, 16);
    pp[i][1] = __builtin_amdgcn_raw_buffer_load_b128(rws, lane * 16 + 1024, xb + (kq * 4 + (kq ^ (i + 1))) * 2048, 16);
  }
  sfence();
  float v[16];
#pragma unroll
  for (int e = 0; e < 16; ++e) v[e] = acc[0][e];
#pragma unroll
  for (int i = 0; i < 3; ++i)
#pragma unroll
    for (int e = 0; e < 8; ++e) { v[2 * e] += lo_f(pp[i][e >> 2][e & 3]); v[2 * e + 1] += hi_f(pp[i][e >> 2][e & 3]); }
  const float rs = rlv * LN2;
#pragma unroll
  for (int k = 0; k < 2; ++k) {
#pragma unroll
    for (int e = 0; e < 4; ++e) {
      auto sw = __builtin_amdgcn_permlane32_swap(__float_as_uint(v[8 * k + e]), __float_as_uint(v[8 * k + 4 + e]), false, false);
      v[8 * k + e] = __uint_as_float(sw[0]); v[8 * k + 4 + e] = __uint_as_float(sw[1]);
    }
    const u32x4 xr = k ? xr1 : xr0;
    u32x4 o;
#pragma unroll
    for (int e = 0; e < 4; ++e) o[e] = pack2(__builtin_fmaf(v[8 * k + 2 * e], rs, lo_f(xr[e])), __builtin_fmaf(v[8 * k + 2 * e + 1], rs, hi_f(xr[e])));
    *(GAS u32x4*)(p.out + oo + k * 32) = o;
  }
  if (b == 0 && w == 0) store_one((u32x4){(u32)tag, (u32)(tag >> 32), 0u, 0u}, srd_ws, WS_EPOCH);
  STAMP(13);
}

__global__ __launch_bounds__(512, 2) void fused_block(const char* __restrict__ x, const char* __restrict__ wqkv, const char* __restrict__ w1,
                                                      char* ws, const char* __restrict__ w2, const char* __restrict__ wo, char* __restrict__ out) {
  __shared__ __attribute__((aligned(16))) char lds_all[L_TOTAL];
  u32 stamp[32] = {};
  u64 pc;
  asm volatile("s_getpc_b64 %0" : "=s"(pc) : "s"((u32)(size_t)lds_all));
  STAMP(0);
  const u32 w = __builtin_amdgcn_readfirstlane(threadIdx.x >> 6);
  {
    u32 d0, d1, d2, d3;
    const u64 cp = (pc & ~127ull) + min((blockIdx.x >> 3) * 8 + w * 2, CODE_LINES - 2) * 128;
    asm volatile("s_cmp_gt_u32 %5, 3\n\ts_cbranch_scc1 1f\n\ts_load_dword %0, %4, 0\n\ts_load_dword %1, %4, 64\n\ts_load_dword %2, %4, 128\n\t"
                 "s_load_dword %3, %4, 192\n\ts_waitcnt lgkmcnt(0)\n1:"
                 : "=&s"(d0), "=&s"(d1), "=&s"(d2), "=&s"(d3) : "s"(cp), "s"(w) : "memory", "scc");
  }
  asm volatile("" ::"s"(x), "s"(wqkv), "s"(w1), "s"(ws), "s"(w2), "s"(wo), "s"(out));
  const Ptrs p = {x, wqkv, w1, ws, w2, wo, out};
  if (w < 4) group0(p, opaque_s(blockIdx.x), opaque_v(threadIdx.x) & 63, w, stamp);
  else group1(p, blockIdx.x, threadIdx.x & 63, w, stamp);
  const u32 b = opaque_s(blockIdx.x), lane = opaque_v(threadIdx.x) & 63;
  { u32 lim = 4000; if (stamp[1] && (w >> 2) == PGRP && ((b >> 6) & 1) == PTYPE) lim += 10 * (stamp[1] - stamp[0]);
    while ((u32)__builtin_amdgcn_s_memrealtime() - stamp[0] < lim) __builtin_amdgcn_s_sleep(2); }
}

extern "C" void kernel_launch(void* const* d_in, const int* in_sizes, int n_in, void* d_out, int out_size, void* d_ws, size_t ws_size,
                              hipStream_t stream) {
  fused_block<<<256, 512, 0, stream>>>((const char*)d_in[0], (const char*)d_in[1], (const char*)d_in[4], (char*)d_ws, (const char*)d_in[5],
                                       (const char*)d_in[2], (char*)d_out);
}
```

```cpp
#define TOUCH 1
#define ROT1 1
#include <hip/hip_runtime.h>
#include <hip/hip_bf16.h>
#include <type_traits>

typedef unsigned u32;
typedef unsigned long long u64;
typedef __bf16 bf16x8 __attribute__((ext_vector_type(8)));
typedef __bf16 bf16x2 __attribute__((ext_vector_type(2)));
typedef short s16x4 __attribute__((ext_vector_type(4)));
typedef short s16x2 __attribute__((ext_vector_type(2)));
typedef float f32x16 __attribute__((ext_vector_type(16)));
typedef float f32x4 __attribute__((ext_vector_type(4)));
typedef float f32x2 __attribute__((ext_vector_type(2)));
typedef u32 u32x4 __attribute__((ext_vector_type(4)));
typedef u32 u32x2 __attribute__((ext_vector_type(2)));
#define LAS __attribute__((address_space(3)))
#define GAS __attribute__((address_space(1)))
#define DEV __device__ __forceinline__
#define RLX_AGENT __ATOMIC_RELAXED, __HIP_MEMORY_SCOPE_AGENT
#define LDSP(T, a) ((LAS T*)(size_t)(a))

#define PF 0
#define QWIN 3
#define AWIN 5
#define DLY 0
#define SLP 2
#define PSC "sc1"
#define SL 10
#define SO 16
#define YPRIO 0
#define PRIO2 1

constexpr u32 WS_EPOCH = 0;
constexpr u32 WS_STATS = 4096;
constexpr u32 WS_FLAGS = WS_STATS + 16384;
constexpr u32 WS_XFL = WS_FLAGS + 32 * 48 * 128;
constexpr u32 WS_KVF = WS_XFL + 1024 * 128;
constexpr u32 WS_KVZ = WS_KVF + 32768;
constexpr u32 WS_KVP = WS_KVZ + 2048;
constexpr u32 KVH_B = 34816;
constexpr u32 WS_A = 2u << 20;
constexpr u32 NSTEP = 118;
constexpr u32 WS_X = WS_A + 64 * NSTEP * 1024;
constexpr u32 WS_END = WS_X + 4096 * 2048;
constexpr u32 WS_STAMP = 32u << 20;
static_assert(WS_KVP + 32 * KVH_B <= WS_A, "ws map");
constexpr u32 OOB = 0x40000000u;

constexpr u32 L_WE = 65536;
constexpr u32 L_SCR = 131072;
constexpr u32 L_CORRA = L_SCR;
constexpr u32 L_MN = L_SCR + 2048;
constexpr u32 L_CORRB = L_SCR + 4096;
constexpr u32 L_GRAN = L_SCR + 5120;
constexpr u32 L_RL = L_SCR + 6144;
constexpr u32 L_SYNC = L_SCR + 6656;
constexpr u32 L_XCH = L_SCR + 8192;
constexpr u32 L_KVF = L_SCR + 16384;
constexpr u32 L_FINE = L_SCR + 24576;
constexpr u32 L_TOTAL = 163840;

constexpr u32 CODE_LINES = 176;
constexpr u32 SPIN_CAP = 1u << 18;
constexpr float LOG2E = 1.4426950408889634f, LN2 = 0.6931471805599453f;

DEV void sfence() { __builtin_amdgcn_sched_barrier(0); }
template <int N> DEV void wait_vm() { asm volatile("s_waitcnt vmcnt(%0)" ::"i"(N) : "memory"); }
DEV void wait_lgkm() { sfence(); __builtin_amdgcn_s_waitcnt(0xC07F); asm volatile("" ::: "memory"); sfence(); }
DEV void barrier() { sfence(); __builtin_amdgcn_s_waitcnt(0xC07F); asm volatile("s_barrier" ::: "memory"); sfence(); }

DEV u32x4 make_srd(const void* p, u32 bytes) {
  u64 a = (u64)p;
  return (u32x4){(u32)a, (u32)(a >> 32) & 0xffffu, bytes, 0x00020000u};
}
template <int POL> DEV void dma(u32x4 srd, u32 voff, u32 soff, u32 ldsaddr) {
  if (POL == 1)
    asm volatile("s_mov_b32 m0, %3\n\ts_nop 0\n\tbuffer_load_dwordx4 %0, %1, %2 offen nt lds" ::"v"(voff), "s"(srd), "s"(soff), "s"(ldsaddr) : "memory");
  else if (POL == 2)
    asm volatile("s_mov_b32 m0, %3\n\ts_nop 0\n\tbuffer_load_dwordx4 %0, %1, %2 offen sc1 lds" ::"v"(voff), "s"(srd), "s"(soff), "s"(ldsaddr) : "memory");
  else
    asm volatile("s_mov_b32 m0, %3\n\ts_nop 0\n\tbuffer_load_dwordx4 %0, %1, %2 offen lds" ::"v"(voff), "s"(srd), "s"(soff), "s"(ldsaddr) : "memory");
}
template <int POL> DEV void dma4(u32x4 srd, u32x4 v, u32 soff, u32 ldsaddr) {
  if (POL == 1)
    asm volatile("s_mov_b32 m0, %6\n\ts_nop 0\n\tbuffer_load_dwordx4 %0, %4, %5 offen nt lds\n\tbuffer_load_dwordx4 %1, %4, %5 offen offset:1024 nt lds\n\t"
                 "buffer_load_dwordx4 %2, %4, %5 offen offset:2048 nt lds\n\tbuffer_load_dwordx4 %3, %4, %5 offen offset:3072 nt lds"
                 ::"v"(v.x), "v"(v.y), "v"(v.z), "v"(v.w), "s"(srd), "s"(soff), "s"(ldsaddr) : "memory");
  else
    asm volatile("s_mov_b32 m0, %6\n\ts_nop 0\n\tbuffer_load_dwordx4 %0, %4, %5 offen lds\n\tbuffer_load_dwordx4 %1, %4, %5 offen offset:1024 lds\n\t"
                 "buffer_load_dwordx4 %2, %4, %5 offen offset:2048 lds\n\tbuffer_load_dwordx4 %3, %4, %5 offen offset:3072 lds"
                 ::"v"(v.x), "v"(v.y), "v"(v.z), "v"(v.w), "s"(srd), "s"(soff), "s"(ldsaddr) : "memory");
}
DEV void touch64(u32x4 srd, u32 voff, u32 soff, u32 ldsaddr) {
  asm volatile("s_mov_b32 m0, %3\n\ts_nop 0\n\tbuffer_load_dword %0, %1, %2 offen lds" ::"v"(voff), "s"(srd), "s"(soff), "s"(ldsaddr) : "memory");
}
DEV u32x4 steps4(u32 v, u32 step) { step -= 1024; return (u32x4){v, v + step, v + 2 * step, v + 3 * step}; }
DEV void store_one(u32x4 v, u32x4 srd, u32 soff) {
  asm volatile("s_mov_b64 exec, 1\n\tbuffer_store_dwordx4 %0, off, %1, %2 sc1\n\ts_mov_b64 exec, -1\n\ts_nop 0" ::"v"(v), "s"(srd), "s"(soff) : "memory");
}
DEV void store_dev(u32x4 v, u32x4 srd, u32 voff, u32 soff) {
  asm volatile("buffer_store_dwordx4 %0, %1, %2, %3 offen sc1\n\ts_nop 1" ::"v"(v), "v"(voff), "s"(srd), "s"(soff) : "memory");
}
#define LDSO(T, a, o) ((LAS T*)(LDSP(char, a) + (o)))
DEV bf16x8 lds_b128(u32 a, int o = 0) { return *LDSO(bf16x8, a, o); }
template <int D> DEV bf16x8 lds_tr(u32 a, int o) {
  s16x4 lo = __builtin_amdgcn_ds_read_tr16_b64_v4i16(LDSO(s16x4, a, o));
  s16x4 hi = __builtin_amdgcn_ds_read_tr16_b64_v4i16(LDSO(s16x4, a, o + D));
  return __builtin_bit_cast(bf16x8, __builtin_shufflevector(lo, hi, 0, 1, 2, 3, 4, 5, 6, 7));
}
DEV f32x16 mfma32(bf16x8 a, bf16x8 b, f32x16 c) { return __builtin_amdgcn_mfma_f32_32x32x16_bf16(a, b, c, 0, 0, 0); }
DEV f32x4 mfma16(bf16x8 a, bf16x8 b, f32x4 c) { return __builtin_amdgcn_mfma_f32_16x16x32_bf16(a, b, c, 0, 0, 0); }
DEV bf16x8 as_bf(u32x4 v) { return __builtin_bit_cast(bf16x8, v); }
DEV u32 pack2(float a, float b) { return __builtin_bit_cast(u32, __builtin_convertvector((f32x2){a, b}, bf16x2)); }
DEV u32 relu2(u32 v) { return __builtin_bit_cast(u32, __builtin_elementwise_max(__builtin_bit_cast(s16x2, v), (s16x2){0, 0})); }
DEV float lo_f(u32 v) { return __uint_as_float(v << 16); }
DEV float hi_f(u32 v) { return __uint_as_float(v & 0xffff0000u); }
DEV float rbf(float f) { return lo_f(pack2(f, 0.f)); }
DEV u32 split_hi_lo(float c) { float h = rbf(c); return pack2(h, c - h); }
DEV float mul1(float a, float b) { float r = a * b; asm("" : "+v"(r)); return r; }
template <int CTRL> DEV float dpp_add(float v) {
  return v + __uint_as_float(__builtin_amdgcn_update_dpp(0u, __float_as_uint(v), CTRL, 0xf, 0xf, true));
}
DEV float swap16_add(float v) {
  auto r = __builtin_amdgcn_permlane16_swap(__float_as_uint(v), __float_as_uint(v), false, false);
  return __uint_as_float(r[0]) + __uint_as_float(r[1]);
}
DEV float swap32_add(float v) {
  auto r = __builtin_amdgcn_permlane32_swap(__float_as_uint(v), __float_as_uint(v), false, false);
  return __uint_as_float(r[0]) + __uint_as_float(r[1]);
}
DEV float sum32(float v) {
  v = dpp_add<0xB1>(v); v = dpp_add<0x4E>(v); v = dpp_add<0x141>(v); v = dpp_add<0x140>(v);
  return swap16_add(v);
}
DEV u32x4 phi8(const float (&t2)[8]) {
  float e[8], m[8];
#pragma unroll
  for (int i = 0; i < 8; ++i) e[i] = __builtin_amdgcn_fmed3f(__builtin_amdgcn_exp2f(t2[i]), 0.f, 1.f);
  sfence();
#pragma unroll
  for (int i = 0; i < 8; ++i) asm("v_max_f32 %0, %1, %2" : "=v"(m[i]) : "s"(1e-6f * LOG2E), "v"(t2[i]));
  sfence();
#pragma unroll
  for (int i = 0; i < 8; ++i) { e[i] = __builtin_fmaf(m[i], LN2, e[i]); asm("" : "+v"(e[i])); }
  sfence();
  return (u32x4){pack2(e[0], e[1]), pack2(e[2], e[3]), pack2(e[4], e[5]), pack2(e[6], e[7])};
}
DEV void lds_signal(u32 addr) {
  asm volatile("s_mov_b64 exec, 1\n\tds_add_u32 %0, %1\n\ts_mov_b64 exec, -1" ::"v"(addr), "v"(1u) : "memory");
}
DEV void lds_wait(u32 addr, u32 waves) {
  u32 v, s, n = SPIN_CAP;
  asm volatile("1:\n\tds_read_b32 %0, %3\n\ts_waitcnt lgkmcnt(0)\n\tv_readfirstlane_b32 %1, %0\n\ts_sub_u32 %2, %2, 1\n\ts_cbranch_scc1 2f\n\t"
               "s_cmp_lt_u32 %1, %4\n\ts_cbranch_scc1 1b\n2:"
               : "=&v"(v), "=&s"(s), "+s"(n) : "v"(addr), "s"(waves) : "memory", "scc");
}
DEV u64 poll4(const char* p, u64 tag, u32 n) {
  u64 f0, f1, f2, f3;
  asm volatile("1:\n\ts_load_dwordx2 %0, %5, 0 glc\n\ts_load_dwordx2 %1, %5, 128 glc\n\ts_load_dwordx2 %2, %5, 256 glc\n\t"
               "s_load_dwordx2 %3, %5, 384 glc\n\ts_waitcnt lgkmcnt(0)\n\t"
               "s_xor_b64 %0, %0, %6\n\ts_xor_b64 %1, %1, %6\n\ts_xor_b64 %2, %2, %6\n\ts_xor_b64 %3, %3, %6\n\t"
               "s_or_b64 %0, %0, %1\n\ts_or_b64 %2, %2, %3\n\ts_or_b64 %0, %0, %2\n\ts_cbranch_scc0 2f\n\t"
               "s_sub_u32 %4, %4, 1\n\ts_cbranch_scc0 1b\n2:"
               : "=&s"(f0), "=&s"(f1), "=&s"(f2), "=&s"(f3), "+s"(n) : "s"(p), "s"(tag) : "memory", "scc");
  return f0;
}
template <int D> DEV void poll2(const char* p, u64 tag, u64 mask) {
  u64 f0, f1; u32 n = SPIN_CAP;
  asm volatile("1:\n\ts_load_dwordx2 %0, %3, 0 glc\n\ts_load_dwordx2 %1, %3, %6 glc\n\ts_waitcnt lgkmcnt(0)\n\t"
               "s_xor_b64 %0, %0, %4\n\ts_xor_b64 %1, %1, %4\n\ts_or_b64 %0, %0, %1\n\ts_and_b64 %0, %0, %5\n\ts_cbranch_scc0 2f\n\t"
               "s_sub_u32 %2, %2, 1\n\ts_cbranch_scc0 1b\n2:"
               : "=&s"(f0), "=&s"(f1), "+s"(n) : "s"(p), "s"(tag), "s"(mask), "i"(D) : "memory", "scc");
}
DEV u32x4 load_dev_wait(u32x4 srd, u32 voff, u32 soff) {
  u32x4 v;
  asm volatile("buffer_load_dwordx4 %0, %1, %2, %3 offen sc1\n\ts_waitcnt vmcnt(0)" : "=&v"(v) : "v"(voff), "s"(srd), "s"(soff) : "memory");
  return v;
}
template <int N, int I = 0, class F> DEV void static_for(F&& f) {
  if constexpr (I < N) { f(std::integral_constant<int, I>{}); static_for<N, I + 1>(f); }
}
template <int T, int K> DEV void need(u32x4& v) {
  constexpr int issued = 16 + 4 * ((T < 6 ? T : 6) / 2 + 1) + (T >= SL ? 6 : 0) + (T >= SO ? 8 : 0), idx = K < 16 ? K : 16 + K;
  asm volatile("s_waitcnt vmcnt(%1)" : "+v"(v) : "i"(issued - idx - 1));
}
template <class T> DEV void keep(T& v) { asm volatile("" : "+v"(v)); }
template <class T> DEV T opaque_v(T v) { asm volatile("" : "+v"(v)); return v; }
template <class T> DEV T opaque_s(T v) { asm volatile("" : "+s"(v)); return v; }

#define STAMP(i)
#define FSTAMP(w, i)

struct Ptrs { const char *x, *wqkv, *w1; char* ws; const char *w2, *wo; char* out; };

DEV void group1(const Ptrs& p, u32 b, u32 lane, u32 w, u32* stamp) {
  const u32 xcd = b & 7, r = b >> 3, j = w & 3;
  const u32 rt = (xcd & 1) * 8 + (r & 7);
  const u32 rot = ROT1 == 0 ? 0 : ROT1 == 1 ? (r & 7) : (r & 7) + 2 * (r >> 3);
  const u32x4 srd_x = make_srd(p.x, 2048 * 1024);
  const u32 vx = (lane >> 3) * 1024 + (((lane & 7) ^ (lane >> 4)) << 4);
  const u32x4 vx4 = {vx, (vx ^ 64) + 7168, vx + 14336, (vx ^ 64) + 21504};
  auto issue_x = [&](int st) { dma4<0>(srd_x, vx4, (rt * 128 + 32 * j) * 1024 + ((st + rot) & 7) * 128, (st & 3) * 32768 + j * 4096); };
  issue_x(0);
  STAMP(10);
  sfence();
  const u32 c4 = (xcd >> 1) * 4 + (r >> 3), g = c4 >> 1;
  const bool xt = (c4 & 1) == 0;
  const u32 hh = lane >> 5, q = (lane & 15) >> 2;
  const u32 wst = xt ? 3072 : 2752;
  const u32x4 srd_w = make_srd(xt ? p.wqkv : p.w1, 512 * wst);
  const u32 ym = xt ? 0u : ~0u, l4 = lane >> 4;
  const u32 blk = ((q ^ l4) + (((q & l4) << 1) & ym)) & 3;
  const u32x4 vwa4 = steps4(l4 * wst + (lane & 3) * 16 + (blk & 1) * 64 + (blk >> 1) * (xt ? 1024 : 128) + (xt ? 1024 + g * 128 : g * 256), 4 * wst);
  auto issue_w = [&](int st) { dma4<0>(srd_w, vwa4, (((st + rot) & 7) * 64 + 16 * j) * wst, (st & 3) * 32768 + 16384 + j * 4096); };
  issue_w(0);
  const u32 row = rt * 128 + c4 * 8 + 2 * j;
  u32x4 a0, a1;
  asm volatile("global_load_dwordx4 %0, %2, %3\n\tglobal_load_dwordx4 %1, %2, %3 offset:512"
               : "=&v"(a0), "=&v"(a1) : "v"((row + hh) * 1024 + (lane & 31) * 16), "s"(p.x) : "memory");
  STAMP(11);
  issue_x(1); issue_w(1);
  sfence();
  u64 tag;
  asm volatile("s_load_dwordx2 %0, %1, 0\n\ts_waitcnt lgkmcnt(0)" : "=&s"(tag) : "s"(p.ws) : "memory");
  tag += 0x9E3779B97F4A7C15ull;
  const u32 nl = (xt || (g < 6 && !(g & 1))) ? 2 : 1, lstep = xt ? 86 + g * 4 : 64 + (g >> 1) * 6 + ((g & 1) ? (g < 6 ? 4 : 2) : 0);
  const u32x4 vwb4 = steps4((lane >> 3) * wst + ((((lane & 7) ^ ((lane >> 2) & 4)) << 4) & (nl == 2 ? 127 : 48)) + (xt ? g * 128 : lstep * 32), 8 * wst);
  auto issue = [&](int st, int part) {
    if (st < 8) { if (part) issue_w(st); else issue_x(st); }
    else dma4<0>(srd_w, vwb4, ((((st - 8) * 4 + j + rot) & 7) * 64 + 32 * part) * wst, (st - 8) * 32768 + j * 8192 + part * 4096);
  };
  const u32x4 srd_ws = make_srd(p.ws, WS_END);
  const u32 xa = (j * 32 + (lane & 31)) * 128 + ((hh ^ ((lane >> 1) & 7)) << 4);
  const u32 selv = (((lane >> 4) ^ (lane >> 2)) & 1) ? 0u : 0x3f803f80u;
  const bf16x8 sel = as_bf((u32x4){selv, selv, selv, selv});
  const u32 wa = 16384 + (8 * hh + q) * 256 + (lane & 16) * 2 + (lane & 3) * 8 + ((xt ? (j ^ q) : ((j - q) & 3)) << 6);
  f32x4 cs = {};
  bf16x8 xf[32];
  u32 mn;
  float rl;
#pragma unroll
  for (int st = 0; st < 8; ++st) {
    if (st == 1) wait_vm<9>(); else wait_vm<8>();
    barrier();
    if (st == 0) STAMP(1);
    const u32 slot = (st & 3) * 32768;
    if (st == 5) dma<2>(srd_ws, lane * 16, j == 0 ? WS_STATS + rt * 1024 : OOB, j == 0 ? L_GRAN : L_XCH);
    issue(st + 2, 0);
    sfence();
    u32x4 gr;
    if (st == 7) gr = *LDSP(u32x4, L_GRAN + (j * 16 + ((lane & 31) >> 1)) * 16);
    bf16x8 cw[4];
#pragma unroll
    for (int s = 0; s < 4; ++s) cw[s] = lds_tr<1024>(wa + (slot & 65536), (slot & 32768) + s * 4096);
#pragma unroll
    for (int s = 0; s < 4; ++s) xf[st * 4 + s] = lds_b128((xa ^ (s << 5)) + (slot & 65536), slot & 32768);
    sfence();
    issue(st + 2, 1);
    sfence();
    if (st == 0) {
      asm volatile("" : "+v"(a0), "+v"(a1));
    float v[16];
#pragma unroll
    for (int i = 0; i < 4; ++i) { v[2 * i] = lo_f(a0[i]); v[2 * i + 1] = hi_f(a0[i]); v[8 + 2 * i] = lo_f(a1[i]); v[9 + 2 * i] = hi_f(a1[i]); }
    float s[4] = {};
#pragma unroll
    for (int i = 0; i < 16; ++i) s[i & 3] += v[i];
    const float mean = sum32((s[0] + s[1]) + (s[2] + s[3])) * (1.f / 512.f);
    float qq[4] = {};
#pragma unroll
    for (int i = 0; i < 16; ++i) { float d = v[i] - mean; qq[i & 3] = __builtin_fmaf(d, d, qq[i & 3]); }
    const float var = sum32((qq[0] + qq[1]) + (qq[2] + qq[3])) * (1.f / 512.f);
    const u32 pm = pack2(mean, __builtin_amdgcn_sqrtf(rbf(rbf(var) + 1e-5f)));
    const u32 p1 = __builtin_amdgcn_readlane(pm, 32);
    store_one(opaque_v((u32x4){pm, (u32)tag, p1, (u32)(tag >> 32)}), srd_ws, WS_STATS + row * 8);
      *LDSP(u32x2, L_SYNC + 40) = (u32x2){(u32)tag, (u32)(tag >> 32)};
    }
    if (st == 7) {
#pragma unroll 1
      for (u32 i = 0; i < SPIN_CAP; ++i) {
        if (__all(gr.y == (u32)tag && gr.w == (u32)(tag >> 32))) break;
        gr = load_dev_wait(srd_ws, (j * 16 + ((lane & 31) >> 1)) * 16, WS_STATS + rt * 1024);
      }
      const u32 ms = (lane & 1) ? gr.z : gr.x;
      mn = ((ms & 0xffffu) ^ 0x8000u) * 0x10001u;
      rl = LOG2E * __builtin_amdgcn_rcpf(hi_f(ms));
      *LDSP(u32x4, L_MN + (j * 32 + (lane & 31)) * 16) = (u32x4){mn, mn, mn, mn};
      *LDSP(float, L_RL + (j * 32 + (lane & 31)) * 4) = rl;
      mn = hh ? 0u : mn;
    }
    sfence();
#pragma unroll
    for (int s = 0; s < 4; ++s) cs = mfma16(sel, cw[s], cs);
    asm volatile("" : "+v"(cs));
    sfence();
  }
  *LDSP(u32, L_CORRA + (j * 32 + (lane & 31)) * 16) = split_hi_lo(cs[0]);
  const u32 kq = c4 >> 2, cb = (c4 & 3) * 256;
  const u32x4 vw2 = steps4(l4 * 1024 + (lane & 3) * 16 + ((q ^ l4) << 6), 4096);
  const u32 wb0 = opaque_v(hh * 1024 + q * 128 + (lane & 16) * 2 + (lane & 3) * 8 + ((q & 2) << 5)), wb1 = opaque_v(wb0 ^ 64);
  const u32 wc0 = opaque_v(wb0 + j * 8192), wc1 = opaque_v(wb1 + j * 8192);
  wait_vm<8>();
  __builtin_amdgcn_s_setprio(1);
  barrier();
  STAMP(2);
  dma4<0>(make_srd(p.w2, 1376 * 1024), vw2, (16 * kq + j) * 16384 + cb, L_WE + j * 4096);
  dma4<0>(make_srd(p.w2, 1376 * 1024), vw2, (16 * kq + 4 + j) * 16384 + cb, L_WE + (4 + j) * 4096);
  f32x16 acc[2] = {};
  {
    bf16x8 wf[4][2], cw[3];
    f32x4 c0 = {}, c1 = {};
    u32 cnt = 0;
    auto ldw = [&](int s, int u) { wf[s & 3][u] = lds_tr<512>(u ? wb1 : wb0, s * 2048); sfence(); };
#pragma unroll
    for (int s = 0; s < 3; ++s) { ldw(s, 0); ldw(s, 1); }
#pragma unroll
    for (int s = 0; s < 32; ++s) {
      const int cr = s < 8 ? s : (s >= 14 && s < 22 ? s - 6 : -1), cm = (s >= 2 && s < 10) ? s - 2 : (s >= 16 && s < 24 ? s - 8 : -1);
      acc[0] = mfma32(wf[s & 3][0], xf[s], acc[0]); sfence();
      if (s == 12 && cnt < 4) lds_wait(L_SYNC, 4);
      if (s == 31 && cnt < 8) lds_wait(L_SYNC, 8);
      if (s + 3 < 32) ldw(s + 3, 0);
      if (s == 31) { wf[0][0] = lds_b128(L_CORRB + (lane & 31) * 16); wf[0][1] = lds_b128(L_CORRB + (lane & 31) * 16, 512); sfence(); }
      if (cr >= 0) { cw[cr % 3] = lds_tr<512>((cr & 1) ? wc1 : wc0, (cr >> 3) * 32768 + ((cr >> 1) & 3) * 2048); sfence(); }
      acc[1] = mfma32(wf[s & 3][1], xf[s], acc[1]); sfence();
      if (s + 3 < 32) ldw(s + 3, 1);
      if (cm >= 0) { if (cm & 1) c1 = mfma16(sel, cw[cm % 3], c1); else c0 = mfma16(sel, cw[cm % 3], c0); sfence(); }
      if (s == 7) { wait_vm<8>(); lds_signal(L_SYNC); }
      if (s == 9 || s == 29) cnt = __hip_atomic_load(LDSP(u32, L_SYNC), __ATOMIC_RELAXED, __HIP_MEMORY_SCOPE_WORKGROUP);
      if (s == 11 || s == 30) cnt = __builtin_amdgcn_readfirstlane(cnt);
      if (s == 25) *LDSP(u32, L_CORRB + lane * 16 + j * 4) = split_hi_lo(hh ? c1[0] : c0[0]);
      if (s == 28) { wait_vm<0>(); lds_signal(L_SYNC); }
      sfence();
    }
    const bf16x8 mf = as_bf((u32x4){mn, mn, mn, mn});
    acc[0] = mfma32(wf[0][0], mf, acc[0]);
    acc[1] = mfma32(wf[0][1], mf, acc[1]);
  }
  STAMP(3);
  __builtin_amdgcn_s_setprio(3);
  {
    const u32 arow = WS_A + ((rt * 4 + j) * NSTEP + lstep) * 1024;
    if (!xt) {
#pragma unroll
      for (int h = 0; h < 4; ++h) {
        u32x4 o;
#pragma unroll
        for (int i = 0; i < 4; ++i) o[i] = relu2(pack2(acc[h >> 1][8 * (h & 1) + 2 * i], acc[h >> 1][8 * (h & 1) + 2 * i + 1]));
        store_dev(o, srd_ws, lane * 16, (u32)(h >> 1) < nl ? arow + h * 1024 : OOB);
      }
    } else {
#pragma unroll
      for (int h = 0; h < 4; ++h) {
        float t2[8];
#pragma unroll
        for (int i = 0; i < 8; ++i) t2[i] = mul1(acc[h >> 1][8 * (h & 1) + i], rl);
        sfence();
        store_dev(phi8(t2), srd_ws, lane * 16, arow + h * 1024);
      }
    }
  }
  STAMP(4);
  const u32 hd = g * 4 + j, kvb = WS_KVP + hd * KVH_B;
  const bool zown = rt == (hd & 15);
  const u32 sa = xt ? OOB : kvb + rt * 2048, sz = (!xt && zown) ? kvb + 32768 : OOB;
  u32x4 a, a2, z, z2;
#define SLICE_LOADS "buffer_load_dwordx4 %0, %4, %5, %6 offen sc1\n\tbuffer_load_dwordx4 %1, %4, %5, %6 offen offset:1024 sc1\n\t" \
                    "buffer_load_dwordx4 %2, %4, %5, %7 offen sc1\n\tbuffer_load_dwordx4 %3, %4, %5, %7 offen offset:1024 sc1"
  asm volatile(SLICE_LOADS : "=&v"(a), "=&v"(a2), "=&v"(z), "=&v"(z2) : "v"(lane * 16), "s"(srd_ws), "s"(sa), "s"(sz) : "memory");
  wait_vm<4>();
  store_one((u32x4){(u32)tag, (u32)(tag >> 32), 0u, 0u}, srd_ws, WS_FLAGS + ((rt * 2 + (j >> 1)) * 48 + (xt ? 32 : 16) + g * 2 + (j & 1)) * 128);
  asm volatile("s_waitcnt vmcnt(1)" : "+v"(a), "+v"(a2), "+v"(z), "+v"(z2));
  STAMP(5);
  if (!xt) {
#pragma unroll 1
    for (u32 i = 0; i < 64; ++i) {
      u32 bad = (a.y ^ (u32)tag) | (a.w ^ (u32)(tag >> 32)), bad2 = (a2.y ^ (u32)tag) | (a2.w ^ (u32)(tag >> 32));
      if (zown) { bad |= (z.y ^ (u32)tag) | (z.w ^ (u32)(tag >> 32)); bad2 |= (z2.y ^ (u32)tag) | (z2.w ^ (u32)(tag >> 32)); }
      const u64 m = __ballot(bad != 0), m2 = __ballot(bad2 != 0);
      if ((m | m2) == 0) break;
      poll2<0>(p.ws + sa + (m ? __builtin_ctzll(m) : 64 + __builtin_ctzll(m2)) * 16, tag << 32, 0xffffffff00000000ull);
      asm volatile(SLICE_LOADS "\n\ts_waitcnt vmcnt(0)" : "=&v"(a), "=&v"(a2), "=&v"(z), "=&v"(z2) : "v"(lane * 16), "s"(srd_ws), "s"(sa), "s"(sz) : "memory");
    }
    float v[8] = {lo_f(a.x) + lo_f(a2.x), hi_f(a.x) + hi_f(a2.x), lo_f(a.z) + lo_f(a2.z), hi_f(a.z) + hi_f(a2.z),
                  lo_f(z.x) + lo_f(z2.x), hi_f(z.x) + hi_f(z2.x), lo_f(z.z) + lo_f(z2.z), hi_f(z.z) + hi_f(z2.z)};
#pragma unroll
    for (int i = 0; i < 8; ++i) v[i] = dpp_add<0x128>(v[i]);
#pragma unroll
    for (int i = 0; i < 8; ++i) v[i] = dpp_add<0x124>(v[i]);
#pragma unroll
    for (int i = 0; i < 4; ++i) {
      auto sw = __builtin_amdgcn_permlane16_swap(__float_as_uint(v[i]), __float_as_uint(v[4 + i]), false, false);
      v[i] = swap32_add(__uint_as_float(sw[0]) + __uint_as_float(sw[1]));
    }
    const u32 fo = lane < 4 ? WS_KVF + hd * 1024 + rt * 64 + lane * 16 : ((lane >> 2) == 4 && zown ? WS_KVZ + hd * 64 + (lane & 3) * 16 : OOB);
    store_dev((u32x4){pack2(v[0], v[1]), (u32)tag, pack2(v[2], v[3]), (u32)(tag >> 32)}, srd_ws, fo, 0);
  }
  __builtin_amdgcn_s_setprio(0);
  STAMP(6);
  const char* fq = p.ws + WS_FLAGS + ((rt * 2 + (j >> 1)) * 48 + 16 + kq * 4 + (j & 1)) * 128;
  const u32 rdy = L_SYNC + 48 + j * 4;
  u32 now = 0;
#pragma unroll 1
  for (u32 i = 0; i < SPIN_CAP && now == 0; ++i) {
    u64 f0, f1, f2, f3;
    asm volatile("s_load_dwordx2 %0, %4, 0 glc\n\ts_load_dwordx2 %1, %4, 256 glc\n\ts_load_dwordx2 %2, %4, 2048 glc\n\t"
                 "s_load_dwordx2 %3, %4, 2304 glc\n\ts_waitcnt lgkmcnt(0)"
                 : "=&s"(f0), "=&s"(f1), "=&s"(f2), "=&s"(f3) : "s"(fq) : "memory");
    now = ((f0 ^ tag) | (f1 ^ tag)) ? 0 : (((f2 ^ tag) | (f3 ^ tag)) ? 1 : 2);
  }
  *LDSP(u32, rdy) = now;
  __builtin_amdgcn_s_setprio(3);
  STAMP(7);
  u32x4 k0, k1, z0, z1;
#define KV_LOADS "buffer_load_dwordx4 %0, %4, %6, %7 offen sc1\n\tbuffer_load_dwordx4 %1, %4, %6, %7 offen offset:1024 sc1\n\t" \
                 "buffer_load_dwordx4 %2, %5, %6, %8 offen sc1\n\tbuffer_load_dwordx4 %3, %5, %6, %8 offen offset:64 sc1"
#define KV_OPS : "=&v"(k0), "=&v"(k1), "=&v"(z0), "=&v"(z1) \
               : "v"(lane * 16), "v"(l4 * 16), "s"(srd_ws), "s"(WS_KVF + (kq * 8 + j * 2) * 1024), "s"(WS_KVZ + (kq * 8 + j * 2) * 64) : "memory"
  asm volatile(KV_LOADS KV_OPS);
  poll2<256>(fq + 2048, tag, ~0ull);
  *LDSP(u32, rdy) = 2u;
  asm volatile("s_waitcnt vmcnt(0)" : "+v"(k0), "+v"(k1), "+v"(z0), "+v"(z1));
  STAMP(8);
#pragma unroll 1
  for (u32 i = 0; i < SPIN_CAP; ++i) {
    const u32 bad = ((k0.y ^ (u32)tag) | (k1.y ^ (u32)tag) | (z0.y ^ (u32)tag) | (z1.y ^ (u32)tag)) |
                    ((k0.w ^ (u32)(tag >> 32)) | (k1.w ^ (u32)(tag >> 32)) | (z0.w ^ (u32)(tag >> 32)) | (z1.w ^ (u32)(tag >> 32)));
    if (__all(bad == 0)) break;
    __builtin_amdgcn_s_sleep(4);
    asm volatile(KV_LOADS "\n\ts_waitcnt vmcnt(0)" KV_OPS);
  }
  STAMP(9);
  {
    auto a0 = __builtin_amdgcn_permlane16_swap(k0.x, z0.x, false, false), a1 = __builtin_amdgcn_permlane16_swap(k0.z, z0.z, false, false);
    auto b0 = __builtin_amdgcn_permlane16_swap(k1.x, z1.x, false, false), b1 = __builtin_amdgcn_permlane16_swap(k1.z, z1.z, false, false);
    *LDSP(u32x4, L_KVF + j * 2048 + lane * 16) = (u32x4){a0[0], a1[0], a0[1], a1[1]};
    *LDSO(u32x4, L_KVF + j * 2048 + lane * 16, 1024) = (u32x4){b0[0], b1[0], b0[1], b1[1]};
    lds_signal(L_SYNC + 36);
  }
  STAMP(13);
}

template <bool XT> DEV void group0_first(const Ptrs& p, u32 b, u32 lane, u32 w, u64& tag, u32* stamp) {
  const u32 xcd = b & 7, r = b >> 3, rt = (xcd & 1) * 8 + (r & 7), c4 = (xcd >> 1) * 4 + (r >> 3), g = c4 >> 1;
  const u32 mh = w >> 1, nh = w & 1, hh = lane >> 5, q = (lane & 15) >> 2;
  f32x16 acc[2][2] = {};
  bf16x8 xf[4][2] = {}, wf[4][2] = {};
  u32 xa = (mh * 64 + (lane & 31)) * 128 + ((hh ^ ((lane >> 1) & 7)) << 4);
  u32 wa = 16384 + (8 * hh + q) * 256 + (lane & 16) * 2 + (lane & 3) * 8 + ((XT ? (nh ^ q) : ((nh - q) & 3)) << 6), wb = wa ^ 128;
  u32 ca = L_CORRA + (nh * 32 + (lane & 31)) * 16;
  const u32x4 srd_ws = make_srd(p.ws, WS_END);
  u32x4 vw2 = steps4((lane >> 4) * 1024 + (lane & 3) * 16 + ((q ^ (lane >> 4)) << 6), 4096);
  auto wchunk = [&](int i) { dma4<0>(make_srd(p.w2, 1376 * 1024), vw2, (16 * (c4 >> 2) + 4 * i + w) * 16384 + (c4 & 3) * 256, L_WE + (4 * i + w) * 4096); };
  u32 ma0 = hh ? L_SYNC + 16 : L_MN + (mh * 64 + (lane & 31)) * 16, ma1 = hh ? L_SYNC + 16 : ma0 + 512;
  *LDSP(u32, L_SYNC + (lane & 15) * 4) = 0u;
  *LDSP(u32x4, L_CORRA + (w * 32 + (lane & 31)) * 16) = (u32x4){0u, 0u, 0u, 0u};
  asm volatile("" : "+v"(acc[0][0]), "+v"(acc[0][1]), "+v"(acc[1][0]), "+v"(acc[1][1]), "+v"(vw2));
  asm volatile("" : "+v"(xf[2][0]), "+v"(xf[2][1]), "+v"(wf[2][0]), "+v"(wf[2][1]), "+v"(xf[3][0]), "+v"(xf[3][1]), "+v"(wf[3][0]), "+v"(wf[3][1]),
               "+v"(xa), "+v"(wa), "+v"(wb), "+v"(ca), "+v"(ma0), "+v"(ma1));
  STAMP(1);
  __builtin_amdgcn_s_setprio(1);
  auto ldx = [&](int s, int h) { xf[s][h] = lds_b128(xa ^ (s << 5), h * 4096); sfence(); };
  auto ldw = [&](int s, int h) { wf[s][h] = lds_tr<1024>(h ? wb : wa, s * 4096); sfence(); };
  auto m1 = [&](int s, int i) {
    const int t = (i + 1) >> 1 & 1, u = i >> 1;
    acc[t][u] = XT ? mfma32(xf[s][t], wf[s][u], acc[t][u]) : mfma32(wf[s][u], xf[s][t], acc[t][u]);
    sfence();
  };
  barrier();
#pragma unroll 1
  for (u32 st = 0; st < 8; ++st) {
    m1(2, 0); ldw(0, 0); m1(2, 1); ldx(0, 0); ldx(0, 1); m1(2, 2); ldw(0, 1); m1(2, 3); ldw(1, 0);
    m1(3, 0); ldx(1, 0); ldx(1, 1); m1(3, 1); ldw(1, 1); m1(3, 2); ldw(2, 0); m1(3, 3); ldx(2, 0); ldx(2, 1);
    m1(0, 0); ldw(2, 1); m1(0, 1); ldw(3, 0); m1(0, 2); ldx(3, 0); ldx(3, 1); m1(0, 3); ldw(3, 1);
    m1(1, 0);
    xa = (xa + 32768) & 131071; wa = (wa + 32768) & 131071; wb = (wb + 32768) & 131071;
    asm volatile("" : "+v"(xa), "+v"(wa), "+v"(wb));
    sfence();
    m1(1, 1);
    m1(1, 2); m1(1, 3);
    barrier();
  }
  __builtin_amdgcn_s_setprio(2);
  f32x4 rl[8];
  m1(2, 0);
  const u32x2 tg = *LDSP(u32x2, L_SYNC + 40);
  wf[0][0] = lds_b128(ca); xf[0][0] = lds_b128(ma0); wf[0][1] = lds_b128(ca, 1024); xf[0][1] = lds_b128(ma1);
  sfence();
  m1(2, 3);
  if (XT) {
    const u32 ra = opaque_v(L_RL + (mh * 64 + hh * 4) * 4);
#pragma unroll
    for (int i = 0; i < 8; ++i) rl[i] = *LDSO(f32x4, ra, i * 32);
    sfence();
  }
  m1(3, 0);
  tag = ((u64)(u32)__builtin_amdgcn_readfirstlane(tg.y) << 32) | (u32)__builtin_amdgcn_readfirstlane(tg.x);
  sfence();
  m1(3, 3); m1(0, 0); m1(0, 3); m1(2, 1); m1(2, 2); m1(3, 1); m1(3, 2); m1(0, 1); m1(0, 2);
  STAMP(2);
  if constexpr (!XT) {
    __builtin_amdgcn_s_setprio(YPRIO);
#pragma unroll
    for (int k = 0; k < 8; ++k) {
      const int t = k >> 2, u = (k >> 1) & 1, s = k & 1;
      u32x4 o;
#pragma unroll
      for (int i = 0; i < 4; ++i) o[i] = relu2(pack2(acc[t][u][8 * s + 2 * i], acc[t][u][8 * s + 2 * i + 1]));
      store_dev(o, srd_ws, lane * 16, WS_A + ((rt * 4 + mh * 2 + t) * NSTEP + g * 8 + u * 4 + nh * 2 + s) * 1024);
    }
    STAMP(3);
    wchunk(2);
    wait_vm<4>();
    store_one((u32x4){(u32)tag, (u32)(tag >> 32), 0u, 0u}, srd_ws, WS_FLAGS + ((rt * 2 + mh) * 48 + g * 2 + nh) * 128);
    __builtin_amdgcn_s_setprio(0);
    STAMP(4);
    wchunk(3);
  } else {
    f32x16 kv = {}, zz = {};
    const bf16x8 ones = as_bf((u32x4){0x3f803f80u, 0x3f803f80u, 0x3f803f80u, 0x3f803f80u});
    const bool up = lane & 16;
    const u32 kvb = WS_KVP + (g * 4 + nh * 2) * KVH_B + (rt * 2 + mh) * 64;
    const u32 o0 = opaque_v((up ? KVH_B : 0) + (hh * 8 + q) * 2048 + (lane & 3) * 16);
    const u32 oz = opaque_v((lane & 14) ? OOB : (up ? KVH_B : 0) + 32768 + hh * 32 + (lane & 1) * 16);
#pragma unroll
    for (int k = 0; k < 4; ++k) {
      const int t = k >> 1, s = k & 1;
      float t2[8], v2[8];
#pragma unroll
      for (int i = 0; i < 8; ++i) t2[i] = mul1(acc[t][0][8 * s + i], rl[2 * k + (i >> 2)][i & 3]);
#pragma unroll
      for (int i = 0; i < 8; ++i) v2[i] = mul1(acc[t][1][8 * s + i], rl[2 * k + (i >> 2)][i & 3]);
      sfence();
      const u32x4 pv = {pack2(v2[0], v2[1]), pack2(v2[2], v2[3]), pack2(v2[4], v2[5]), pack2(v2[6], v2[7])};
      const u32x4 pk = phi8(t2);
      kv = mfma32(as_bf(pk), as_bf(pv), kv);
      zz = mfma32(as_bf(pk), ones, zz);
      sfence();
    }
    u32 ks[4], zs[4];
#pragma unroll
    for (int i = 0; i < 4; ++i) { const u32 lo = pack2(kv[2 * i], kv[2 * i + 1]), hi = pack2(kv[8 + 2 * i], kv[9 + 2 * i]); ks[i] = up ? hi : lo; }
    store_dev((u32x4){ks[0], (u32)tag, ks[1], (u32)(tag >> 32)}, srd_ws, o0, kvb);
    store_dev((u32x4){ks[2], (u32)tag, ks[3], (u32)(tag >> 32)}, srd_ws, o0, kvb + 8192);
#pragma unroll
    for (int i = 0; i < 4; ++i) { const u32 lo = pack2(zz[2 * i], zz[2 * i + 1]), hi = pack2(zz[8 + 2 * i], zz[9 + 2 * i]); zs[i] = up ? hi : lo; }
    const bool od = lane & 1;
    store_dev((u32x4){od ? zs[2] : zs[0], (u32)tag, od ? zs[3] : zs[1], (u32)(tag >> 32)}, srd_ws, oz, kvb);
    STAMP(3);
    __builtin_amdgcn_s_setprio(0);
    wchunk(2); wchunk(3);
  }
}

DEV void group0(const Ptrs& p, u32 b, u32 lane, u32 w, u32* stamp) {
  u64 tag;
  if (TOUCH) {
    const u32 rt = (b & 1) * 8 + ((b >> 3) & 7), kq = (b >> 1) & 3;
    u32x4 srd; u32 vo;
    if (w == 0) { srd = make_srd(p.w2, 1376 * 1024); vo = lane < 8 ? (16 * kq + 2 * lane) * 16384 : lane < 11 ? (48 + 6 * kq + 2 * lane) * 16384 : OOB; }
    else if (w == 1) { srd = make_srd(p.wo, 512 * 1024); vo = lane < 4 ? (8 * kq + 2 * lane) * 16384 : OOB; }
    else if (w == 2) { srd = make_srd(p.out, 2048 * 1024); vo = lane < 2 ? (rt * 2 + lane) * 65536 : OOB; }
    else { srd = make_srd(p.ws, WS_END); vo = lane < 9 ? lane * 2097152 + 2048 : OOB; }
    touch64(srd, vo, 0, L_XCH + 1024);
  }
  if (b & 64) group0_first<false>(p, b, lane, w, tag, stamp); else group0_first<true>(p, b, lane, w, tag, stamp);
  STAMP(5);
  b = opaque_s(b); lane = opaque_v(lane);
  const u32 rt = (b & 1) * 8 + ((b >> 3) & 7), kq = (b >> 1) & 3, ct = b >> 6, hh = lane >> 5, q = (lane & 15) >> 2;
  const __amdgpu_buffer_rsrc_t rws = __builtin_amdgcn_make_buffer_rsrc(p.ws, 0, WS_END, 0x00020000);
  const u32x4 srd_ws = make_srd(p.ws, WS_END);
  const char* fp = p.ws + WS_FLAGS + ((rt * 2 + (w >> 1)) * 48 + kq * 4) * 128;
  const u32 ab = WS_A + (rt * 4 + w) * NSTEP * 1024;
  u32 wl[4], we[4];
#pragma unroll
  for (int i = 0; i < 4; ++i) {
    wl[i] = opaque_v((4 * hh + q) * 256 + (lane & 16) * 2 + (lane & 3) * 8 + ((i ^ kq ^ q) << 6));
    we[i] = opaque_v(wl[i] + L_WE);
  }
  const float rlv = *LDSP(float, L_RL + (w * 32 + (lane & 31)) * 4);
  const u32 tok = rt * 128 + w * 32 + (lane & 31), oo = tok * 1024 + ct * 256 + kq * 64 + hh * 16;
  u32x4 xr0, xr1;
  const u32 rdy = opaque_v(L_SYNC + 48 + w * 4), cl = opaque_v(L_SYNC + 32);
  u32x4 af[30];
  const u32 l16 = opaque_v(lane * 16);
  auto lda = [&](int s) {
    const u32 so = s < 16 ? ab + (16 * kq + s) * 1024 : s < 22 ? ((s < 20 || kq < 3) ? ab + (48 + 6 * kq + s) * 1024 : OOB) : ab + (64 + 8 * kq + s) * 1024;
    asm volatile("buffer_load_dwordx4 %0, %1, %2, %3 offen sc1" : "=v"(af[s]) : "v"(l16), "s"(srd_ws), "s"(so) : "memory");
  };
  bf16x8 wf[3][4], kf[8];
  f32x16 acc[4] = {}, d[2];
  auto ldw = [&](int s, int i) { wf[s % 3][i] = lds_tr<2048>(s < 16 ? we[i] : wl[i], (s & 15) * 4096); sfence(); };
  auto peek = [&](u32 a, int o) { return __hip_atomic_load(LDSO(u32, a, o), __ATOMIC_RELAXED, __HIP_MEMORY_SCOPE_WORKGROUP); };
  auto pin = [&](f32x16& v) { asm volatile("" : "+v"(v)); sfence(); };
  const float sc = __builtin_amdgcn_rcpf(rlv);
  const u32x4 vw2 = steps4((lane >> 4) * 1024 + (lane & 3) * 16 + ((q ^ (lane >> 4)) << 6), 4096);
  u32 c0 = 0, c1 = 0;
  poll4(fp, tag, SPIN_CAP);
  STAMP(6);
#pragma unroll
  for (int s = 0; s < 8; ++s) lda(s);
  lds_wait(L_SYNC, 8);
#pragma unroll
  for (int i = 0; i < 6; ++i) ldw(i / 3, 1 + i % 3);
  wait_vm<8>();
  lds_signal(L_SYNC + 4);
#pragma unroll
  for (int s = 8; s < 16; ++s) lda(s);
  STAMP(7);
  static_for<30>([&](auto S_) {
    constexpr int s = S_;
    if (s == 0) dma4<0>(make_srd(p.w2, 1376 * 1024), vw2, (64 + 6 * kq + w) * 16384 + ct * 256, w * 4096);
    if (s == 2) dma4<0>(make_srd(p.w2, 1376 * 1024), vw2, w < 2 ? (68 + 6 * kq + w) * 16384 + ct * 256 : OOB, (w < 2 ? 4 + w : 14) * 4096);
    if (s == 4) dma4<0>(make_srd(p.wo, 512 * 1024), vw2, (8 * kq + w) * 16384 + ct * 256, (6 + w) * 4096);
    if (s == 6) dma4<0>(make_srd(p.wo, 512 * 1024), vw2, (8 * kq + 4 + w) * 16384 + ct * 256, (10 + w) * 4096);
    if (s == SL - 2 || s == SO - 2) { c0 = peek(rdy, 0); sfence(); }
    if (s == 4) { c1 = peek(cl, -28); sfence(); }
    if (s == 6 && (u32)__builtin_amdgcn_readfirstlane(c1) < 4) lds_wait(L_SYNC + 4, 4);
    if (s == SL) {
      if ((u32)__builtin_amdgcn_readfirstlane(c0) < 1) lds_wait(rdy, 1);
      STAMP(8);
#pragma unroll
      for (int k = 16; k < 22; ++k) lda(k);
    }
    if (s == 12) { wait_vm<6>(); lds_signal(cl); }
    if (s == 14) lds_wait(cl, 4);
    if (s == 17) { c1 = peek(cl, 4); sfence(); }
    if (s == SO) {
      if ((u32)__builtin_amdgcn_readfirstlane(c0) < 2) lds_wait(rdy, 2);
      STAMP(9);
#pragma unroll
      for (int k = 22; k < 30; ++k) lda(k);
    }
    if (s == 19) {
      if ((u32)__builtin_amdgcn_readfirstlane(c1) < 4) lds_wait(cl + 4, 4);
#pragma unroll
      for (int h = 0; h < 4; ++h) kf[h] = lds_b128(L_KVF + lane * 16, h * 1024);
      sfence();
    }
    if constexpr (s >= 21 && s < 25) { kf[s - 17] = lds_b128(L_KVF + lane * 16, (s - 17) * 1024); sfence(); }
    constexpr int h = s - 21;
    constexpr bool at = h >= 0 && h < 8;
    float f, m[8];
    if constexpr (s < 22) need<s, s>(af[s]);
    if constexpr (h >= -1 && h < 7) { need<s, s + 2>(af[s + 2]); d[(h + 1) & 1] = mfma32(kf[h + 1], as_bf(af[s + 2]), (f32x16){}); pin(d[(h + 1) & 1]); }
    if constexpr (at) { f = mul1(sc, __builtin_amdgcn_rcpf(d[h & 1][8] + 1e-6f)); sfence(); }
    acc[1] = mfma32(wf[s % 3][1], as_bf(af[s]), acc[1]); pin(acc[1]);
    if (s + 2 < 30) ldw(s + 2, 1);
    if constexpr (at) { for (int i = 0; i < 4; ++i) m[i] = mul1(d[h & 1][i], f); sfence(); }
    acc[2] = mfma32(wf[s % 3][2], as_bf(af[s]), acc[2]); pin(acc[2]);
    if (s + 2 < 30) ldw(s + 2, 2);
    if constexpr (at) { for (int i = 4; i < 8; ++i) m[i] = mul1(d[h & 1][i], f); keep(d[h & 1]); sfence(); }
    acc[3] = mfma32(wf[s % 3][3], as_bf(af[s]), acc[3]); pin(acc[3]);
    if (s + 2 < 30) ldw(s + 2, 3);
    if constexpr (at) { af[s + 1] = (u32x4){pack2(m[0], m[1]), pack2(m[2], m[3]), pack2(m[4], m[5]), pack2(m[6], m[7])}; keep(af[s + 1]); sfence(); }
  });
  STAMP(10);
  bf16x8 w0[6];
  auto ld0 = [&](int s) { w0[s % 6] = lds_tr<2048>(s < 16 ? we[0] : wl[0], (s & 15) * 4096); sfence(); };
#pragma unroll
  for (int s = 0; s < 5; ++s) ld0(s);
  const u32 xb = WS_X + ((rt * 4 + ct) * 4 + w) * 32768;
#pragma unroll
  for (int i = 1; i < 4; ++i)
#pragma unroll
    for (int k = 0; k < 2; ++k)
      store_dev((u32x4){pack2(acc[i][8 * k], acc[i][8 * k + 1]), pack2(acc[i][8 * k + 2], acc[i][8 * k + 3]), pack2(acc[i][8 * k + 4], acc[i][8 * k + 5]),
                        pack2(acc[i][8 * k + 6], acc[i][8 * k + 7])}, srd_ws, lane * 16, xb + ((kq ^ i) * 4 + kq) * 2048 + k * 1024);
  xr0 = *(const GAS u32x4*)(p.x + oo); xr1 = *(const GAS u32x4*)(p.x + oo + 32);
  sfence();
#pragma unroll
  for (int s = 0; s < 30; ++s) {
    acc[0] = mfma32(w0[s % 6], as_bf(af[s]), acc[0]); pin(acc[0]);
    if (s + 5 < 30) ld0(s + 5);
  }
  wait_vm<2>();
  const u32 xf = WS_XFL + ((rt * 4 + ct) * 4 + w) * 512;
  store_one((u32x4){(u32)tag, (u32)(tag >> 32), 0u, 0u}, srd_ws, xf + kq * 128);
  STAMP(11);
  poll4(p.ws + xf, tag, SPIN_CAP);
  STAMP(12);
  u32x4 pp[3][2];
#pragma unroll
  for (int i = 0; i < 3; ++i) {
    pp[i][0] = __builtin_amdgcn_raw_buffer_load_b128(rws, lane * 16, xb + (kq * 4 + (kq ^ (i + 1))) * 2048, 16);
    pp[i][1] = __builtin_amdgcn_raw_buffer_load_b128(rws, lane * 16 + 1024, xb + (kq * 4 + (kq ^ (i + 1))) * 2048, 16);
  }
  sfence();
  float v[16];
#pragma unroll
  for (int e = 0; e < 16; ++e) v[e] = acc[0][e];
#pragma unroll
  for (int i = 0; i < 3; ++i)
#pragma unroll
    for (int e = 0; e < 8; ++e) { v[2 * e] += lo_f(pp[i][e >> 2][e & 3]); v[2 * e + 1] += hi_f(pp[i][e >> 2][e & 3]); }
  const float rs = rlv * LN2;
#pragma unroll
  for (int k = 0; k < 2; ++k) {
#pragma unroll
    for (int e = 0; e < 4; ++e) {
      auto sw = __builtin_amdgcn_permlane32_swap(__float_as_uint(v[8 * k + e]), __float_as_uint(v[8 * k + 4 + e]), false, false);
      v[8 * k + e] = __uint_as_float(sw[0]); v[8 * k + 4 + e] = __uint_as_float(sw[1]);
    }
    const u32x4 xr = k ? xr1 : xr0;
    u32x4 o;
#pragma unroll
    for (int e = 0; e < 4; ++e) o[e] = pack2(__builtin_fmaf(v[8 * k + 2 * e], rs, lo_f(xr[e])), __builtin_fmaf(v[8 * k + 2 * e + 1], rs, hi_f(xr[e])));
    *(GAS u32x4*)(p.out + oo + k * 32) = o;
  }
  if (b == 0 && w == 0) store_one((u32x4){(u32)tag, (u32)(tag >> 32), 0u, 0u}, srd_ws, WS_EPOCH);
  STAMP(13);
}

__global__ __launch_bounds__(512, 2) void fused_block(const char* __restrict__ x, const char* __restrict__ wqkv, const char* __restrict__ w1,
                                                      char* ws, const char* __restrict__ w2, const char* __restrict__ wo, char* __restrict__ out) {
  __shared__ __attribute__((aligned(16))) char lds_all[L_TOTAL];
  u32 stamp[32] = {};
  u64 pc;
  asm volatile("s_getpc_b64 %0" : "=s"(pc) : "s"((u32)(size_t)lds_all));
  STAMP(0);
  const u32 w = __builtin_amdgcn_readfirstlane(threadIdx.x >> 6);
  {
    u32 d0, d1, d2, d3;
    const u64 cp = (pc & ~127ull) + min((blockIdx.x >> 3) * 8 + w * 2, CODE_LINES - 2) * 128;
    asm volatile("s_cmp_gt_u32 %5, 3\n\ts_cbranch_scc1 1f\n\ts_load_dword %0, %4, 0\n\ts_load_dword %1, %4, 64\n\ts_load_dword %2, %4, 128\n\t"
                 "s_load_dword %3, %4, 192\n\ts_waitcnt lgkmcnt(0)\n1:"
                 : "=&s"(d0), "=&s"(d1), "=&s"(d2), "=&s"(d3) : "s"(cp), "s"(w) : "memory", "scc");
  }
  asm volatile("" ::"s"(x), "s"(wqkv), "s"(w1), "s"(ws), "s"(w2), "s"(wo), "s"(out));
  const Ptrs p = {x, wqkv, w1, ws, w2, wo, out};
  if (w < 4) group0(p, opaque_s(blockIdx.x), opaque_v(threadIdx.x) & 63, w, stamp);
  else group1(p, blockIdx.x, threadIdx.x & 63, w, stamp);
}

extern "C" void kernel_launch(void* const* d_in, const int* in_sizes, int n_in, void* d_out, int out_size, void* d_ws, size_t ws_size,
                              hipStream_t stream) {
  fused_block<<<256, 512, 0, stream>>>((const char*)d_in[0], (const char*)d_in[1], (const char*)d_in[4], (char*)d_ws, (const char*)d_in[5],
                                       (const char*)d_in[2], (char*)d_out);
}
```

```cpp
#include <hip/hip_runtime.h>
#include <hip/hip_bf16.h>
#include <type_traits>

#if defined(__HIP_DEVICE_COMPILE__)
#pragma clang attribute push(__attribute__((target("no-packed-fp32-ops"))), apply_to = function)
#endif
typedef unsigned u32;
typedef unsigned long long u64;
typedef __bf16 bf16x8 __attribute__((ext_vector_type(8)));
typedef __bf16 bf16x2 __attribute__((ext_vector_type(2)));
typedef short s16x4 __attribute__((ext_vector_type(4)));
typedef short s16x2 __attribute__((ext_vector_type(2)));
typedef float f32x16 __attribute__((ext_vector_type(16)));
typedef float f32x4 __attribute__((ext_vector_type(4)));
typedef float f32x2 __attribute__((ext_vector_type(2)));
typedef u32 u32x4 __attribute__((ext_vector_type(4)));
typedef u32 u32x2 __attribute__((ext_vector_type(2)));
#define LAS __attribute__((address_space(3)))
#define GAS __attribute__((address_space(1)))
#define DEV __device__ __forceinline__
#define RLX_AGENT __ATOMIC_RELAXED, __HIP_MEMORY_SCOPE_AGENT
#define LDSP(T, a) ((LAS T*)(size_t)(a))

#define ROT1 1
#define PF 1
#define QWIN 3
#define AWIN 5
#define DLY 0
#define SLP 2
#define PSC "sc1"
#define SL 8
#define SO 12
#define YPRIO 0
#define PFMODE 0
#define GATE 1
#define KVMODE 1
#define PRIO2 1

constexpr u32 WS_EPOCH = 0;
constexpr u32 WS_STATS = 4096;
constexpr u32 WS_FLAGS = WS_STATS + 16384;
constexpr u32 WS_XFL = WS_FLAGS + 32 * 48 * 128;
constexpr u32 WS_KVF = WS_XFL + 1024 * 128;
constexpr u32 WS_KVZ = WS_KVF + 32768;
constexpr u32 WS_KVP = WS_KVZ + 2048;
constexpr u32 KVH_B = 34816;
constexpr u32 WS_A = 2u << 20;
constexpr u32 NSTEP = 118;
constexpr u32 WS_X = WS_A + 64 * NSTEP * 1024;
constexpr u32 WS_END = WS_X + 4096 * 2048;
constexpr u32 WS_STAMP = 32u << 20;
static_assert(WS_KVP + 32 * KVH_B <= WS_A, "ws map");
constexpr u32 OOB = 0x40000000u;

constexpr u32 L_WE = 65536;
constexpr u32 L_SCR = 131072;
constexpr u32 L_CORRA = L_SCR;
constexpr u32 L_MN = L_SCR + 2048;
constexpr u32 L_CORRB = L_SCR + 4096;
constexpr u32 L_GRAN = L_SCR + 5120;
constexpr u32 L_RL = L_SCR + 6144;
constexpr u32 L_SYNC = L_SCR + 6656;
constexpr u32 L_JUNK = L_SCR + 7168;
constexpr u32 L_WL = L_SCR + 8192;
constexpr u32 L_KVF = 32768;
constexpr u32 L_FINE = 49152;
constexpr u32 L_TOTAL = 163840;

constexpr u32 CODE_LINES = 176;
constexpr u32 SPIN_CAP = 1u << 18;
constexpr float LOG2E = 1.4426950408889634f, LN2 = 0.6931471805599453f;

DEV float asf(u32 v) { return __builtin_bit_cast(float, v); }
DEV u32 asu(float v) { return __builtin_bit_cast(u32, v); }
DEV bool all_lanes(bool v) { return __builtin_amdgcn_ballot_w64(!v) == 0; }
DEV u32 umin(u32 a, u32 b) { return a < b ? a : b; }
DEV void sfence() { __builtin_amdgcn_sched_barrier(0); }
template <int N> DEV void wait_vm() { asm volatile("s_waitcnt vmcnt(%0)" ::"i"(N) : "memory"); }
DEV void wait_lgkm() { sfence(); __builtin_amdgcn_s_waitcnt(0xC07F); asm volatile("" ::: "memory"); sfence(); }
DEV void barrier() { sfence(); __builtin_amdgcn_s_waitcnt(0xC07F); asm volatile("s_barrier" ::: "memory"); sfence(); }

DEV u32x4 make_srd(const void* p, u32 bytes) {
  u64 a = (u64)p;
  return (u32x4){(u32)a, (u32)(a >> 32) & 0xffffu, bytes, 0x00020000u};
}
template <int POL> DEV void dma(u32x4 srd, u32 voff, u32 soff, u32 ldsaddr) {
  if (POL == 1)
    asm volatile("s_mov_b32 m0, %3\n\ts_nop 0\n\tbuffer_load_dwordx4 %0, %1, %2 offen nt lds" ::"v"(voff), "s"(srd), "s"(soff), "s"(ldsaddr) : "memory");
  else if (POL == 2)
    asm volatile("s_mov_b32 m0, %3\n\ts_nop 0\n\tbuffer_load_dwordx4 %0, %1, %2 offen sc1 lds" ::"v"(voff), "s"(srd), "s"(soff), "s"(ldsaddr) : "memory");
  else
    asm volatile("s_mov_b32 m0, %3\n\ts_nop 0\n\tbuffer_load_dwordx4 %0, %1, %2 offen lds" ::"v"(voff), "s"(srd), "s"(soff), "s"(ldsaddr) : "memory");
}
template <int POL> DEV void dma4(u32x4 srd, u32x4 v, u32 soff, u32 ldsaddr) {
  if (POL == 1)
    asm volatile("s_mov_b32 m0, %6\n\ts_nop 0\n\tbuffer_load_dwordx4 %0, %4, %5 offen nt lds\n\tbuffer_load_dwordx4 %1, %4, %5 offen offset:1024 nt lds\n\t"
                 "buffer_load_dwordx4 %2, %4, %5 offen offset:2048 nt lds\n\tbuffer_load_dwordx4 %3, %4, %5 offen offset:3072 nt lds"
                 ::"v"(v.x), "v"(v.y), "v"(v.z), "v"(v.w), "s"(srd), "s"(soff), "s"(ldsaddr) : "memory");
  else
    asm volatile("s_mov_b32 m0, %6\n\ts_nop 0\n\tbuffer_load_dwordx4 %0, %4, %5 offen lds\n\tbuffer_load_dwordx4 %1, %4, %5 offen offset:1024 lds\n\t"
                 "buffer_load_dwordx4 %2, %4, %5 offen offset:2048 lds\n\tbuffer_load_dwordx4 %3, %4, %5 offen offset:3072 lds"
                 ::"v"(v.x), "v"(v.y), "v"(v.z), "v"(v.w), "s"(srd), "s"(soff), "s"(ldsaddr) : "memory");
}
DEV void touch64(u32x4 srd, u32 voff, u32 soff, u32 ldsaddr) {
  asm volatile("s_mov_b32 m0, %3\n\ts_nop 0\n\tbuffer_load_dword %0, %1, %2 offen lds" ::"v"(voff), "s"(srd), "s"(soff), "s"(ldsaddr) : "memory");
}
DEV u32x4 steps4(u32 v, u32 step) { step -= 1024; return (u32x4){v, v + step, v + 2 * step, v + 3 * step}; }
DEV void store_one(u32x4 v, u32x4 srd, u32 soff) {
  asm volatile("s_mov_b64 exec, 1\n\tbuffer_store_dwordx4 %0, off, %1, %2 sc1\n\ts_mov_b64 exec, -1\n\ts_nop 0" ::"v"(v), "s"(srd), "s"(soff) : "memory");
}
DEV void store_dev(u32x4 v, u32x4 srd, u32 voff, u32 soff) {
  asm volatile("buffer_store_dwordx4 %0, %1, %2, %3 offen sc1\n\ts_nop 1" ::"v"(v), "v"(voff), "s"(srd), "s"(soff) : "memory");
}
#define LDSO(T, a, o) ((LAS T*)(LDSP(char, a) + (o)))
DEV bf16x8 lds_b128(u32 a, int o = 0) { return *LDSO(bf16x8, a, o); }
template <int D> DEV bf16x8 lds_tr(u32 a, int o) {
  s16x4 lo = __builtin_amdgcn_ds_read_tr16_b64_v4i16(LDSO(s16x4, a, o));
  s16x4 hi = __builtin_amdgcn_ds_read_tr16_b64_v4i16(LDSO(s16x4, a, o + D));
  return __builtin_bit_cast(bf16x8, __builtin_shufflevector(lo, hi, 0, 1, 2, 3, 4, 5, 6, 7));
}
DEV f32x16 mfma32(bf16x8 a, bf16x8 b, f32x16 c) { return __builtin_amdgcn_mfma_f32_32x32x16_bf16(a, b, c, 0, 0, 0); }
DEV f32x4 mfma16(bf16x8 a, bf16x8 b, f32x4 c) { return __builtin_amdgcn_mfma_f32_16x16x32_bf16(a, b, c, 0, 0, 0); }
DEV bf16x8 as_bf(u32x4 v) { return __builtin_bit_cast(bf16x8, v); }
DEV u32 pack2(float a, float b) { return __builtin_bit_cast(u32, __builtin_convertvector((f32x2){a, b}, bf16x2)); }
DEV u32 relu2(u32 v) { return __builtin_bit_cast(u32, __builtin_elementwise_max(__builtin_bit_cast(s16x2, v), (s16x2){0, 0})); }
DEV float lo_f(u32 v) { return asf(v << 16); }
DEV float hi_f(u32 v) { return asf(v & 0xffff0000u); }
DEV float rbf(float f) { return lo_f(pack2(f, 0.f)); }
DEV u32 split_hi_lo(float c) { float h = rbf(c); return pack2(h, c - h); }
DEV float mul1(float a, float b) { float r = a * b; asm("" : "+v"(r)); return r; }
template <int CTRL> DEV float dpp_add(float v) {
  return v + asf(__builtin_amdgcn_update_dpp(0u, asu(v), CTRL, 0xf, 0xf, true));
}
DEV float swap16_add(float v) {
  auto r = __builtin_amdgcn_permlane16_swap(asu(v), asu(v), false, false);
  return asf(r[0]) + asf(r[1]);
}
DEV float swap32_add(float v) {
  auto r = __builtin_amdgcn_permlane32_swap(asu(v), asu(v), false, false);
  return asf(r[0]) + asf(r[1]);
}
DEV float sum32(float v) {
  v = dpp_add<0xB1>(v); v = dpp_add<0x4E>(v); v = dpp_add<0x141>(v); v = dpp_add<0x140>(v);
  return swap16_add(v);
}
DEV u32x4 phi8(const float (&t2)[8]) {
  float e[8], m[8];
#pragma unroll
  for (int i = 0; i < 8; ++i) e[i] = __builtin_amdgcn_fmed3f(__builtin_amdgcn_exp2f(t2[i]), 0.f, 1.f);
  sfence();
#pragma unroll
  for (int i = 0; i < 8; ++i) asm("v_max_f32 %0, %1, %2" : "=v"(m[i]) : "s"(1e-6f * LOG2E), "v"(t2[i]));
  sfence();
#pragma unroll
  for (int i = 0; i < 8; ++i) { e[i] = __builtin_fmaf(m[i], LN2, e[i]); asm("" : "+v"(e[i])); }
  sfence();
  return (u32x4){pack2(e[0], e[1]), pack2(e[2], e[3]), pack2(e[4], e[5]), pack2(e[6], e[7])};
}
DEV void lds_signal(u32 addr) {
  asm volatile("s_mov_b64 exec, 1\n\tds_add_u32 %0, %1\n\ts_mov_b64 exec, -1" ::"v"(addr), "v"(1u) : "memory");
}
DEV void lds_wait(u32 addr, u32 waves) {
  u32 v, s, n = SPIN_CAP;
  asm volatile("1:\n\tds_read_b32 %0, %3\n\ts_waitcnt lgkmcnt(0)\n\tv_readfirstlane_b32 %1, %0\n\ts_sub_u32 %2, %2, 1\n\ts_cbranch_scc1 2f\n\t"
               "s_cmp_lt_u32 %1, %4\n\ts_cbranch_scc1 1b\n2:"
               : "=&v"(v), "=&s"(s), "+s"(n) : "v"(addr), "s"(waves) : "memory", "scc");
}
DEV u64 poll4(const char* p, u64 tag, u32 n) {
  u64 f0, f1, f2, f3;
  asm volatile("1:\n\ts_load_dwordx2 %0, %5, 0 glc\n\ts_load_dwordx2 %1, %5, 128 glc\n\ts_load_dwordx2 %2, %5, 256 glc\n\t"
               "s_load_dwordx2 %3, %5, 384 glc\n\ts_waitcnt lgkmcnt(0)\n\t"
               "s_xor_b64 %0, %0, %6\n\ts_xor_b64 %1, %1, %6\n\ts_xor_b64 %2, %2, %6\n\ts_xor_b64 %3, %3, %6\n\t"
               "s_or_b64 %0, %0, %1\n\ts_or_b64 %2, %2, %3\n\ts_or_b64 %0, %0, %2\n\ts_cbranch_scc0 2f\n\t"
               "s_sub_u32 %4, %4, 1\n\ts_cbranch_scc0 1b\n2:"
               : "=&s"(f0), "=&s"(f1), "=&s"(f2), "=&s"(f3), "+s"(n) : "s"(p), "s"(tag) : "memory", "scc");
  return f0;
}
template <int D> DEV void poll2(const char* p, u64 tag, u64 mask) {
  u64 f0, f1; u32 n = SPIN_CAP;
  asm volatile("1:\n\ts_load_dwordx2 %0, %3, 0 glc\n\ts_load_dwordx2 %1, %3, %6 glc\n\ts_waitcnt lgkmcnt(0)\n\t"
               "s_xor_b64 %0, %0, %4\n\ts_xor_b64 %1, %1, %4\n\ts_or_b64 %0, %0, %1\n\ts_and_b64 %0, %0, %5\n\ts_cbranch_scc0 2f\n\t"
               "s_sub_u32 %2, %2, 1\n\ts_cbranch_scc0 1b\n2:"
               : "=&s"(f0), "=&s"(f1), "+s"(n) : "s"(p), "s"(tag), "s"(mask), "i"(D) : "memory", "scc");
}
DEV u32x4 load_dev_wait(u32x4 srd, u32 voff, u32 soff) {
  u32x4 v;
  asm volatile("buffer_load_dwordx4 %0, %1, %2, %3 offen sc1\n\ts_waitcnt vmcnt(0)" : "=&v"(v) : "v"(voff), "s"(srd), "s"(soff) : "memory");
  return v;
}
template <int N, int I = 0, class F> DEV void static_for(F&& f) {
  if constexpr (I < N) { f(std::integral_constant<int, I>{}); static_for<N, I + 1>(f); }
}
constexpr int ops_upto(int t) { return 8 + 2 * (t < 4 ? t + 1 : 4) + (t >= 4 ? 4 : 0) + (t >= 6 ? 4 : 0) + (t >= SL ? 6 : 0) + (t >= SO ? 8 : 0); }
static_assert(SL > 6 && SO >= SL + 2 && SO <= 15, "schedule");
template <int T, int K> DEV void need(u32x4& v) {
  asm volatile("s_waitcnt vmcnt(%1)" : "+v"(v) : "i"(ops_upto(T) - (K < 16 ? K : 8 + K) - 1));
}
template <class T> DEV void keep(T& v) { asm volatile("" : "+v"(v)); }
template <class T> DEV T opaque_v(T v) { asm volatile("" : "+v"(v)); return v; }
template <class T> DEV T opaque_s(T v) { asm volatile("" : "+s"(v)); return v; }

#define STAMP(i)
#define FSTAMP(w, i)

struct Ptrs { const char *x, *wqkv, *w1; char* ws; const char *w2, *wo; char* out; };

DEV void group1(const Ptrs& p, u32 b, u32 lane, u32 w, u32* stamp) {
  const u32 xcd = b & 7, r = b >> 3, j = w & 3;
  const u32 rt = (xcd & 1) * 8 + (r & 7);
  const u32 rot = ROT1 == 0 ? 0 : ROT1 == 1 ? (r & 7) : (r & 7) + 2 * (r >> 3);
  const u32x4 srd_x = make_srd(p.x, 2048 * 1024);
  const u32 vx = (lane >> 3) * 1024 + (((lane & 7) ^ (lane >> 4)) << 4);
  const u32x4 vx4 = {vx, (vx ^ 64) + 7168, vx + 14336, (vx ^ 64) + 21504};
  auto issue_x = [&](int st) { dma4<0>(srd_x, vx4, (rt * 128 + 32 * j) * 1024 + ((st + rot) & 7) * 128, (st & 3) * 32768 + j * 4096); };
  issue_x(0);
  STAMP(10);
  sfence();
  const u32 c4 = (xcd >> 1) * 4 + (r >> 3), g = c4 >> 1;
  const bool xt = (c4 & 1) == 0;
  const u32 hh = lane >> 5, q = (lane & 15) >> 2;
  const u32 wst = xt ? 3072 : 2752;
  const u32x4 srd_w = make_srd(xt ? p.wqkv : p.w1, 512 * wst);
  const u32 ym = xt ? 0u : ~0u, l4 = lane >> 4;
  const u32 blk = ((q ^ l4) + (((q & l4) << 1) & ym)) & 3;
  const u32x4 vwa4 = steps4(l4 * wst + (lane & 3) * 16 + (blk & 1) * 64 + (blk >> 1) * (xt ? 1024 : 128) + (xt ? 1024 + g * 128 : g * 256), 4 * wst);
  auto issue_w = [&](int st) { dma4<0>(srd_w, vwa4, (((st + rot) & 7) * 64 + 16 * j) * wst, (st & 3) * 32768 + 16384 + j * 4096); };
  issue_w(0);
  const u32 row = rt * 128 + c4 * 8 + 2 * j;
  u32x4 a0, a1;
  asm volatile("global_load_dwordx4 %0, %2, %3\n\tglobal_load_dwordx4 %1, %2, %3 offset:512"
               : "=&v"(a0), "=&v"(a1) : "v"((row + hh) * 1024 + (lane & 31) * 16), "s"(p.x) : "memory");
  STAMP(11);
  issue_x(1); issue_w(1);
  sfence();
  u64 tag;
  asm volatile("s_load_dwordx2 %0, %1, 0\n\ts_waitcnt lgkmcnt(0)" : "=&s"(tag) : "s"(p.ws) : "memory");
  tag += 0x9E3779B97F4A7C15ull;
  const u32 nl = (xt || (g < 6 && !(g & 1))) ? 2 : 1, lstep = xt ? 86 + g * 4 : 64 + (g >> 1) * 6 + ((g & 1) ? (g < 6 ? 4 : 2) : 0);
  const u32x4 vwb4 = steps4((lane >> 3) * wst + ((((lane & 7) ^ ((lane >> 2) & 4)) << 4) & (nl == 2 ? 127 : 48)) + (xt ? g * 128 : lstep * 32), 8 * wst);
  auto issue = [&](int st, int part) {
    if (st < 8) { if (part) issue_w(st); else issue_x(st); }
    else dma4<0>(srd_w, vwb4, ((((st - 8) * 4 + j + rot) & 7) * 64 + 32 * part) * wst, (st - 8) * 32768 + j * 8192 + part * 4096);
  };
  const u32x4 srd_pf = j == 0 ? srd_w : (j == 3 ? make_srd(p.wo, 512 * 1024) : make_srd(p.w2, 1376 * 1024));
  const u32 vpf = j == 0 ? lane * wst : (lane < (j == 1 ? 64 : j == 2 ? 24 : 32) ? lane * 128 : OOB);
  const u32 spf = j == 0 ? (r & 7) * 64 * wst + (xt ? g * 128 : lstep * 32) : j == 1 ? (c4 >> 2) * 262144 + r * 8192 : j == 2 ? (64 + 6 * (c4 >> 2)) * 16384 + r * 3072 : (c4 >> 2) * 131072 + r * 4096;
  const u32 pfst = PFMODE == 0 ? 1 + (r & 3) : PFMODE == 1 ? (j == 0 ? 1 + (r & 3) : 2 + ((r + j) & 3)) : (j == 0 ? (r % 5) : 1 + (r + j) % 6);
  const u32x4 srd_ws = make_srd(p.ws, WS_END);
  const u32 xa = (j * 32 + (lane & 31)) * 128 + ((hh ^ ((lane >> 1) & 7)) << 4);
  const u32 selv = (((lane >> 4) ^ (lane >> 2)) & 1) ? 0u : 0x3f803f80u;
  const bf16x8 sel = as_bf((u32x4){selv, selv, selv, selv});
  const u32 wa = 16384 + (8 * hh + q) * 256 + (lane & 16) * 2 + (lane & 3) * 8 + ((xt ? (j ^ q) : ((j - q) & 3)) << 6);
  f32x4 cs = {};
  bf16x8 xf[32];
  u32 mn;
  float rl;
#pragma unroll
  for (int st = 0; st < 8; ++st) {
    if (st == 1) wait_vm<9>(); else wait_vm<8>();
    barrier();
    if (st == 0) STAMP(1);
    const u32 slot = (st & 3) * 32768;
    if (st == 5) dma<2>(srd_ws, lane * 16, j == 0 ? WS_STATS + rt * 1024 : OOB, j == 0 ? L_GRAN : L_JUNK);
    if (PF && st < 7 && st == pfst) touch64(srd_pf, vpf, spf, L_JUNK);
    issue(st + 2, 0);
    sfence();
    u32x4 gr;
    if (st == 7) gr = *LDSP(u32x4, L_GRAN + (j * 16 + ((lane & 31) >> 1)) * 16);
    bf16x8 cw[4];
#pragma unroll
    for (int s = 0; s < 4; ++s) cw[s] = lds_tr<1024>(wa + (slot & 65536), (slot & 32768) + s * 4096);
#pragma unroll
    for (int s = 0; s < 4; ++s) xf[st * 4 + s] = lds_b128((xa ^ (s << 5)) + (slot & 65536), slot & 32768);
    sfence();
    issue(st + 2, 1);
    sfence();
    if (st == 0) {
      asm volatile("" : "+v"(a0), "+v"(a1));
    float v[16];
#pragma unroll
    for (int i = 0; i < 4; ++i) { v[2 * i] = lo_f(a0[i]); v[2 * i + 1] = hi_f(a0[i]); v[8 + 2 * i] = lo_f(a1[i]); v[9 + 2 * i] = hi_f(a1[i]); }
    float s[4] = {};
#pragma unroll
    for (int i = 0; i < 16; ++i) s[i & 3] += v[i];
    const float mean = sum32((s[0] + s[1]) + (s[2] + s[3])) * (1.f / 512.f);
    float qq[4] = {};
#pragma unroll
    for (int i = 0; i < 16; ++i) { float d = v[i] - mean; qq[i & 3] = __builtin_fmaf(d, d, qq[i & 3]); }
    const float var = sum32((qq[0] + qq[1]) + (qq[2] + qq[3])) * (1.f / 512.f);
    const u32 pm = pack2(mean, __builtin_amdgcn_sqrtf(rbf(rbf(var) + 1e-5f)));
    const u32 p1 = __builtin_amdgcn_readlane(pm, 32);
    store_one(opaque_v((u32x4){pm, (u32)tag, p1, (u32)(tag >> 32)}), srd_ws, WS_STATS + row * 8);
      *LDSP(u32x2, L_SYNC + 40) = (u32x2){(u32)tag, (u32)(tag >> 32)};
    }
    if (st == 7) {
#pragma unroll 1
      for (u32 i = 0; i < SPIN_CAP; ++i) {
        if (all_lanes(gr.y == (u32)tag && gr.w == (u32)(tag >> 32))) break;
        gr = load_dev_wait(srd_ws, (j * 16 + ((lane & 31) >> 1)) * 16, WS_STATS + rt * 1024);
      }
      const u32 ms = (lane & 1) ? gr.z : gr.x;
      mn = ((ms & 0xffffu) ^ 0x8000u) * 0x10001u;
      rl = LOG2E * __builtin_amdgcn_rcpf(hi_f(ms));
      *LDSP(u32x4, L_MN + (j * 32 + (lane & 31)) * 16) = (u32x4){mn, mn, mn, mn};
      *LDSP(float, L_RL + (j * 32 + (lane & 31)) * 4) = rl;
      mn = hh ? 0u : mn;
    }
    sfence();
#pragma unroll
    for (int s = 0; s < 4; ++s) cs = mfma16(sel, cw[s], cs);
    asm volatile("" : "+v"(cs));
    sfence();
  }
  *LDSP(u32, L_CORRA + (j * 32 + (lane & 31)) * 16) = split_hi_lo(cs[0]);
  const u32 kq = c4 >> 2, cb = (c4 & 3) * 256;
  const u32x4 vw2 = steps4(l4 * 1024 + (lane & 3) * 16 + ((q ^ l4) << 6), 4096);
  const u32 wb0 = opaque_v(hh * 1024 + q * 128 + (lane & 16) * 2 + (lane & 3) * 8 + ((q & 2) << 5)), wb1 = opaque_v(wb0 ^ 64);
  const u32 wc0 = opaque_v(wb0 + j * 8192), wc1 = opaque_v(wb1 + j * 8192);
  wait_vm<8>();
  __builtin_amdgcn_s_setprio(1);
  barrier();
  STAMP(2);
  dma4<0>(make_srd(p.w2, 1376 * 1024), vw2, (16 * kq + j) * 16384 + cb, L_WE + j * 4096);
  dma4<0>(make_srd(p.w2, 1376 * 1024), vw2, (16 * kq + 4 + j) * 16384 + cb, L_WE + (4 + j) * 4096);
  f32x16 acc[2] = {};
  {
    bf16x8 wf[4][2], cw[3];
    f32x4 c0 = {}, c1 = {};
    u32 cnt = 0;
    auto ldw = [&](int s, int u) { wf[s & 3][u] = lds_tr<512>(u ? wb1 : wb0, s * 2048); sfence(); };
#pragma unroll
    for (int s = 0; s < 3; ++s) { ldw(s, 0); ldw(s, 1); }
#pragma unroll
    for (int s = 0; s < 32; ++s) {
      const int cr = s < 8 ? s : (s >= 14 && s < 22 ? s - 6 : -1), cm = (s >= 2 && s < 10) ? s - 2 : (s >= 16 && s < 24 ? s - 8 : -1);
      acc[0] = mfma32(wf[s & 3][0], xf[s], acc[0]); sfence();
      if (s == 12 && cnt < 4) lds_wait(L_SYNC, 4);
      if (s == 31 && cnt < 8) lds_wait(L_SYNC, 8);
      if (s + 3 < 32) ldw(s + 3, 0);
      if (s == 31) { wf[0][0] = lds_b128(L_CORRB + (lane & 31) * 16); wf[0][1] = lds_b128(L_CORRB + (lane & 31) * 16, 512); sfence(); }
      if (cr >= 0) { cw[cr % 3] = lds_tr<512>((cr & 1) ? wc1 : wc0, (cr >> 3) * 32768 + ((cr >> 1) & 3) * 2048); sfence(); }
      acc[1] = mfma32(wf[s & 3][1], xf[s], acc[1]); sfence();
      if (s + 3 < 32) ldw(s + 3, 1);
      if (cm >= 0) { if (cm & 1) c1 = mfma16(sel, cw[cm % 3], c1); else c0 = mfma16(sel, cw[cm % 3], c0); sfence(); }
      if (s == 7) { wait_vm<8>(); lds_signal(L_SYNC); }
      if (s == 9 || s == 29) cnt = __hip_atomic_load(LDSP(u32, L_SYNC), __ATOMIC_RELAXED, __HIP_MEMORY_SCOPE_WORKGROUP);
      if (s == 11 || s == 30) cnt = __builtin_amdgcn_readfirstlane(cnt);
      if (s == 25) *LDSP(u32, L_CORRB + lane * 16 + j * 4) = split_hi_lo(hh ? c1[0] : c0[0]);
      if (s == 28) { wait_vm<0>(); lds_signal(L_SYNC); }
      sfence();
    }
    const bf16x8 mf = as_bf((u32x4){mn, mn, mn, mn});
    acc[0] = mfma32(wf[0][0], mf, acc[0]);
    acc[1] = mfma32(wf[0][1], mf, acc[1]);
  }
  STAMP(3);
  __builtin_amdgcn_s_setprio(3);
  {
    const u32 arow = WS_A + ((rt * 4 + j) * NSTEP + lstep) * 1024;
    if (!xt) {
#pragma unroll
      for (int h = 0; h < 4; ++h) {
        u32x4 o;
#pragma unroll
        for (int i = 0; i < 4; ++i) o[i] = relu2(pack2(acc[h >> 1][8 * (h & 1) + 2 * i], acc[h >> 1][8 * (h & 1) + 2 * i + 1]));
        store_dev(o, srd_ws, lane * 16, (u32)(h >> 1) < nl ? arow + h * 1024 : OOB);
      }
    } else {
#pragma unroll
      for (int h = 0; h < 4; ++h) {
        float t2[8];
#pragma unroll
        for (int i = 0; i < 8; ++i) t2[i] = mul1(acc[h >> 1][8 * (h & 1) + i], rl);
        sfence();
        store_dev(phi8(t2), srd_ws, lane * 16, arow + h * 1024);
      }
    }
  }
  STAMP(4);
  const u32 hd = g * 4 + j, kvb = WS_KVP + hd * KVH_B;
  const bool zown = rt == (hd & 15);
  const u32 sa = xt ? OOB : kvb + rt * 2048, sz = (!xt && zown) ? kvb + 32768 : OOB;
  u32x4 a, a2, z, z2;
#define SLICE_LOADS "buffer_load_dwordx4 %0, %4, %5, %6 offen sc1\n\tbuffer_load_dwordx4 %1, %4, %5, %6 offen offset:1024 sc1\n\t" \
                    "buffer_load_dwordx4 %2, %4, %5, %7 offen sc1\n\tbuffer_load_dwordx4 %3, %4, %5, %7 offen offset:1024 sc1"
  asm volatile(SLICE_LOADS : "=&v"(a), "=&v"(a2), "=&v"(z), "=&v"(z2) : "v"(lane * 16), "s"(srd_ws), "s"(sa), "s"(sz) : "memory");
  lds_signal(L_SYNC + 12);
  wait_vm<4>();
  store_one((u32x4){(u32)tag, (u32)(tag >> 32), 0u, 0u}, srd_ws, WS_FLAGS + ((rt * 2 + (j >> 1)) * 48 + (xt ? 32 : 16) + g * 2 + (j & 1)) * 128);
  asm volatile("s_waitcnt vmcnt(1)" : "+v"(a), "+v"(a2), "+v"(z), "+v"(z2));
  STAMP(5);
  if (!xt) {
#pragma unroll 1
    for (u32 i = 0; i < 64; ++i) {
      u32 bad = (a.y ^ (u32)tag) | (a.w ^ (u32)(tag >> 32)), bad2 = (a2.y ^ (u32)tag) | (a2.w ^ (u32)(tag >> 32));
      if (zown) { bad |= (z.y ^ (u32)tag) | (z.w ^ (u32)(tag >> 32)); bad2 |= (z2.y ^ (u32)tag) | (z2.w ^ (u32)(tag >> 32)); }
      const u64 m = __builtin_amdgcn_ballot_w64(bad != 0), m2 = __builtin_amdgcn_ballot_w64(bad2 != 0);
      if ((m | m2) == 0) break;
      poll2<0>(p.ws + sa + (m ? __builtin_ctzll(m) : 64 + __builtin_ctzll(m2)) * 16, tag << 32, 0xffffffff00000000ull);
      asm volatile(SLICE_LOADS "\n\ts_waitcnt vmcnt(0)" : "=&v"(a), "=&v"(a2), "=&v"(z), "=&v"(z2) : "v"(lane * 16), "s"(srd_ws), "s"(sa), "s"(sz) : "memory");
    }
    float v[8] = {lo_f(a.x) + lo_f(a2.x), hi_f(a.x) + hi_f(a2.x), lo_f(a.z) + lo_f(a2.z), hi_f(a.z) + hi_f(a2.z),
                  lo_f(z.x) + lo_f(z2.x), hi_f(z.x) + hi_f(z2.x), lo_f(z.z) + lo_f(z2.z), hi_f(z.z) + hi_f(z2.z)};
#pragma unroll
    for (int i = 0; i < 8; ++i) v[i] = dpp_add<0x128>(v[i]);
#pragma unroll
    for (int i = 0; i < 8; ++i) v[i] = dpp_add<0x124>(v[i]);
#pragma unroll
    for (int i = 0; i < 4; ++i) {
      auto sw = __builtin_amdgcn_permlane16_swap(asu(v[i]), asu(v[4 + i]), false, false);
      v[i] = swap32_add(asf(sw[0]) + asf(sw[1]));
    }
    const u32 fo = lane < 4 ? WS_KVF + hd * 1024 + rt * 64 + lane * 16 : ((lane >> 2) == 4 && zown ? WS_KVZ + hd * 64 + (lane & 3) * 16 : OOB);
    store_dev((u32x4){pack2(v[0], v[1]), (u32)tag, pack2(v[2], v[3]), (u32)(tag >> 32)}, srd_ws, fo, 0);
  }
  lds_signal(L_SYNC + 8);
  __builtin_amdgcn_s_setprio(0);
  STAMP(6);
  const char* fq = p.ws + WS_FLAGS + ((rt * 2 + (j >> 1)) * 48 + 16 + kq * 4 + (j & 1)) * 128;
  const u32 rdy = L_SYNC + 48 + j * 4;
  u32 now = 0;
#pragma unroll 1
  for (u32 i = 0; i < SPIN_CAP && now < (KVMODE == 1 ? 2 : 1); ++i) {
    u64 f0, f1, f2, f3;
    asm volatile("s_load_dwordx2 %0, %4, 0 glc\n\ts_load_dwordx2 %1, %4, 256 glc\n\ts_load_dwordx2 %2, %4, 2048 glc\n\t"
                 "s_load_dwordx2 %3, %4, 2304 glc\n\ts_waitcnt lgkmcnt(0)"
                 : "=&s"(f0), "=&s"(f1), "=&s"(f2), "=&s"(f3) : "s"(fq) : "memory");
    now = ((f0 ^ tag) | (f1 ^ tag)) ? 0 : (((f2 ^ tag) | (f3 ^ tag)) ? 1 : 2);
    *LDSP(u32, rdy) = now;
  }
  if (KVMODE >= 2) __builtin_amdgcn_s_sleep(KVMODE);
  __builtin_amdgcn_s_setprio(3);
  STAMP(7);
  u32x4 k0, k1, z0, z1;
#define KV_LOADS "buffer_load_dwordx4 %0, %4, %6, %7 offen sc1\n\tbuffer_load_dwordx4 %1, %4, %6, %7 offen offset:1024 sc1\n\t" \
                 "buffer_load_dwordx4 %2, %5, %6, %8 offen sc1\n\tbuffer_load_dwordx4 %3, %5, %6, %8 offen offset:64 sc1"
  u32 l16 = lane * 16, lz = l4 * 16, ko = WS_KVF + (kq * 8 + j * 2) * 1024, zo = WS_KVZ + (kq * 8 + j * 2) * 64;
  u32x4 srd2 = srd_ws;
#pragma unroll 1
  for (u32 i = 0; i < SPIN_CAP; ++i) {
    u64 f2, f3;
    asm volatile(KV_LOADS "\n\ts_load_dwordx2 %9, %11, 2048 glc\n\ts_load_dwordx2 %10, %11, 2304 glc\n\ts_waitcnt lgkmcnt(0)"
                 : "=&v"(k0), "=&v"(k1), "=&v"(z0), "=&v"(z1), "+v"(l16), "+v"(lz), "+s"(srd2), "+s"(ko), "+s"(zo), "=&s"(f2), "=&s"(f3) : "s"(fq) : "memory");
    if (now < 2 && ((f2 ^ tag) | (f3 ^ tag)) == 0) { now = 2; *LDSP(u32, rdy) = 2u; }
    asm volatile("s_waitcnt vmcnt(0)" : "+v"(k0), "+v"(k1), "+v"(z0), "+v"(z1));
    const u32 bad = ((k0.y ^ (u32)tag) | (k1.y ^ (u32)tag) | (z0.y ^ (u32)tag) | (z1.y ^ (u32)tag)) |
                    ((k0.w ^ (u32)(tag >> 32)) | (k1.w ^ (u32)(tag >> 32)) | (z0.w ^ (u32)(tag >> 32)) | (z1.w ^ (u32)(tag >> 32)));
    if (all_lanes(bad == 0)) break;
  }
  STAMP(9);
  {
    auto a0 = __builtin_amdgcn_permlane16_swap(k0.x, z0.x, false, false), a1 = __builtin_amdgcn_permlane16_swap(k0.z, z0.z, false, false);
    auto b0 = __builtin_amdgcn_permlane16_swap(k1.x, z1.x, false, false), b1 = __builtin_amdgcn_permlane16_swap(k1.z, z1.z, false, false);
    *LDSP(u32x4, L_KVF + j * 2048 + lane * 16) = (u32x4){a0[0], a1[0], a0[1], a1[1]};
    *LDSO(u32x4, L_KVF + j * 2048 + lane * 16, 1024) = (u32x4){b0[0], b1[0], b0[1], b1[1]};
    lds_signal(L_SYNC + 36);
  }
  if (now < 2) { poll2<256>(fq + 2048, tag, ~0ull); *LDSP(u32, rdy) = 2u; }
  STAMP(13);
}

template <bool XT> DEV void group0_first(const Ptrs& p, u32 b, u32 lane, u32 w, u64& tag, u32* stamp) {
  const u32 xcd = b & 7, r = b >> 3, rt = (xcd & 1) * 8 + (r & 7), c4 = (xcd >> 1) * 4 + (r >> 3), g = c4 >> 1;
  const u32 mh = w >> 1, nh = w & 1, hh = lane >> 5, q = (lane & 15) >> 2;
  f32x16 acc[2][2] = {};
  bf16x8 xf[4][2] = {}, wf[4][2] = {};
  u32 xa = (mh * 64 + (lane & 31)) * 128 + ((hh ^ ((lane >> 1) & 7)) << 4);
  u32 wa = 16384 + (8 * hh + q) * 256 + (lane & 16) * 2 + (lane & 3) * 8 + ((XT ? (nh ^ q) : ((nh - q) & 3)) << 6), wb = wa ^ 128;
  u32 ca = L_CORRA + (nh * 32 + (lane & 31)) * 16;
  const u32x4 srd_ws = make_srd(p.ws, WS_END);
  u32x4 vw2 = steps4((lane >> 4) * 1024 + (lane & 3) * 16 + ((q ^ (lane >> 4)) << 6), 4096);
  auto wlate = [&]() {
    dma4<0>(make_srd(p.w2, 1376 * 1024), vw2, (64 + 6 * (c4 >> 2) + w) * 16384 + (c4 & 3) * 256, L_WL + w * 4096);
    if (w < 2) dma4<0>(make_srd(p.w2, 1376 * 1024), vw2, (68 + 6 * (c4 >> 2) + w) * 16384 + (c4 & 3) * 256, L_WL + (4 + w) * 4096);
  };
  auto wchunk = [&](int i) { dma4<0>(make_srd(p.w2, 1376 * 1024), vw2, (16 * (c4 >> 2) + 4 * i + w) * 16384 + (c4 & 3) * 256, L_WE + (4 * i + w) * 4096); };
  u32 ma0 = hh ? L_SYNC + 16 : L_MN + (mh * 64 + (lane & 31)) * 16, ma1 = hh ? L_SYNC + 16 : ma0 + 512;
  *LDSP(u32, L_SYNC + (lane & 15) * 4) = 0u;
  *LDSP(u32x4, L_CORRA + (w * 32 + (lane & 31)) * 16) = (u32x4){0u, 0u, 0u, 0u};
  asm volatile("" : "+v"(acc[0][0]), "+v"(acc[0][1]), "+v"(acc[1][0]), "+v"(acc[1][1]), "+v"(vw2));
  asm volatile("" : "+v"(xf[2][0]), "+v"(xf[2][1]), "+v"(wf[2][0]), "+v"(wf[2][1]), "+v"(xf[3][0]), "+v"(xf[3][1]), "+v"(wf[3][0]), "+v"(wf[3][1]),
               "+v"(xa), "+v"(wa), "+v"(wb), "+v"(ca), "+v"(ma0), "+v"(ma1));
  STAMP(1);
  __builtin_amdgcn_s_setprio(1);
  auto ldx = [&](int s, int h) { xf[s][h] = lds_b128(xa ^ (s << 5), h * 4096); sfence(); };
  auto ldw = [&](int s, int h) { wf[s][h] = lds_tr<1024>(h ? wb : wa, s * 4096); sfence(); };
  auto m1 = [&](int s, int i) {
    const int t = (i + 1) >> 1 & 1, u = i >> 1;
    acc[t][u] = XT ? mfma32(xf[s][t], wf[s][u], acc[t][u]) : mfma32(wf[s][u], xf[s][t], acc[t][u]);
    sfence();
  };
  barrier();
#pragma unroll 1
  for (u32 st = 0; st < 8; ++st) {
    m1(2, 0); ldw(0, 0); m1(2, 1); ldx(0, 0); ldx(0, 1); m1(2, 2); ldw(0, 1); m1(2, 3); ldw(1, 0);
    m1(3, 0); ldx(1, 0); ldx(1, 1); m1(3, 1); ldw(1, 1); m1(3, 2); ldw(2, 0); m1(3, 3); ldx(2, 0); ldx(2, 1);
    m1(0, 0); ldw(2, 1); m1(0, 1); ldw(3, 0); m1(0, 2); ldx(3, 0); ldx(3, 1); m1(0, 3); ldw(3, 1);
    m1(1, 0);
    xa = (xa + 32768) & 131071; wa = (wa + 32768) & 131071; wb = (wb + 32768) & 131071;
    asm volatile("" : "+v"(xa), "+v"(wa), "+v"(wb));
    sfence();
    m1(1, 1);
    m1(1, 2); m1(1, 3);
    barrier();
  }
  __builtin_amdgcn_s_setprio(2);
  f32x4 rl[8];
  m1(2, 0);
  const u32x2 tg = *LDSP(u32x2, L_SYNC + 40);
  wf[0][0] = lds_b128(ca); xf[0][0] = lds_b128(ma0); wf[0][1] = lds_b128(ca, 1024); xf[0][1] = lds_b128(ma1);
  sfence();
  m1(2, 3);
  if (XT) {
    const u32 ra = opaque_v(L_RL + (mh * 64 + hh * 4) * 4);
#pragma unroll
    for (int i = 0; i < 8; ++i) rl[i] = *LDSO(f32x4, ra, i * 32);
    sfence();
  }
  m1(3, 0);
  tag = ((u64)(u32)__builtin_amdgcn_readfirstlane(tg.y) << 32) | (u32)__builtin_amdgcn_readfirstlane(tg.x);
  sfence();
  m1(3, 3); m1(0, 0); m1(0, 3); m1(2, 1); m1(2, 2); m1(3, 1); m1(3, 2); m1(0, 1); m1(0, 2);
  STAMP(2);
  if constexpr (!XT) {
    __builtin_amdgcn_s_setprio(YPRIO);
#pragma unroll
    for (int k = 0; k < 8; ++k) {
      const int t = k >> 2, u = (k >> 1) & 1, s = k & 1;
      u32x4 o;
#pragma unroll
      for (int i = 0; i < 4; ++i) o[i] = relu2(pack2(acc[t][u][8 * s + 2 * i], acc[t][u][8 * s + 2 * i + 1]));
      store_dev(o, srd_ws, lane * 16, WS_A + ((rt * 4 + mh * 2 + t) * NSTEP + g * 8 + u * 4 + nh * 2 + s) * 1024);
    }
    STAMP(3);
    wchunk(2);
    wait_vm<4>();
    store_one((u32x4){(u32)tag, (u32)(tag >> 32), 0u, 0u}, srd_ws, WS_FLAGS + ((rt * 2 + mh) * 48 + g * 2 + nh) * 128);
    __builtin_amdgcn_s_setprio(0);
    STAMP(4);
    wchunk(3); wlate();
  } else {
    f32x16 kv = {}, zz = {};
    const bf16x8 ones = as_bf((u32x4){0x3f803f80u, 0x3f803f80u, 0x3f803f80u, 0x3f803f80u});
    const bool up = lane & 16;
    const u32 kvb = WS_KVP + (g * 4 + nh * 2) * KVH_B + (rt * 2 + mh) * 64;
    const u32 o0 = opaque_v((up ? KVH_B : 0) + (hh * 8 + q) * 2048 + (lane & 3) * 16);
    const u32 oz = opaque_v((lane & 14) ? OOB : (up ? KVH_B : 0) + 32768 + hh * 32 + (lane & 1) * 16);
#pragma unroll
    for (int k = 0; k < 4; ++k) {
      const int t = k >> 1, s = k & 1;
      float t2[8], v2[8];
#pragma unroll
      for (int i = 0; i < 8; ++i) t2[i] = mul1(acc[t][0][8 * s + i], rl[2 * k + (i >> 2)][i & 3]);
#pragma unroll
      for (int i = 0; i < 8; ++i) v2[i] = mul1(acc[t][1][8 * s + i], rl[2 * k + (i >> 2)][i & 3]);
      sfence();
      const u32x4 pv = {pack2(v2[0], v2[1]), pack2(v2[2], v2[3]), pack2(v2[4], v2[5]), pack2(v2[6], v2[7])};
      const u32x4 pk = phi8(t2);
      kv = mfma32(as_bf(pk), as_bf(pv), kv);
      zz = mfma32(as_bf(pk), ones, zz);
      sfence();
    }
    u32 ks[4], zs[4];
#pragma unroll
    for (int i = 0; i < 4; ++i) { const u32 lo = pack2(kv[2 * i], kv[2 * i + 1]), hi = pack2(kv[8 + 2 * i], kv[9 + 2 * i]); ks[i] = up ? hi : lo; }
    store_dev((u32x4){ks[0], (u32)tag, ks[1], (u32)(tag >> 32)}, srd_ws, o0, kvb);
    store_dev((u32x4){ks[2], (u32)tag, ks[3], (u32)(tag >> 32)}, srd_ws, o0, kvb + 8192);
#pragma unroll
    for (int i = 0; i < 4; ++i) { const u32 lo = pack2(zz[2 * i], zz[2 * i + 1]), hi = pack2(zz[8 + 2 * i], zz[9 + 2 * i]); zs[i] = up ? hi : lo; }
    const bool od = lane & 1;
    store_dev((u32x4){od ? zs[2] : zs[0], (u32)tag, od ? zs[3] : zs[1], (u32)(tag >> 32)}, srd_ws, oz, kvb);
    STAMP(3);
    __builtin_amdgcn_s_setprio(0);
    wchunk(2); wchunk(3); wlate();
  }
}

DEV void group0(const Ptrs& p, u32 b, u32 lane, u32 w, u32* stamp) {
  u64 tag;
  if (b & 64) group0_first<false>(p, b, lane, w, tag, stamp); else group0_first<true>(p, b, lane, w, tag, stamp);
  STAMP(5);
  b = opaque_s(b); lane = opaque_v(lane);
  const u32 rt = (b & 1) * 8 + ((b >> 3) & 7), kq = (b >> 1) & 3, ct = b >> 6, hh = lane >> 5, q = (lane & 15) >> 2;
  const __amdgpu_buffer_rsrc_t rws = __builtin_amdgcn_make_buffer_rsrc(p.ws, 0, WS_END, 0x00020000);
  const u32x4 srd_ws = make_srd(p.ws, WS_END);
  const char* fp = p.ws + WS_FLAGS + ((rt * 2 + (w >> 1)) * 48 + kq * 4) * 128;
  const u32 ab = WS_A + (rt * 4 + w) * NSTEP * 1024;
  u32 wl[4], we[4], wm[4];
#pragma unroll
  for (int i = 0; i < 4; ++i) {
    wl[i] = opaque_v((4 * hh + q) * 256 + (lane & 16) * 2 + (lane & 3) * 8 + ((i ^ kq ^ q) << 6));
    we[i] = opaque_v(wl[i] + L_WE);
    wm[i] = opaque_v(wl[i] + L_WL);
  }
  const float rlv = *LDSP(float, L_RL + (w * 32 + (lane & 31)) * 4);
  const u32 tok = rt * 128 + w * 32 + (lane & 31), oo = tok * 1024 + ct * 256 + kq * 64 + hh * 16;
  u32x4 xr0, xr1;
  const u32 rdy = opaque_v(L_SYNC + 48 + w * 4), cl = opaque_v(L_SYNC + 32);
  u32x4 af[30];
  const u32 l16 = opaque_v(lane * 16);
  auto lda = [&](int s) {
    const u32 so = s < 16 ? ab + (16 * kq + s) * 1024 : s < 22 ? ((s < 20 || kq < 3) ? ab + (48 + 6 * kq + s) * 1024 : OOB) : ab + (64 + 8 * kq + s) * 1024;
    asm volatile("buffer_load_dwordx4 %0, %1, %2, %3 offen sc1" : "=v"(af[s]) : "v"(l16), "s"(srd_ws), "s"(so) : "memory");
  };
  bf16x8 wf[3][4], kf[8];
  f32x16 acc[4] = {}, d[2];
  auto ldw = [&](int s, int i) { wf[s % 3][i] = lds_tr<2048>(s < 16 ? we[i] : s < 22 ? wm[i] : wl[i], (s < 16 ? s : s < 22 ? s - 16 : s - 22) * 4096); sfence(); };
  auto peek = [&](u32 a, int o) { return __hip_atomic_load(LDSO(u32, a, o), __ATOMIC_RELAXED, __HIP_MEMORY_SCOPE_WORKGROUP); };
  auto pin = [&](f32x16& v) { asm volatile("" : "+v"(v)); sfence(); };
  const float sc = __builtin_amdgcn_rcpf(rlv);
  const u32x4 vw2 = steps4((lane >> 4) * 1024 + (lane & 3) * 16 + ((q ^ (lane >> 4)) << 6), 4096);
  u32 c0 = 0, c1 = 0;
  wait_vm<0>();
  lds_signal(L_SYNC + 4);
  poll4(fp, tag, SPIN_CAP);
  STAMP(6);
  lds_wait(L_SYNC, 8);
#pragma unroll
  for (int i = 0; i < 8; ++i) ldw(i >> 2, i & 3);
  lds_wait(L_SYNC + 12, 4);
#pragma unroll
  for (int s = 0; s < 8; ++s) lda(s);
  lds_wait(L_SYNC + 8, 4);
  STAMP(7);
  static_for<30>([&](auto S_) {
    constexpr int s = S_;
    if constexpr (s < 4) { lda(8 + 2 * s); lda(9 + 2 * s); }
    if (s == 4) dma4<0>(make_srd(p.wo, 512 * 1024), vw2, (8 * kq + w) * 16384 + ct * 256, w * 4096);
    if (s == 6) dma4<0>(make_srd(p.wo, 512 * 1024), vw2, (8 * kq + 4 + w) * 16384 + ct * 256, (4 + w) * 4096);
    if (s == SL - 2 || s == SO - 2) { c0 = peek(rdy, 0); sfence(); }
    if (s == 4) { c1 = peek(cl, -28); sfence(); }
    if (s == 6 && (u32)__builtin_amdgcn_readfirstlane(c1) < 4) lds_wait(L_SYNC + 4, 4);
    if (s == SL) {
      if ((u32)__builtin_amdgcn_readfirstlane(c0) < 1) lds_wait(rdy, 1);
      STAMP(8);
#pragma unroll
      for (int k = 16; k < 22; ++k) lda(k);
    }
    if (s == SO) {
      if ((u32)__builtin_amdgcn_readfirstlane(c0) < 2) lds_wait(rdy, 2);
      STAMP(9);
#pragma unroll
      for (int k = 22; k < 30; ++k) lda(k);
    }
    if (s == 15) { wait_vm<14>(); lds_signal(cl); }
    if (s == 17) { c0 = peek(cl, 0); c1 = peek(cl, 4); sfence(); }
    if (s == 19) {
      if ((u32)__builtin_amdgcn_readfirstlane(c0) < 4) lds_wait(cl, 4);
      if ((u32)__builtin_amdgcn_readfirstlane(c1) < 4) lds_wait(cl + 4, 4);
#pragma unroll
      for (int h = 0; h < 4; ++h) kf[h] = lds_b128(L_KVF + lane * 16, h * 1024);
      sfence();
    }
    if constexpr (s >= 21 && s < 25) { kf[s - 17] = lds_b128(L_KVF + lane * 16, (s - 17) * 1024); sfence(); }
    constexpr int h = s - 21;
    constexpr bool at = h >= 0 && h < 8;
    float f, m[8];
    if constexpr (s < 22) need<s, s>(af[s]);
    if constexpr (h >= -1 && h < 7) { need<s, s + 2>(af[s + 2]); d[(h + 1) & 1] = mfma32(kf[h + 1], as_bf(af[s + 2]), (f32x16){}); pin(d[(h + 1) & 1]); }
    if constexpr (at) { f = mul1(sc, __builtin_amdgcn_rcpf(d[h & 1][8] + 1e-6f)); sfence(); }
    acc[1] = mfma32(wf[s % 3][1], as_bf(af[s]), acc[1]); pin(acc[1]);
    if (s + 2 < 30) ldw(s + 2, 1);
    if constexpr (at) { for (int i = 0; i < 4; ++i) m[i] = mul1(d[h & 1][i], f); sfence(); }
    acc[2] = mfma32(wf[s % 3][2], as_bf(af[s]), acc[2]); pin(acc[2]);
    if (s + 2 < 30) ldw(s + 2, 2);
    if constexpr (at) { for (int i = 4; i < 8; ++i) m[i] = mul1(d[h & 1][i], f); keep(d[h & 1]); sfence(); }
    acc[3] = mfma32(wf[s % 3][3], as_bf(af[s]), acc[3]); pin(acc[3]);
    if (s + 2 < 30) ldw(s + 2, 3);
    if constexpr (s < 16) { acc[0] = mfma32(wf[s % 3][0], as_bf(af[s]), acc[0]); pin(acc[0]); }
    if constexpr (s < 14) ldw(s + 2, 0);
    if constexpr (at) { af[s + 1] = (u32x4){pack2(m[0], m[1]), pack2(m[2], m[3]), pack2(m[4], m[5]), pack2(m[6], m[7])}; keep(af[s + 1]); sfence(); }
  });
  STAMP(10);
  bf16x8 w0[6];
  auto ld0 = [&](int s) { w0[s % 6] = lds_tr<2048>(s < 22 ? wm[0] : wl[0], (s < 22 ? s - 16 : s - 22) * 4096); sfence(); };
#pragma unroll
  for (int s = 16; s < 21; ++s) ld0(s);
  const u32 xb = WS_X + ((rt * 4 + ct) * 4 + w) * 32768;
#pragma unroll
  for (int i = 1; i < 4; ++i)
#pragma unroll
    for (int k = 0; k < 2; ++k)
      store_dev((u32x4){pack2(acc[i][8 * k], acc[i][8 * k + 1]), pack2(acc[i][8 * k + 2], acc[i][8 * k + 3]), pack2(acc[i][8 * k + 4], acc[i][8 * k + 5]),
                        pack2(acc[i][8 * k + 6], acc[i][8 * k + 7])}, srd_ws, lane * 16, xb + ((kq ^ i) * 4 + kq) * 2048 + k * 1024);
  xr0 = *(const GAS u32x4*)(p.x + oo); xr1 = *(const GAS u32x4*)(p.x + oo + 32);
  sfence();
#pragma unroll
  for (int s = 16; s < 30; ++s) {
    acc[0] = mfma32(w0[s % 6], as_bf(af[s]), acc[0]); pin(acc[0]);
    if (s + 5 < 30) ld0(s + 5);
  }
  wait_vm<2>();
  const u32 xf = WS_XFL + ((rt * 4 + ct) * 4 + w) * 512;
  store_one((u32x4){(u32)tag, (u32)(tag >> 32), 0u, 0u}, srd_ws, xf + kq * 128);
  STAMP(11);
  poll4(p.ws + xf, tag, SPIN_CAP);
  STAMP(12);
  u32x4 pp[3][2];
#pragma unroll
  for (int i = 0; i < 3; ++i) {
    pp[i][0] = __builtin_amdgcn_raw_buffer_load_b128(rws, lane * 16, xb + (kq * 4 + (kq ^ (i + 1))) * 2048, 16);
    pp[i][1] = __builtin_amdgcn_raw_buffer_load_b128(rws, lane * 16 + 1024, xb + (kq * 4 + (kq ^ (i + 1))) * 2048, 16);
  }
  sfence();
  float v[16];
#pragma unroll
  for (int e = 0; e < 16; ++e) v[e] = acc[0][e];
#pragma unroll
  for (int i = 0; i < 3; ++i)
#pragma unroll
    for (int e = 0; e < 8; ++e) { v[2 * e] += lo_f(pp[i][e >> 2][e & 3]); v[2 * e + 1] += hi_f(pp[i][e >> 2][e & 3]); }
  const float rs = rlv * LN2;
#pragma unroll
  for (int k = 0; k < 2; ++k) {
#pragma unroll
    for (int e = 0; e < 4; ++e) {
      auto sw = __builtin_amdgcn_permlane32_swap(asu(v[8 * k + e]), asu(v[8 * k + 4 + e]), false, false);
      v[8 * k + e] = asf(sw[0]); v[8 * k + 4 + e] = asf(sw[1]);
    }
    const u32x4 xr = k ? xr1 : xr0;
    u32x4 o;
#pragma unroll
    for (int e = 0; e < 4; ++e) o[e] = pack2(__builtin_fmaf(v[8 * k + 2 * e], rs, lo_f(xr[e])), __builtin_fmaf(v[8 * k + 2 * e + 1], rs, hi_f(xr[e])));
    *(GAS u32x4*)(p.out + oo + k * 32) = o;
  }
  if (b == 0 && w == 0) store_one((u32x4){(u32)tag, (u32)(tag >> 32), 0u, 0u}, srd_ws, WS_EPOCH);
  STAMP(13);
}

__global__ __launch_bounds__(512, 2) void fused_block(const char* __restrict__ x, const char* __restrict__ wqkv, const char* __restrict__ w1,
                                                      char* ws, const char* __restrict__ w2, const char* __restrict__ wo, char* __restrict__ out) {
  __shared__ __attribute__((aligned(16))) char lds_all[L_TOTAL];
  u32 stamp[32] = {};
  u64 pc;
  asm volatile("s_getpc_b64 %0" : "=s"(pc) : "s"((u32)(size_t)lds_all));
  STAMP(0);
  const u32 w = __builtin_amdgcn_readfirstlane(__builtin_amdgcn_workitem_id_x() >> 6);
  {
    u32 d0, d1, d2, d3;
    const u64 cp = (pc & ~127ull) + umin((__builtin_amdgcn_workgroup_id_x() >> 3) * 8 + w * 2, CODE_LINES - 2) * 128;
    asm volatile("s_cmp_gt_u32 %5, 3\n\ts_cbranch_scc1 1f\n\ts_load_dword %0, %4, 0\n\ts_load_dword %1, %4, 64\n\ts_load_dword %2, %4, 128\n\t"
                 "s_load_dword %3, %4, 192\n\ts_waitcnt lgkmcnt(0)\n1:"
                 : "=&s"(d0), "=&s"(d1), "=&s"(d2), "=&s"(d3) : "s"(cp), "s"(w) : "memory", "scc");
  }
  asm volatile("" ::"s"(x), "s"(wqkv), "s"(w1), "s"(ws), "s"(w2), "s"(wo), "s"(out));
  const Ptrs p = {x, wqkv, w1, ws, w2, wo, out};
  if (w < 4) group0(p, opaque_s(__builtin_amdgcn_workgroup_id_x()), opaque_v(__builtin_amdgcn_workitem_id_x()) & 63, w, stamp);
  else group1(p, __builtin_amdgcn_workgroup_id_x(), __builtin_amdgcn_workitem_id_x() & 63, w, stamp);
}

#if defined(__HIP_DEVICE_COMPILE__)
#pragma clang attribute pop
#endif

extern "C" void kernel_launch(void* const* d_in, const int* in_sizes, int n_in, void* d_out, int out_size, void* d_ws, size_t ws_size,
                              hipStream_t stream) {
  fused_block<<<256, 512, 0, stream>>>((const char*)d_in[0], (const char*)d_in[1], (const char*)d_in[4], (char*)d_ws, (const char*)d_in[5],
                                       (const char*)d_in[2], (char*)d_out);
}
```

```cpp
#include <hip/hip_runtime.h>
#include <hip/hip_bf16.h>
#include <type_traits>

#if defined(__HIP_DEVICE_COMPILE__)
#pragma clang attribute push(__attribute__((target("no-packed-fp32-ops"))), apply_to = function)
#endif
typedef unsigned u32;
typedef unsigned long long u64;
typedef __bf16 bf16x8 __attribute__((ext_vector_type(8)));
typedef __bf16 bf16x2 __attribute__((ext_vector_type(2)));
typedef short s16x4 __attribute__((ext_vector_type(4)));
typedef short s16x2 __attribute__((ext_vector_type(2)));
typedef float f32x16 __attribute__((ext_vector_type(16)));
typedef float f32x4 __attribute__((ext_vector_type(4)));
typedef float f32x2 __attribute__((ext_vector_type(2)));
typedef u32 u32x4 __attribute__((ext_vector_type(4)));
typedef u32 u32x2 __attribute__((ext_vector_type(2)));
#define LAS __attribute__((address_space(3)))
#define GAS __attribute__((address_space(1)))
#define DEV __device__ __forceinline__
#define RLX_AGENT __ATOMIC_RELAXED, __HIP_MEMORY_SCOPE_AGENT
#define LDSP(T, a) ((LAS T*)(size_t)(a))

#define ROT1 1
#define PF 1
#define QWIN 3
#define AWIN 5
#define DLY 0
#define SLP 2
#define PSC "sc1"
#define SL 8
#define SO 12
#define YPRIO 0
#define PFMODE 0
#define GATE 1
#define KVMODE 6
#define STATST 0
#define SPINSLP "s_sleep 1\n\t"
#define LORD 2
#define GST 5
#define YW2 1
#define YBAR 1
#define KVAFTER 2
#define ZDLY 16
#define RSLP 2
#define XHOLD 2
#define KPRIO 0
#define GPRIO 1
#define G2EARLY 0
#define KSLP 8
#define KGATE 1
#define PRIO2 1

constexpr u32 WS_EPOCH = 0;
constexpr u32 WS_STATS = 4096;
constexpr u32 WS_FLAGS = WS_STATS + 16384;
constexpr u32 WS_XFL = WS_FLAGS + 32 * 48 * 128;
constexpr u32 WS_KVF = WS_XFL + 1024 * 128;
constexpr u32 WS_KVZ = WS_KVF + 32768;
constexpr u32 WS_ZP = WS_KVZ + 2048;
constexpr u32 WS_KVP = WS_ZP + 65536;
constexpr u32 KVH_B = 32768;
constexpr u32 WS_CS = 3u << 19;
constexpr u32 WS_A = 2u << 20;
constexpr u32 NSTEP = 118;
constexpr u32 WS_X = WS_A + 64 * NSTEP * 1024;
constexpr u32 WS_END = WS_X + 4096 * 2048;
constexpr u32 WS_STAMP = 32u << 20;
static_assert(WS_KVP + 32 * KVH_B <= WS_CS && WS_CS + 524288 <= WS_A, "ws map");
constexpr u32 OOB = 0x40000000u;

constexpr u32 L_WE = 65536;
constexpr u32 L_SCR = 131072;
constexpr u32 L_CORRA = L_SCR;
constexpr u32 L_CORRB = L_SCR + 4096;
constexpr u32 L_MN = L_SCR + 6144;
constexpr u32 L_RL = L_SCR + 6656;
constexpr u32 L_SYNC = L_SCR + 7168;
constexpr u32 L_JUNK = L_SCR + 7424;
constexpr u32 L_WL = L_SCR + 8192;
constexpr u32 L_KVF = 32768;
constexpr u32 L_FINE = 49152;
constexpr u32 L_TOTAL = 163840;

constexpr u32 CODE_LINES = 192;
constexpr u32 SPIN_CAP = 1u << 18;
constexpr u32 HINT_CAP = 1u << 12;
constexpr float LOG2E = 1.4426950408889634f, LN2 = 0.6931471805599453f;

DEV float asf(u32 v) { return __builtin_bit_cast(float, v); }
DEV u32 asu(float v) { return __builtin_bit_cast(u32, v); }
DEV bool all_lanes(bool v) { return __builtin_amdgcn_ballot_w64(!v) == 0; }
DEV u32 umin(u32 a, u32 b) { return a < b ? a : b; }
DEV void sfence() { __builtin_amdgcn_sched_barrier(0); }
template <int N> DEV void wait_vm() { asm volatile("s_waitcnt vmcnt(%0)" ::"i"(N) : "memory"); }
DEV void wait_lgkm() { sfence(); __builtin_amdgcn_s_waitcnt(0xC07F); asm volatile("" ::: "memory"); sfence(); }
DEV void barrier() { sfence(); __builtin_amdgcn_s_waitcnt(0xC07F); asm volatile("s_barrier" ::: "memory"); sfence(); }

DEV u32x4 make_srd(const void* p, u32 bytes) {
  u64 a = (u64)p;
  return (u32x4){(u32)a, (u32)(a >> 32) & 0xffffu, bytes, 0x00020000u};
}
template <int POL> DEV void dma(u32x4 srd, u32 voff, u32 soff, u32 ldsaddr) {
  if (POL == 1)
    asm volatile("s_mov_b32 m0, %3\n\ts_nop 0\n\tbuffer_load_dwordx4 %0, %1, %2 offen nt lds" ::"v"(voff), "s"(srd), "s"(soff), "s"(ldsaddr) : "memory");
  else if (POL == 2)
    asm volatile("s_mov_b32 m0, %3\n\ts_nop 0\n\tbuffer_load_dwordx4 %0, %1, %2 offen sc1 lds" ::"v"(voff), "s"(srd), "s"(soff), "s"(ldsaddr) : "memory");
  else
    asm volatile("s_mov_b32 m0, %3\n\ts_nop 0\n\tbuffer_load_dwordx4 %0, %1, %2 offen lds" ::"v"(voff), "s"(srd), "s"(soff), "s"(ldsaddr) : "memory");
}
template <int POL> DEV void dma4(u32x4 srd, u32x4 v, u32 soff, u32 ldsaddr) {
  if (POL == 1)
    asm volatile("s_mov_b32 m0, %6\n\ts_nop 0\n\tbuffer_load_dwordx4 %0, %4, %5 offen nt lds\n\tbuffer_load_dwordx4 %1, %4, %5 offen offset:1024 nt lds\n\t"
                 "buffer_load_dwordx4 %2, %4, %5 offen offset:2048 nt lds\n\tbuffer_load_dwordx4 %3, %4, %5 offen offset:3072 nt lds"
                 ::"v"(v.x), "v"(v.y), "v"(v.z), "v"(v.w), "s"(srd), "s"(soff), "s"(ldsaddr) : "memory");
  else
    asm volatile("s_mov_b32 m0, %6\n\ts_nop 0\n\tbuffer_load_dwordx4 %0, %4, %5 offen lds\n\tbuffer_load_dwordx4 %1, %4, %5 offen offset:1024 lds\n\t"
                 "buffer_load_dwordx4 %2, %4, %5 offen offset:2048 lds\n\tbuffer_load_dwordx4 %3, %4, %5 offen offset:3072 lds"
                 ::"v"(v.x), "v"(v.y), "v"(v.z), "v"(v.w), "s"(srd), "s"(soff), "s"(ldsaddr) : "memory");
}
DEV void dma2(u32x4 srd, u32 v0, u32 v1, u32 soff, u32 ldsaddr) {
  asm volatile("s_mov_b32 m0, %4\n\ts_nop 0\n\tbuffer_load_dwordx4 %0, %2, %3 offen lds\n\tbuffer_load_dwordx4 %1, %2, %3 offen offset:1024 lds"
               ::"v"(v0), "v"(v1), "s"(srd), "s"(soff), "s"(ldsaddr) : "memory");
}
DEV void touch64(u32x4 srd, u32 voff, u32 soff, u32 ldsaddr) {
  asm volatile("s_mov_b32 m0, %3\n\ts_nop 0\n\tbuffer_load_dword %0, %1, %2 offen lds" ::"v"(voff), "s"(srd), "s"(soff), "s"(ldsaddr) : "memory");
}
DEV u32x4 steps4(u32 v, u32 step) { step -= 1024; return (u32x4){v, v + step, v + 2 * step, v + 3 * step}; }
DEV void store_one(u32x4 v, u32x4 srd, u32 soff) {
  asm volatile("s_mov_b64 exec, 1\n\tbuffer_store_dwordx4 %0, off, %1, %2 sc1\n\ts_mov_b64 exec, -1\n\ts_nop 0" ::"v"(v), "s"(srd), "s"(soff) : "memory");
}
DEV void store_dev(u32x4 v, u32x4 srd, u32 voff, u32 soff) {
  asm volatile("buffer_store_dwordx4 %0, %1, %2, %3 offen sc1\n\ts_nop 1" ::"v"(v), "v"(voff), "s"(srd), "s"(soff) : "memory");
}
#define LDSO(T, a, o) ((LAS T*)(LDSP(char, a) + (o)))
DEV bf16x8 lds_b128(u32 a, int o = 0) { return *LDSO(bf16x8, a, o); }
template <int D> DEV bf16x8 lds_tr(u32 a, int o) {
  s16x4 lo = __builtin_amdgcn_ds_read_tr16_b64_v4i16(LDSO(s16x4, a, o));
  s16x4 hi = __builtin_amdgcn_ds_read_tr16_b64_v4i16(LDSO(s16x4, a, o + D));
  return __builtin_bit_cast(bf16x8, __builtin_shufflevector(lo, hi, 0, 1, 2, 3, 4, 5, 6, 7));
}
DEV f32x16 mfma32(bf16x8 a, bf16x8 b, f32x16 c) { return __builtin_amdgcn_mfma_f32_32x32x16_bf16(a, b, c, 0, 0, 0); }
DEV f32x4 mfma16(bf16x8 a, bf16x8 b, f32x4 c) { return __builtin_amdgcn_mfma_f32_16x16x32_bf16(a, b, c, 0, 0, 0); }
DEV bf16x8 as_bf(u32x4 v) { return __builtin_bit_cast(bf16x8, v); }
DEV u32 pack2(float a, float b) { return __builtin_bit_cast(u32, __builtin_convertvector((f32x2){a, b}, bf16x2)); }
DEV u32 relu2(u32 v) { return __builtin_bit_cast(u32, __builtin_elementwise_max(__builtin_bit_cast(s16x2, v), (s16x2){0, 0})); }
DEV float lo_f(u32 v) { return asf(v << 16); }
DEV float hi_f(u32 v) { return asf(v & 0xffff0000u); }
DEV float rbf(float f) { return lo_f(pack2(f, 0.f)); }
DEV u32 split_hi_lo(float c) { float h = rbf(c); return pack2(h, c - h); }
DEV float mul1(float a, float b) { float r = a * b; asm("" : "+v"(r)); return r; }
template <int CTRL> DEV float dpp_add(float v) {
  return v + asf(__builtin_amdgcn_update_dpp(0u, asu(v), CTRL, 0xf, 0xf, true));
}
DEV float swap16_add(float v) {
  auto r = __builtin_amdgcn_permlane16_swap(asu(v), asu(v), false, false);
  return asf(r[0]) + asf(r[1]);
}
DEV float swap32_add(float v) {
  auto r = __builtin_amdgcn_permlane32_swap(asu(v), asu(v), false, false);
  return asf(r[0]) + asf(r[1]);
}
DEV float sum32(float v) {
  v = dpp_add<0xB1>(v); v = dpp_add<0x4E>(v); v = dpp_add<0x141>(v); v = dpp_add<0x140>(v);
  return swap16_add(v);
}
DEV float max_eps(float t2) { float m; asm("v_max_f32 %0, %1, %2" : "=v"(m) : "s"(1e-6f * LOG2E), "v"(t2)); return m; }
DEV u32x4 phi8(const float (&t2)[8]) {
  float e[8], m[8];
#pragma unroll
  for (int i = 0; i < 8; ++i) e[i] = __builtin_amdgcn_fmed3f(__builtin_amdgcn_exp2f(t2[i]), 0.f, 1.f);
  sfence();
#pragma unroll
  for (int i = 0; i < 8; ++i) asm("v_max_f32 %0, %1, %2" : "=v"(m[i]) : "s"(1e-6f * LOG2E), "v"(t2[i]));
  sfence();
#pragma unroll
  for (int i = 0; i < 8; ++i) { e[i] = __builtin_fmaf(m[i], LN2, e[i]); asm("" : "+v"(e[i])); }
  sfence();
  return (u32x4){pack2(e[0], e[1]), pack2(e[2], e[3]), pack2(e[4], e[5]), pack2(e[6], e[7])};
}
DEV void lds_signal(u32 addr, u32 inc = 1u) {
  asm volatile("s_mov_b64 exec, 1\n\tds_add_u32 %0, %1\n\ts_mov_b64 exec, -1" ::"v"(addr), "v"(inc) : "memory");
}
DEV void lds_wait(u32 addr, u32 waves) {
  u32 v, s, n = SPIN_CAP;
  asm volatile("1:\n\tds_read_b32 %0, %3\n\ts_waitcnt lgkmcnt(0)\n\tv_readfirstlane_b32 %1, %0\n\ts_sub_u32 %2, %2, 1\n\ts_cbranch_scc1 2f\n\t"
               "s_cmp_lt_u32 %1, %4\n\ts_cbranch_scc1 1b\n2:"
               : "=&v"(v), "=&s"(s), "+s"(n) : "v"(addr), "s"(waves) : "memory", "scc");
}
DEV u64 poll4(const char* p, u64 tag, u32 n) {
  u64 f0, f1, f2, f3;
  asm volatile("1:\n\ts_load_dwordx2 %0, %5, 0 glc\n\ts_load_dwordx2 %1, %5, 128 glc\n\ts_load_dwordx2 %2, %5, 256 glc\n\t"
               "s_load_dwordx2 %3, %5, 384 glc\n\ts_waitcnt lgkmcnt(0)\n\t"
               "s_xor_b64 %0, %0, %6\n\ts_xor_b64 %1, %1, %6\n\ts_xor_b64 %2, %2, %6\n\ts_xor_b64 %3, %3, %6\n\t"
               "s_or_b64 %0, %0, %1\n\ts_or_b64 %2, %2, %3\n\ts_or_b64 %0, %0, %2\n\ts_cbranch_scc0 2f\n\t"
               "s_sub_u32 %4, %4, 1\n\ts_cbranch_scc0 1b\n2:"
               : "=&s"(f0), "=&s"(f1), "=&s"(f2), "=&s"(f3), "+s"(n) : "s"(p), "s"(tag) : "memory", "scc");
  return f0;
}
template <int D> DEV void poll2(const char* p, u64 tag, u64 mask) {
  u64 f0, f1; u32 n = SPIN_CAP;
  asm volatile("1:\n\ts_load_dwordx2 %0, %3, 0 glc\n\ts_load_dwordx2 %1, %3, %6 glc\n\ts_waitcnt lgkmcnt(0)\n\t"
               "s_xor_b64 %0, %0, %4\n\ts_xor_b64 %1, %1, %4\n\ts_or_b64 %0, %0, %1\n\ts_and_b64 %0, %0, %5\n\ts_cbranch_scc0 2f\n\t"
               "s_sub_u32 %2, %2, 1\n\ts_cbranch_scc0 1b\n2:"
               : "=&s"(f0), "=&s"(f1), "+s"(n) : "s"(p), "s"(tag), "s"(mask), "i"(D) : "memory", "scc");
}
DEV u32x4 load_dev_wait(u32x4 srd, u32 voff, u32 soff) {
  u32x4 v;
  asm volatile("buffer_load_dwordx4 %0, %1, %2, %3 offen sc1\n\ts_waitcnt vmcnt(0)" : "=&v"(v) : "v"(voff), "s"(srd), "s"(soff) : "memory");
  return v;
}
template <int N, int I = 0, class F> DEV void static_for(F&& f) {
  if constexpr (I < N) { f(std::integral_constant<int, I>{}); static_for<N, I + 1>(f); }
}
constexpr int ops_upto(int t) { return 8 + 2 * (t < 4 ? t + 1 : 4) + (t >= SL ? 6 : 0); }
template <int T, int K> DEV void need(u32x4& v) {
  asm volatile("s_waitcnt vmcnt(%1)" : "+v"(v) : "i"(ops_upto(T) - K - 1));
}
template <class T> DEV void keep(T& v) { asm volatile("" : "+v"(v)); }
template <class T> DEV T opaque_v(T v) { asm volatile("" : "+v"(v)); return v; }
template <class T> DEV T opaque_s(T v) { asm volatile("" : "+s"(v)); return v; }

#define STAMP(i)
#define FSTAMP(w, i)

struct Ptrs { const char *x, *wqkv, *w1; char* ws; const char *w2, *wo; char* out; };

DEV void group1(const Ptrs& p, u32 b, u32 lane, u32 w, u32* stamp) {
  const u32 xcd = b & 7, r = b >> 3, j = w & 3;
  const u32 rt = (xcd & 1) * 8 + (r & 7);
  const u32 rot = ROT1 == 0 ? 0 : ROT1 == 1 ? (r & 7) : (r & 7) + 2 * (r >> 3);
  const u32x4 srd_x = make_srd(p.x, 2048 * 1024);
  const u32 vx = (lane >> 3) * 1024 + (((lane & 7) ^ (lane >> 4)) << 4);
  const u32x4 vx4 = {vx, (vx ^ 64) + 7168, vx + 14336, (vx ^ 64) + 21504};
  auto issue_x = [&](int st) { dma4<0>(srd_x, vx4, (rt * 128 + 32 * j) * 1024 + ((st + rot) & 7) * 128, (st & 3) * 32768 + j * 4096); };
  issue_x(0);
  sfence();
  const u32 c4 = (xcd >> 1) * 4 + (r >> 3), g = c4 >> 1;
  const bool xt = (c4 & 1) == 0;
  const u32 hh = lane >> 5, q = (lane & 15) >> 2;
  const u32 wst = xt ? 3072 : 2752;
  const u32x4 srd_w = make_srd(xt ? p.wqkv : p.w1, 512 * wst);
  const u32 ym = xt ? 0u : ~0u, l4 = lane >> 4;
  const u32 blk = ((q ^ l4) + (((q & l4) << 1) & ym)) & 3;
  const u32x4 vwa4 = steps4(l4 * wst + (lane & 3) * 16 + (blk & 1) * 64 + (blk >> 1) * (xt ? 1024 : 128) + (xt ? 1024 + g * 128 : g * 256), 4 * wst);
  auto issue_w = [&](int st) { dma4<0>(srd_w, vwa4, (((st + rot) & 7) * 64 + 16 * j) * wst, (st & 3) * 32768 + 16384 + j * 4096); };
  issue_w(0);
  const u32 row = rt * 128 + c4 * 8 + 2 * j;
  u32x4 a0, a1;
  asm volatile("global_load_dwordx4 %0, %2, %3\n\tglobal_load_dwordx4 %1, %2, %3 offset:512"
               : "=&v"(a0), "=&v"(a1) : "v"((row + hh) * 1024 + (lane & 31) * 16), "s"(p.x) : "memory");
  issue_x(1); issue_w(1);
  sfence();
  u64 tag;
  asm volatile("s_load_dwordx2 %0, %1, 0\n\ts_waitcnt lgkmcnt(0)" : "=&s"(tag) : "s"(p.ws) : "memory");
  tag += 0x9E3779B97F4A7C15ull;
  const u32 nl = (xt || (g < 6 && !(g & 1))) ? 2 : 1, lstep = xt ? 86 + g * 4 : 64 + (g >> 1) * 6 + ((g & 1) ? (g < 6 ? 4 : 2) : 0);
  const u32x4 vwb4 = steps4((lane >> 3) * wst + ((((lane & 7) ^ ((lane >> 2) & 4)) << 4) & (nl == 2 ? 127 : 48)) + (xt ? g * 128 : lstep * 32), 8 * wst);
  auto issue = [&](int st, int part) {
    if (st < 8) { if (part) issue_w(st); else issue_x(st); }
    else dma4<0>(srd_w, vwb4, ((((st - 8) * 4 + j + rot) & 7) * 64 + 32 * part) * wst, (st - 8) * 32768 + j * 8192 + part * 4096);
  };
  dma2(srd_w, vwb4.x, vwb4.y, ((rot & 7) * 64 + 16 * j) * wst, L_WL + j * 2048);
  const u32x4 srd_pf = j == 3 ? make_srd(p.wo, 512 * 1024) : make_srd(p.w2, 1376 * 1024);
  const u32 vpf = j == 3 ? lane * 1024 : lane < (j == 2 ? 24 : 32) ? lane * 128 : OOB;
  const u32 spf = j < 2 ? (c4 >> 2) * 262144 + r * 8192 + j * 4096 : j == 2 ? (64 + 6 * (c4 >> 2)) * 16384 + r * 3072 : (r & 7) * 65536 + (c4 & 3) * 256 + ((c4 >> 2) & 1) * 128;
  const u32 pfst = 2 + (r & 3);
  const u32x4 srd_ws = make_srd(p.ws, WS_END);
  const u32 xa = (j * 32 + (lane & 31)) * 128 + ((hh ^ ((lane >> 1) & 7)) << 4);
  const u32 wb0 = opaque_v(hh * 1024 + q * 128 + (lane & 16) * 2 + (lane & 3) * 8 + ((q & 2) << 5)), wb1 = opaque_v(wb0 ^ 64);
  const u32 kq = c4 >> 2, cb = (c4 & 3) * 256;
  const u32x4 vw2 = steps4(l4 * 1024 + (lane & 3) * 16 + ((q ^ l4) << 6), 4096);
  const u32 csb = WS_CS + (c4 * 2 + (xcd & 1)) * 16384 + j * 512;
  bf16x8 xf[32];
  u32x4 gr[5];
  u32 mn;
  float rl;
#define GRAN_LOADS "buffer_load_dwordx4 %0, %5, %7, %8 offen sc1\n\tbuffer_load_dwordx4 %1, %5, %7, %8 offen offset:2048 sc1\n\t" \
                   "buffer_load_dwordx4 %2, %5, %7, %9 offen sc1\n\tbuffer_load_dwordx4 %3, %5, %7, %9 offen offset:2048 sc1\n\tbuffer_load_dwordx4 %4, %6, %7, %10 offen sc1"
#define GRAN_OPS : "=&v"(gr[0]), "=&v"(gr[1]), "=&v"(gr[2]), "=&v"(gr[3]), "=&v"(gr[4]) \
                 : "v"(hh * 8192 + (lane & 31) * 16), "v"(((lane & 31) >> 1) * 16), "s"(srd_ws), "s"(csb), "s"(csb + 4096), "s"(WS_STATS + rt * 1024 + j * 256) : "memory"
#pragma unroll
  for (int st = 0; st < 8; ++st) {
    const int n = st + 2;
    if (st == 0) wait_vm<10>(); else if (st == 1 || st == 2) wait_vm<9>(); else wait_vm<8>();
    barrier();
    const u32 slot = (st & 3) * 32768;
    if (st == GST) asm volatile(GRAN_LOADS GRAN_OPS);
    if (PF && st >= 2 && st < 6 && st == pfst) touch64(srd_pf, vpf, spf, L_JUNK);
    issue(n, 0);
    sfence();
    bf16x8 cw[8];
    if (st == 1) {
      const u32 wa = 16384 + (8 * hh + q) * 256 + (lane & 16) * 2 + (lane & 3) * 8 + ((xt ? (j ^ q) : ((j - q) & 3)) << 6), wc = L_WL + (wb0 ^ ((j & 1) << 6));
#pragma unroll
      for (int s = 0; s < 4; ++s) { cw[s] = lds_tr<1024>(wa, s * 4096); cw[4 + s] = lds_tr<512>(wc, s * 2048); }
    }
#pragma unroll
    for (int s = 0; s < 4; ++s) xf[st * 4 + s] = lds_b128((xa ^ (s << 5)) + (slot & 65536), slot & 32768);
    sfence();
    issue(n, 1);
    sfence();
    if (st == 0) {
      __builtin_amdgcn_s_setprio(3);
      asm volatile("" : "+v"(a0), "+v"(a1));
    float v[16];
#pragma unroll
    for (int i = 0; i < 4; ++i) { v[2 * i] = lo_f(a0[i]); v[2 * i + 1] = hi_f(a0[i]); v[8 + 2 * i] = lo_f(a1[i]); v[9 + 2 * i] = hi_f(a1[i]); }
    float s[4] = {};
#pragma unroll
    for (int i = 0; i < 16; ++i) s[i & 3] += v[i];
    const float mean = sum32((s[0] + s[1]) + (s[2] + s[3])) * (1.f / 512.f);
    float qq[4] = {};
#pragma unroll
    for (int i = 0; i < 16; ++i) { float d = v[i] - mean; qq[i & 3] = __builtin_fmaf(d, d, qq[i & 3]); }
    const float var = sum32((qq[0] + qq[1]) + (qq[2] + qq[3])) * (1.f / 512.f);
    const u32 pm = pack2(mean, __builtin_amdgcn_sqrtf(rbf(rbf(var) + 1e-5f)));
    const u32 p1 = __builtin_amdgcn_readlane(pm, 32);
    store_one(opaque_v((u32x4){pm, (u32)tag, p1, (u32)(tag >> 32)}), srd_ws, WS_STATS + row * 8);
      *LDSP(u32x2, L_SYNC + 40) = (u32x2){(u32)tag, (u32)(tag >> 32)};
      __builtin_amdgcn_s_setprio(0);
    }
    if (st == 1) {
      const u32 selv = (((lane >> 4) ^ (lane >> 2)) & 1) ? 0u : 0x3f803f80u;
      const bf16x8 sel = as_bf((u32x4){selv, selv, selv, selv});
      f32x4 ca = {}, cb2 = {};
      __builtin_amdgcn_s_setprio(3);
#pragma unroll
      for (int s = 0; s < 4; ++s) { ca = mfma16(sel, cw[s], ca); cb2 = mfma16(sel, cw[4 + s], cb2); }
      __builtin_amdgcn_s_setprio(0);
      store_dev((u32x4){split_hi_lo(ca[0]), (u32)tag, split_hi_lo(cb2[0]), (u32)(tag >> 32)}, srd_ws, hh ? OOB : lane * 16, csb + (r & 7) * 2048);
    }
    if (st == GST + 1) {
      asm volatile("" : "+v"(gr[0]), "+v"(gr[1]), "+v"(gr[2]), "+v"(gr[3]), "+v"(gr[4]));
#pragma unroll 1
      for (u32 i = 0; i < SPIN_CAP; ++i) {
        u32 bad = 0;
#pragma unroll
        for (int k = 0; k < 5; ++k) bad |= (gr[k].y ^ (u32)tag) | (gr[k].w ^ (u32)(tag >> 32));
        if (all_lanes(bad == 0)) break;
        __builtin_amdgcn_s_sleep(8);
        asm volatile(GRAN_LOADS "\n\ts_waitcnt vmcnt(0)" GRAN_OPS);
      }
      *LDSP(u32x4, L_CORRA + j * 1024 + lane * 16) = (u32x4){gr[0].x, gr[1].x, gr[2].x, gr[3].x};
      *LDSP(u32x4, L_CORRB + (j & 1) * 1024 + lane * 16) = (u32x4){gr[0].z, gr[1].z, gr[2].z, gr[3].z};
      const u32 ms = (lane & 1) ? gr[4].z : gr[4].x;
      mn = ((ms & 0xffffu) ^ 0x8000u) * 0x10001u;
      rl = LOG2E * __builtin_amdgcn_rcpf(hi_f(ms));
      *LDSP(u32, (hh ? L_RL : L_MN) + (j * 32 + (lane & 31)) * 4) = hh ? asu(rl) : mn;
    }
    sfence();
  }
  wait_vm<8>();
  __builtin_amdgcn_s_setprio(1);
  barrier();
  STAMP(2);
  {
    f32x16 acc[2];
    bf16x8 wf[8];
    const u32 arow = WS_A + ((rt * 4 + j) * NSTEP + lstep) * 1024, l16 = lane * 16;
    auto ldw = [&](int s) { wf[s & 7] = lds_tr<512>(s >= 32 ? wb1 : wb0, (s & 31) * 2048); sfence(); };
    wf[6] = lds_b128(L_CORRB + l16); wf[7] = lds_b128(L_CORRB + l16, 1024);
#pragma unroll
    for (int s = 0; s < 6; ++s) ldw(s);
    dma4<0>(make_srd(p.w2, 1376 * 1024), vw2, (16 * kq + j) * 16384 + cb, L_WE + j * 4096);
    dma4<0>(make_srd(p.w2, 1376 * 1024), vw2, (16 * kq + 4 + j) * 16384 + cb, L_WE + (4 + j) * 4096);
    STAMP(8);
    const bf16x8 mf = as_bf((u32x4){mn, mn, mn, mn});
    acc[0] = mfma32(wf[6], mf, (f32x16){}); acc[1] = mfma32(wf[7], mf, (f32x16){});
    keep(acc[0]); keep(acc[1]);
    sfence();
#pragma unroll
    for (int s = 0; s < 32; ++s) {
      acc[0] = mfma32(wf[s & 7], xf[s], acc[0]); keep(acc[0]); sfence();
      if (s == 10) asm volatile("s_waitcnt vmcnt(8)\n\ts_barrier" ::: "memory");
      ldw(s + 6);
    }
    STAMP(12);
    auto relu_store = [&](const f32x16& a, int h) {
      u32x4 o;
#pragma unroll
      for (int i = 0; i < 4; ++i) o[i] = relu2(pack2(a[8 * (h & 1) + 2 * i], a[8 * (h & 1) + 2 * i + 1]));
      store_dev(o, srd_ws, l16, arow + h * 1024);
    };
    if (xt) {
      wait_vm<0>();
      lds_signal(L_SYNC + 8);
      float t[16], e[16];
      u32x4 o[2];
      static_for<32>([&](auto S_) {
        constexpr int s = S_;
        acc[1] = mfma32(wf[(32 + s) & 7], xf[s], acc[1]); keep(acc[1]); sfence();
        if constexpr (s + 6 < 32) ldw(32 + s + 6);
        static_for<3>([&](auto K_) {
          constexpr int k = (s - 2) * 3 + K_;
          if constexpr (k >= 0 && k < 16) t[k] = mul1(acc[0][k], rl);
          else if constexpr (k >= 16 && k < 32) { e[k - 16] = __builtin_amdgcn_fmed3f(__builtin_amdgcn_exp2f(t[k - 16]), 0.f, 1.f); keep(e[k - 16]); }
          else if constexpr (k >= 32 && k < 48) t[k - 32] = max_eps(t[k - 32]);
          else if constexpr (k >= 48 && k < 64) { e[k - 48] = __builtin_fmaf(t[k - 48], LN2, e[k - 48]); keep(e[k - 48]); }
          else if constexpr (k >= 64 && k < 72) { u32 v = pack2(e[2 * (k - 64)], e[2 * (k - 64) + 1]); keep(v); o[(k - 64) >> 2][(k - 64) & 3] = v; }
        });
        if constexpr (s == 27 || s == 28) store_dev(o[s - 27], srd_ws, l16, arow + (s - 27) * 1024);
        sfence();
      });
      STAMP(3);
      __builtin_amdgcn_s_setprio(3);
#pragma unroll
      for (int h = 2; h < 4; ++h) {
        float t2[8];
#pragma unroll
        for (int i = 0; i < 8; ++i) t2[i] = mul1(acc[1][8 * (h & 1) + i], rl);
        sfence();
        store_dev(phi8(t2), srd_ws, l16, arow + h * 1024);
      }
    } else if (nl == 2) {
#pragma unroll
      for (int s = 0; s < 32; ++s) {
        acc[1] = mfma32(wf[(32 + s) & 7], xf[s], acc[1]); keep(acc[1]); sfence();
        if (s + 6 < 32) ldw(32 + s + 6);
        if (s == 27 || s == 29) relu_store(acc[0], (s - 27) >> 1);
        sfence();
      }
      STAMP(3);
      __builtin_amdgcn_s_setprio(3);
      relu_store(acc[1], 2); relu_store(acc[1], 3);
    } else {
      STAMP(3);
      __builtin_amdgcn_s_setprio(3);
      relu_store(acc[0], 0); relu_store(acc[0], 1);
    }
  }
  STAMP(4);
  const u32 th = kq >> 1, reg = j * 16384;
  const u32x4 vo = steps4((lane >> 3) * 1024 + (((lane & 7) ^ ((lane >> 2) & 4)) << 4), 8192);
  auto wo_chunk = [&](int i) { dma4<0>(make_srd(p.wo, 512 * 1024), vo, (8 * j + 2 * i) * 16384 + cb + (kq & 1) * 128, reg + i * 4096); };
  lds_signal(L_SYNC);
  lds_wait(L_SYNC, 4);
  wo_chunk(0); wo_chunk(1);
  wait_vm<8>();
  store_one((u32x4){(u32)tag, (u32)(tag >> 32), 0u, 0u}, srd_ws, WS_FLAGS + ((rt * 2 + (j >> 1)) * 48 + (xt ? 32 : 16) + g * 2 + (j & 1)) * 128);
  if (!xt) lds_signal(L_SYNC + 8);
  STAMP(5);
  {
    const bool kvjob = ((rt ^ j) & 1) == 0, job = xt && (kvjob || rt == 1);
    const u32 hd = g * 4 + j;
    const u32 sa = !job ? OOB : kvjob ? WS_KVP + hd * KVH_B + (rt >> 1) * 4096 : WS_ZP + (hd >> 1) * 4096;
    const u32 fo = kvjob ? WS_KVF + hd * 1024 + (rt >> 1) * 128 : WS_KVZ + hd * 64;
    u32x4 a[4];
#define SLICE_LOADS "buffer_load_dwordx4 %0, %4, %5, %6 offen sc1\n\tbuffer_load_dwordx4 %1, %4, %5, %6 offen offset:1024 sc1\n\t" \
                    "buffer_load_dwordx4 %2, %4, %5, %6 offen offset:2048 sc1\n\tbuffer_load_dwordx4 %3, %4, %5, %6 offen offset:3072 sc1"
#define SLICE_OPS : "=&v"(a[0]), "=&v"(a[1]), "=&v"(a[2]), "=&v"(a[3]) : "v"(lane * 16), "s"(srd_ws), "s"(sa) : "memory"
    asm volatile(SLICE_LOADS SLICE_OPS);
    poll2<256>(p.ws + WS_FLAGS + ((rt * 2 + (j >> 1)) * 48 + 16 + kq * 4 + (j & 1)) * 128, tag, ~0ull);
    *LDSP(u32, L_SYNC + 48 + j * 4) = 1u;
    asm volatile("s_waitcnt vmcnt(0)" : "+v"(a[0]), "+v"(a[1]), "+v"(a[2]), "+v"(a[3]));
    STAMP(6);
    if (job) {
      __builtin_amdgcn_s_setprio(3);
#pragma unroll 1
      for (u32 i = 0; i < SPIN_CAP; ++i) {
        u32 bad = 0;
#pragma unroll
        for (int k = 0; k < 4; ++k) bad |= (a[k].y ^ (u32)tag) | (a[k].w ^ (u32)(tag >> 32));
        if (all_lanes(bad == 0)) break;
        __builtin_amdgcn_s_sleep(RSLP);
        asm volatile(SLICE_LOADS "\n\ts_waitcnt vmcnt(0)" SLICE_OPS);
      }
      float v[4] = {(lo_f(a[0].x) + lo_f(a[1].x)) + (lo_f(a[2].x) + lo_f(a[3].x)), (hi_f(a[0].x) + hi_f(a[1].x)) + (hi_f(a[2].x) + hi_f(a[3].x)),
                    (lo_f(a[0].z) + lo_f(a[1].z)) + (lo_f(a[2].z) + lo_f(a[3].z)), (hi_f(a[0].z) + hi_f(a[1].z)) + (hi_f(a[2].z) + hi_f(a[3].z))};
#pragma unroll
      for (int i = 0; i < 4; ++i) v[i] = dpp_add<0x128>(v[i]);
#pragma unroll
      for (int i = 0; i < 4; ++i) v[i] = swap16_add(v[i]);
#pragma unroll
      for (int i = 0; i < 4; ++i) v[i] = swap32_add(v[i]);
      store_dev((u32x4){pack2(v[0], v[1]), (u32)tag, pack2(v[2], v[3]), (u32)(tag >> 32)}, srd_ws, lane < 8 ? lane * 16 : OOB, fo);
    }
  }
  __builtin_amdgcn_s_setprio(0);
  STAMP(7);
  wo_chunk(2); wo_chunk(3);
  poll4(p.ws + WS_FLAGS + ((rt * 2 + th) * 48 + 32 + j * 4) * 128, tag, SPIN_CAP);
  STAMP(10);
  const __amdgpu_buffer_rsrc_t rws = __builtin_amdgcn_make_buffer_rsrc(p.ws, 0, WS_END, 0x00020000);
  u32x4 pq[16], kv[8], z;
#pragma unroll
  for (int u = 0; u < 16; ++u) pq[u] = __builtin_amdgcn_raw_buffer_load_b128(rws, lane * 16, WS_A + ((rt * 4 + th * 2 + (u & 1)) * NSTEP + 86 + 8 * j + (u >> 1)) * 1024, 16);
  sfence();
  lds_wait(L_SYNC + 12, 4);
  STAMP(11);
#pragma unroll 1
  for (u32 i = 0; i < SPIN_CAP; ++i) {
    asm volatile("" ::: "memory");
    z = __builtin_amdgcn_raw_buffer_load_b128(rws, (lane & 31) * 16, WS_KVZ + j * 512, 16);
#pragma unroll
    for (int h = 0; h < 8; ++h) kv[h] = __builtin_amdgcn_raw_buffer_load_b128(rws, lane * 16, WS_KVF + (j * 8 + h) * 1024, 16);
    u32 bad = (z.y ^ (u32)tag) | (z.w ^ (u32)(tag >> 32));
#pragma unroll
    for (int h = 0; h < 8; ++h) bad |= (kv[h].y ^ (u32)tag) | (kv[h].w ^ (u32)(tag >> 32));
    if (all_lanes(bad == 0)) break;
    __builtin_amdgcn_s_sleep(KSLP);
  }
  __builtin_amdgcn_s_waitcnt(0x0F70);
  __builtin_amdgcn_s_setprio(KPRIO);
  STAMP(9);
  {
    const u32 ra0 = opaque_v(reg + hh * 512 + q * 128 + (lane & 16) * 2 + (lane & 3) * 8 + ((q & 2) << 5)), ra1 = opaque_v(ra0 ^ 64);
    bf16x8 wf[3][2];
    auto ldw = [&](int h, int c) { wf[h % 3][c] = lds_tr<1024>(c ? ra1 : ra0, h * 2048); sfence(); };
#pragma unroll
    for (int h = 0; h < 8; ++h) {
      kv[h].y = kv[h].z;
      kv[h].z = __builtin_amdgcn_ds_bpermute(h * 16 + l4 * 4, z.x);
      kv[h].w = __builtin_amdgcn_ds_bpermute(h * 16 + l4 * 4, z.z);
    }
    sfence();
    ldw(0, 0); ldw(0, 1); ldw(1, 0); ldw(1, 1);
    f32x16 acc[2][2] = {}, d[2];
    f32x16 ceps = {0.f, 0.f, 0.f, 0.f, 0.f, 0.f, 0.f, 0.f, 1e-6f, 1e-6f, 1e-6f, 1e-6f, 1e-6f, 1e-6f, 1e-6f, 1e-6f};
    keep(ceps);
#pragma unroll
    for (int h = 0; h < 8; ++h) {
      auto s0 = __builtin_amdgcn_permlane16_swap(kv[h].x, kv[h].z, false, false);
      auto s1 = __builtin_amdgcn_permlane16_swap(kv[h].y, kv[h].w, false, false);
      kv[h] = (u32x4){s0[0], s1[0], s0[1], s1[1]};
      keep(kv[h]);
    }
    sfence();
    d[0] = mfma32(as_bf(kv[0]), as_bf(pq[0]), ceps); keep(d[0]); sfence();
    static_for<17>([&](auto U_) {
      constexpr int u = U_, v = u - 1;
      float f, m[8];
      if constexpr (u + 1 < 16) { d[(u + 1) & 1] = mfma32(as_bf(kv[(u + 1) >> 1]), as_bf(pq[u + 1]), ceps); keep(d[(u + 1) & 1]); sfence(); }
      if constexpr (u < 16) { f = __builtin_amdgcn_rcpf(d[u & 1][8]); keep(f); sfence(); for (int i = 0; i < 3; ++i) m[i] = mul1(d[u & 1][i], f); sfence(); }
      if constexpr (v >= 0) { acc[v & 1][0] = mfma32(wf[(v >> 1) % 3][0], as_bf(pq[v]), acc[v & 1][0]); keep(acc[v & 1][0]); sfence(); }
      if constexpr (u < 16) { for (int i = 3; i < 8; ++i) m[i] = mul1(d[u & 1][i], f); keep(d[u & 1]); sfence(); }
      if constexpr (v >= 0) { acc[v & 1][1] = mfma32(wf[(v >> 1) % 3][1], as_bf(pq[v]), acc[v & 1][1]); keep(acc[v & 1][1]); sfence(); }
      if constexpr (u < 16) { pq[u] = (u32x4){pack2(m[0], m[1]), pack2(m[2], m[3]), pack2(m[4], m[5]), pack2(m[6], m[7])}; keep(pq[u]); sfence(); }
      if constexpr ((u >> 1) + 2 < 8) ldw((u >> 1) + 2, u & 1);
    });
    STAMP(1);
#pragma unroll
    for (int t = 0; t < 4; ++t)
#pragma unroll
      for (int r4 = 0; r4 < 4; ++r4)
        *LDSO(f32x4, reg + lane * 16, t * 4096 + r4 * 1024) =
            (f32x4){acc[t >> 1][t & 1][4 * r4], acc[t >> 1][t & 1][4 * r4 + 1], acc[t >> 1][t & 1][4 * r4 + 2], acc[t >> 1][t & 1][4 * r4 + 3]};
  }
  barrier();
  STAMP(13);
}

template <bool XT> DEV void group0_first(const Ptrs& p, u32 b, u32 lane, u32 w, u64& tag, u32* stamp) {
  const u32 xcd = b & 7, r = b >> 3, rt = (xcd & 1) * 8 + (r & 7), c4 = (xcd >> 1) * 4 + (r >> 3), g = c4 >> 1;
  const u32 mh = w >> 1, nh = w & 1, hh = lane >> 5, q = (lane & 15) >> 2;
  f32x16 acc[2][2] = {};
  bf16x8 xf[4][2] = {}, wf[4][2] = {};
  u32 xa = (mh * 64 + (lane & 31)) * 128 + ((hh ^ ((lane >> 1) & 7)) << 4);
  u32 wa = 16384 + (8 * hh + q) * 256 + (lane & 16) * 2 + (lane & 3) * 8 + ((XT ? (nh ^ q) : ((nh - q) & 3)) << 6), wb = wa ^ 128;
  u32 ca = L_CORRA + nh * 1024 + lane * 16;
  const u32x4 srd_ws = make_srd(p.ws, WS_END);
  u32x4 vw2 = steps4((lane >> 4) * 1024 + (lane & 3) * 16 + ((q ^ (lane >> 4)) << 6), 4096);
  auto wlate = [&]() {
    dma4<0>(make_srd(p.w2, 1376 * 1024), vw2, (64 + 6 * (c4 >> 2) + w) * 16384 + (c4 & 3) * 256, L_WL + w * 4096);
    if (w < 2) dma4<0>(make_srd(p.w2, 1376 * 1024), vw2, (68 + 6 * (c4 >> 2) + w) * 16384 + (c4 & 3) * 256, L_WL + (4 + w) * 4096);
  };
  auto wchunk = [&](int i) { dma4<0>(make_srd(p.w2, 1376 * 1024), vw2, (16 * (c4 >> 2) + 4 * i + w) * 16384 + (c4 & 3) * 256, L_WE + (4 * i + w) * 4096); };
  u32 ma0 = L_MN + (mh * 64 + (lane & 31)) * 4;
  *LDSP(u32, L_SYNC + (lane & 31) * 4) = 0u;

  asm volatile("" : "+v"(acc[0][0]), "+v"(acc[0][1]), "+v"(acc[1][0]), "+v"(acc[1][1]), "+v"(vw2));
  asm volatile("" : "+v"(xf[2][0]), "+v"(xf[2][1]), "+v"(wf[2][0]), "+v"(wf[2][1]), "+v"(xf[3][0]), "+v"(xf[3][1]), "+v"(wf[3][0]), "+v"(wf[3][1]),
               "+v"(xa), "+v"(wa), "+v"(wb), "+v"(ca), "+v"(ma0));
  STAMP(1);
  auto ldx = [&](int s, int h) { xf[s][h] = lds_b128(xa ^ (s << 5), h * 4096); sfence(); };
  auto ldw = [&](int s, int h) { wf[s][h] = lds_tr<1024>(h ? wb : wa, s * 4096); sfence(); };
  auto m1 = [&](int s, int i) {
    const int t = (i + 1) >> 1 & 1, u = i >> 1;
    acc[t][u] = XT ? mfma32(xf[s][t], wf[s][u], acc[t][u]) : mfma32(wf[s][u], xf[s][t], acc[t][u]);
    sfence();
  };
  __builtin_amdgcn_s_setprio(2);
  barrier();
#pragma unroll 1
  for (u32 st = 0; st < 8; ++st) {
    m1(2, 0); ldw(0, 0); m1(2, 1); ldx(0, 0); ldx(0, 1); m1(2, 2); ldw(0, 1); m1(2, 3); ldw(1, 0);
    m1(3, 0); ldx(1, 0); ldx(1, 1); m1(3, 1); ldw(1, 1); m1(3, 2); ldw(2, 0); m1(3, 3); ldx(2, 0); ldx(2, 1);
    m1(0, 0); ldw(2, 1); m1(0, 1); ldw(3, 0); m1(0, 2); ldx(3, 0); ldx(3, 1); m1(0, 3); ldw(3, 1);
    m1(1, 0);
    xa = (xa + 32768) & 131071; wa = (wa + 32768) & 131071; wb = (wb + 32768) & 131071;
    asm volatile("" : "+v"(xa), "+v"(wa), "+v"(wb));
    sfence();
    m1(1, 1); m1(1, 2); m1(1, 3);
    barrier();
  }
  f32x4 rl[8];
  m1(2, 0);
  const u32x2 tg = *LDSP(u32x2, L_SYNC + 40);
  const u32 n0 = *LDSP(u32, ma0), n1 = *LDSO(u32, ma0, 128);
  wf[0][0] = lds_b128(ca); wf[0][1] = lds_b128(ca, 2048);
  sfence();
  m1(2, 3);
  if (XT) {
    const u32 ra = opaque_v(L_RL + (mh * 64 + hh * 4) * 4);
#pragma unroll
    for (int i = 0; i < 8; ++i) rl[i] = *LDSO(f32x4, ra, i * 32);
    sfence();
  }
  m1(3, 0);
  tag = ((u64)(u32)__builtin_amdgcn_readfirstlane(tg.y) << 32) | (u32)__builtin_amdgcn_readfirstlane(tg.x);
  xf[0][0] = as_bf((u32x4){n0, n0, n0, n0}); xf[0][1] = as_bf((u32x4){n1, n1, n1, n1});
  keep(xf[0][0]); keep(xf[0][1]);
  sfence();
  m1(3, 3); m1(0, 0); m1(0, 3); m1(2, 1); m1(2, 2); m1(3, 1); m1(3, 2); m1(0, 1); m1(0, 2);
  STAMP(2);
  if constexpr (!XT) {
    __builtin_amdgcn_s_setprio(YPRIO);
#pragma unroll
    for (int k = 0; k < 8; ++k) {
      const int t = k >> 2, u = (k >> 1) & 1, s = k & 1;
      u32x4 o;
#pragma unroll
      for (int i = 0; i < 4; ++i) o[i] = relu2(pack2(acc[t][u][8 * s + 2 * i], acc[t][u][8 * s + 2 * i + 1]));
      store_dev(o, srd_ws, lane * 16, WS_A + ((rt * 4 + mh * 2 + t) * NSTEP + g * 8 + u * 4 + nh * 2 + s) * 1024);
      if (k == YBAR) asm volatile("s_barrier" ::: "memory");
    }
    STAMP(3);
    wchunk(2);
    if (YW2) { wchunk(3); wait_vm<8>(); } else wait_vm<4>();
    store_one((u32x4){(u32)tag, (u32)(tag >> 32), 0u, 0u}, srd_ws, WS_FLAGS + ((rt * 2 + mh) * 48 + g * 2 + nh) * 128);
    __builtin_amdgcn_s_setprio(0);
    if (!YW2) wchunk(3);
    wlate();
  } else {
    f32x16 kv = {}, zz = {};
    const bf16x8 ones = as_bf((u32x4){0x3f803f80u, 0x3f803f80u, 0x3f803f80u, 0x3f803f80u});
    const bool up = lane & 16;
    const u32 kvb = WS_KVP + (g * 4 + nh * 2) * KVH_B + (rt * 2 + mh) * 128;
    const u32 o0 = opaque_v((up ? KVH_B : 0) + hh * 16384 + (lane & 8) * 512 + (lane & 7) * 16);
    const u32 oz = opaque_v((lane & 14) ? OOB : (up ? 64 : 0) + hh * 32 + (lane & 1) * 16);
#pragma unroll
    for (int k = 0; k < 4; ++k) {
      const int t = k >> 1, s = k & 1;
      if (k == XHOLD) asm volatile("s_barrier" ::: "memory");
      if (k == 1 || k == 2) wchunk(k + 1);
      if (k == 3) wlate();
      float t2[8], v2[8];
#pragma unroll
      for (int i = 0; i < 8; ++i) t2[i] = mul1(acc[t][0][8 * s + i], rl[2 * k + (i >> 2)][i & 3]);
#pragma unroll
      for (int i = 0; i < 8; ++i) v2[i] = mul1(acc[t][1][8 * s + i], rl[2 * k + (i >> 2)][i & 3]);
      sfence();
      const u32x4 pv = {pack2(v2[0], v2[1]), pack2(v2[2], v2[3]), pack2(v2[4], v2[5]), pack2(v2[6], v2[7])};
      const u32x4 pk = phi8(t2);
      kv = mfma32(as_bf(pk), as_bf(pv), kv);
      zz = mfma32(as_bf(pk), ones, zz);
      sfence();
    }
    u32 ks[4], zs[4];
#pragma unroll
    for (int i = 0; i < 4; ++i) { const u32 lo = pack2(kv[2 * i], kv[2 * i + 1]), hi = pack2(kv[8 + 2 * i], kv[9 + 2 * i]); ks[i] = up ? hi : lo; }
    store_dev((u32x4){ks[0], (u32)tag, ks[1], (u32)(tag >> 32)}, srd_ws, o0, kvb);
    store_dev((u32x4){ks[2], (u32)tag, ks[3], (u32)(tag >> 32)}, srd_ws, o0, kvb + 8192);
#pragma unroll
    for (int i = 0; i < 4; ++i) { const u32 lo = pack2(zz[2 * i], zz[2 * i + 1]), hi = pack2(zz[8 + 2 * i], zz[9 + 2 * i]); zs[i] = up ? hi : lo; }
    const bool od = lane & 1;
    store_dev((u32x4){od ? zs[2] : zs[0], (u32)tag, od ? zs[3] : zs[1], (u32)(tag >> 32)}, srd_ws, oz, WS_ZP + (g * 2 + nh) * 4096 + (rt * 2 + mh) * 128);
    STAMP(3);
    __builtin_amdgcn_s_setprio(0);
  }
}

DEV void group0(const Ptrs& p, u32 b, u32 lane, u32 w, u32* stamp) {
  u64 tag;
  if (b & 64) group0_first<false>(p, b, lane, w, tag, stamp); else group0_first<true>(p, b, lane, w, tag, stamp);
  STAMP(5);
  b = opaque_s(b); lane = opaque_v(lane);
  const u32 rt = (b & 1) * 8 + ((b >> 3) & 7), kq = (b >> 1) & 3, ct = b >> 6, hh = lane >> 5, q = (lane & 15) >> 2;
  const __amdgpu_buffer_rsrc_t rws = __builtin_amdgcn_make_buffer_rsrc(p.ws, 0, WS_END, 0x00020000);
  const u32x4 srd_ws = make_srd(p.ws, WS_END);
  const char* fp = p.ws + WS_FLAGS + ((rt * 2 + (w >> 1)) * 48 + kq * 4) * 128;
  const u32 ab = WS_A + (rt * 4 + w) * NSTEP * 1024;
  u32 we[4], wm[4];
#pragma unroll
  for (int i = 0; i < 4; ++i) {
    we[i] = opaque_v(L_WE + (4 * hh + q) * 256 + (lane & 16) * 2 + (lane & 3) * 8 + ((i ^ q) << 6));
    wm[i] = opaque_v(we[i] + (L_WL - L_WE));
  }
  const u32 rdy = opaque_v(L_SYNC + 48 + w * 4);
  u32x4 af[22];
  const u32 l16 = opaque_v(lane * 16);
  auto lda = [&](int s) {
    const u32 so = s < 16 ? ab + (16 * kq + s) * 1024 : ((s < 20 || kq < 3) ? ab + (48 + 6 * kq + s) * 1024 : OOB);
    asm volatile("buffer_load_dwordx4 %0, %1, %2, %3 offen sc1" : "=v"(af[s]) : "v"(l16), "s"(srd_ws), "s"(so) : "memory");
  };
  bf16x8 wf[3][4];
  f32x16 acc[4] = {};
  auto ldw = [&](int s, int i) { wf[s % 3][i] = lds_tr<2048>(s < 16 ? we[i] : wm[i], (s & 15) * 4096); sfence(); };
  u32 c0 = 0;
  poll4(fp, tag, SPIN_CAP);
  wait_vm<0>();
  lds_signal(L_SYNC + 4);
  STAMP(6);
  lds_wait(L_SYNC + 8, 4);
#pragma unroll
  for (int s = 0; s < 8; ++s) lda(s);
#pragma unroll
  for (int i = 0; i < 8; ++i) ldw(i >> 2, i & 3);
  lds_wait(L_SYNC + 4, 4);
  __builtin_amdgcn_s_setprio(GPRIO);
  STAMP(7);
  static_for<22>([&](auto S_) {
    constexpr int s = S_;
    if constexpr (s < 4) { lda(8 + 2 * s); lda(9 + 2 * s); }
    if (s == SL - 2) { c0 = __hip_atomic_load(LDSP(u32, rdy), __ATOMIC_RELAXED, __HIP_MEMORY_SCOPE_WORKGROUP); sfence(); }
    if (s == SL) {
      if ((u32)__builtin_amdgcn_readfirstlane(c0) < 1) lds_wait(rdy, 1);
      STAMP(8);
#pragma unroll
      for (int k = 16; k < 22; ++k) lda(k);
    }
    if (KGATE > 1 && s == KGATE) lds_signal(L_SYNC + 12);
    need<s, s>(af[s]);
#pragma unroll
    for (int i = 0; i < 4; ++i) {
      acc[i] = mfma32(wf[s % 3][i], as_bf(af[s]), acc[i]); keep(acc[i]); sfence();
      if (s + 2 < 22) ldw(s + 2, i);
    }
  });
  STAMP(10);
  const u32 xb = WS_X + (rt * 4 + ct) * 131072, xf = WS_XFL + (rt * 4 + ct) * 2048;
#pragma unroll
  for (int i = 0; i < 4; ++i)
#pragma unroll
    for (int k = 0; k < 2; ++k)
      store_dev((u32x4){pack2(acc[i][8 * k], acc[i][8 * k + 1]), pack2(acc[i][8 * k + 2], acc[i][8 * k + 3]), pack2(acc[i][8 * k + 4], acc[i][8 * k + 5]),
                        pack2(acc[i][8 * k + 6], acc[i][8 * k + 7])}, srd_ws, l16, xb + ((w * 4 + i) * 4 + kq) * 2048 + k * 1024);
  if (KGATE == 1) lds_signal(L_SYNC + 12);
  const u32 tg = (kq & 2) + (w >> 1), cu = (kq & 1) * 2 + (w & 1);
  const u32 oo = (rt * 128 + tg * 32 + (lane & 31)) * 1024 + ct * 256 + cu * 64 + hh * 16;
  const u32x4 xr0 = *(const GAS u32x4*)(p.x + oo), xr1 = *(const GAS u32x4*)(p.x + oo + 32);
  const float rs = *LDSP(float, L_RL + (tg * 32 + (lane & 31)) * 4);
  sfence();
  wait_vm<2>();
  store_one((u32x4){(u32)tag, (u32)(tag >> 32), 0u, 0u}, srd_ws, xf + w * 512 + kq * 128);
  __builtin_amdgcn_s_setprio(0);
  STAMP(11);
  poll4(p.ws + xf + tg * 512, tag, SPIN_CAP);
  STAMP(12);
  u32x4 pp[8];
#pragma unroll
  for (int i = 0; i < 8; ++i) pp[i] = __builtin_amdgcn_raw_buffer_load_b128(rws, l16, xb + (tg * 4 + cu) * 8192 + i * 1024, 16);
  sfence();
  float v[16];
#pragma unroll
  for (int e = 0; e < 8; ++e) {
    v[2 * e] = ((lo_f(pp[e >> 2][e & 3]) + lo_f(pp[2 + (e >> 2)][e & 3])) + (lo_f(pp[4 + (e >> 2)][e & 3]) + lo_f(pp[6 + (e >> 2)][e & 3]))) * rs;
    v[2 * e + 1] = ((hi_f(pp[e >> 2][e & 3]) + hi_f(pp[2 + (e >> 2)][e & 3])) + (hi_f(pp[4 + (e >> 2)][e & 3]) + hi_f(pp[6 + (e >> 2)][e & 3]))) * rs;
  }
#pragma unroll
  for (int e = 0; e < 16; ++e) keep(v[e]);
  STAMP(9);
  barrier();
  STAMP(4);
  f32x4 at[4][4];
#pragma unroll
  for (int r4 = 0; r4 < 4; ++r4)
#pragma unroll
    for (int i = 0; i < 4; ++i) at[r4][i] = *LDSO(f32x4, w * 4096 + l16, i * 16384 + r4 * 1024);
  sfence();
#pragma unroll
  for (int k = 0; k < 2; ++k) {
#pragma unroll
    for (int e = 0; e < 8; ++e) { const int r4 = 2 * k + (e >> 2); v[8 * k + e] += (at[r4][0][e & 3] + at[r4][1][e & 3]) + (at[r4][2][e & 3] + at[r4][3][e & 3]); }
#pragma unroll
    for (int e = 0; e < 4; ++e) {
      auto sw = __builtin_amdgcn_permlane32_swap(asu(v[8 * k + e]), asu(v[8 * k + 4 + e]), false, false);
      v[8 * k + e] = asf(sw[0]); v[8 * k + 4 + e] = asf(sw[1]);
    }
    const u32x4 xr = k ? xr1 : xr0;
    u32x4 o;
#pragma unroll
    for (int e = 0; e < 4; ++e) o[e] = pack2(__builtin_fmaf(v[8 * k + 2 * e], LN2, lo_f(xr[e])), __builtin_fmaf(v[8 * k + 2 * e + 1], LN2, hi_f(xr[e])));
    *(GAS u32x4*)(p.out + oo + k * 32) = o;
  }
  if (b == 0 && w == 0) store_one((u32x4){(u32)tag, (u32)(tag >> 32), 0u, 0u}, srd_ws, WS_EPOCH);
  STAMP(13);
}

__global__ __launch_bounds__(512, 2) void fused_block(const char* __restrict__ x, const char* __restrict__ wqkv, const char* __restrict__ w1,
                                                      char* ws, const char* __restrict__ w2, const char* __restrict__ wo, char* __restrict__ out) {
  __shared__ __attribute__((aligned(16))) char lds_all[L_TOTAL];
  u32 stamp[32] = {};
  u64 pc;
  asm volatile("s_getpc_b64 %0" : "=s"(pc) : "s"((u32)(size_t)lds_all));
  STAMP(0);
  const u32 w = __builtin_amdgcn_readfirstlane(__builtin_amdgcn_workitem_id_x() >> 6);
  {
    u32 d0, d1, d2, d3;
    const u64 cp = (pc & ~127ull) + umin((__builtin_amdgcn_workgroup_id_x() >> 3) * 8 + w * 2, CODE_LINES - 2) * 128;
    asm volatile("s_cmp_gt_u32 %5, 3\n\ts_cbranch_scc1 1f\n\ts_load_dword %0, %4, 0\n\ts_load_dword %1, %4, 64\n\ts_load_dword %2, %4, 128\n\t"
                 "s_load_dword %3, %4, 192\n\ts_waitcnt lgkmcnt(0)\n1:"
                 : "=&s"(d0), "=&s"(d1), "=&s"(d2), "=&s"(d3) : "s"(cp), "s"(w) : "memory", "scc");
  }
  asm volatile("" ::"s"(x), "s"(wqkv), "s"(w1), "s"(ws), "s"(w2), "s"(wo), "s"(out));
  const Ptrs p = {x, wqkv, w1, ws, w2, wo, out};
  if (w < 4) group0(p, opaque_s(__builtin_amdgcn_workgroup_id_x()), opaque_v(__builtin_amdgcn_workitem_id_x()) & 63, w, stamp);
  else group1(p, __builtin_amdgcn_workgroup_id_x(), __builtin_amdgcn_workitem_id_x() & 63, w, stamp);
}

#if defined(__HIP_DEVICE_COMPILE__)
#pragma clang attribute pop
#endif

extern "C" void kernel_launch(void* const* d_in, const int* in_sizes, int n_in, void* d_out, int out_size, void* d_ws, size_t ws_size,
                              hipStream_t stream) {
  fused_block<<<256, 512, 0, stream>>>((const char*)d_in[0], (const char*)d_in[1], (const char*)d_in[4], (char*)d_ws, (const char*)d_in[5],
                                       (const char*)d_in[2], (char*)d_out);
}
```

```cpp
#define PROBE 1
#define PGRP 0
#define PTYPE 0
#include <hip/hip_runtime.h>
#include <hip/hip_bf16.h>
#include <type_traits>

#if defined(__HIP_DEVICE_COMPILE__)
#pragma clang attribute push(__attribute__((target("no-packed-fp32-ops"))), apply_to = function)
#endif
typedef unsigned u32;
typedef unsigned long long u64;
typedef __bf16 bf16x8 __attribute__((ext_vector_type(8)));
typedef __bf16 bf16x2 __attribute__((ext_vector_type(2)));
typedef short s16x4 __attribute__((ext_vector_type(4)));
typedef short s16x2 __attribute__((ext_vector_type(2)));
typedef float f32x16 __attribute__((ext_vector_type(16)));
typedef float f32x4 __attribute__((ext_vector_type(4)));
typedef float f32x2 __attribute__((ext_vector_type(2)));
typedef u32 u32x4 __attribute__((ext_vector_type(4)));
typedef u32 u32x2 __attribute__((ext_vector_type(2)));
#define LAS __attribute__((address_space(3)))
#define GAS __attribute__((address_space(1)))
#define DEV __device__ __forceinline__
#define RLX_AGENT __ATOMIC_RELAXED, __HIP_MEMORY_SCOPE_AGENT
#define LDSP(T, a) ((LAS T*)(size_t)(a))

#define ROT1 1
#define PF 1
#define QWIN 3
#define AWIN 5
#define DLY 0
#define SLP 2
#define PSC "sc1"
#define SL 8
#define SO 12
#define YPRIO 0
#define PFMODE 0
#define GATE 1
#define KVMODE 6
#define STATST 0
#define SPINSLP "s_sleep 1\n\t"
#define LORD 2
#define GST 5
#define YW2 1
#define YBAR 1
#define KVAFTER 2
#define ZDLY 2
#define RSLP 2
#define XHOLD 4
#define KPRIO 0
#define GPRIO 1
#define G2EARLY 0
#define PFS 0
#define PFN 5
#define XDMA 1
#define BARS 10
#define WO2 0
#define LEAD 8
#define PRIO2 1

constexpr u32 WS_EPOCH = 0;
constexpr u32 WS_FLAGS = 4096;
constexpr u32 WS_XFL = WS_FLAGS + 32 * 48 * 128;
constexpr u32 WS_KVF = WS_XFL + 1024 * 128;
constexpr u32 WS_KVZ = WS_KVF + 32768;
constexpr u32 WS_ZP = WS_KVZ + 2048;
constexpr u32 WS_KVP = WS_ZP + 65536;
constexpr u32 KVH_B = 32768;
constexpr u32 WS_CS = 3u << 19;
constexpr u32 RT_B = 1u << 20;
constexpr u32 WS_A = 2u << 20;
constexpr u32 NSTEP = 118;
constexpr u32 WS_STATS = WS_A + 4 * NSTEP * 1024;
constexpr u32 WS_X = WS_A + 524288;
constexpr u32 WS_END = WS_A + 16 * RT_B;
constexpr u32 WS_STAMP = 32u << 20;
static_assert(WS_KVP + 32 * KVH_B <= WS_CS && WS_CS + 524288 <= WS_A, "ws map");
constexpr u32 OOB = 0x40000000u;

constexpr u32 L_WE = 65536;
constexpr u32 L_SCR = 131072;
constexpr u32 L_CORRA = L_SCR;
constexpr u32 L_CORRB = L_SCR + 4096;
constexpr u32 L_MN = L_SCR + 6144;
constexpr u32 L_RL = L_SCR + 6656;
constexpr u32 L_SYNC = L_SCR + 7168;
constexpr u32 L_JUNK = L_SCR + 7424;
constexpr u32 L_WL = L_SCR + 8192;
constexpr u32 L_KVF = 32768;
constexpr u32 L_FINE = 49152;
constexpr u32 L_TOTAL = 163840;

constexpr u32 CODE_LINES = 180;
constexpr u32 SPIN_CAP = 1u << 18;
constexpr u32 HINT_CAP = 1u << 12;
constexpr float LOG2E = 1.4426950408889634f, LN2 = 0.6931471805599453f;

DEV float asf(u32 v) { return __builtin_bit_cast(float, v); }
DEV u32 asu(float v) { return __builtin_bit_cast(u32, v); }
DEV bool all_lanes(bool v) { return __builtin_amdgcn_ballot_w64(!v) == 0; }
DEV u32 umin(u32 a, u32 b) { return a < b ? a : b; }
DEV void sfence() { __builtin_amdgcn_sched_barrier(0); }
template <int N> DEV void wait_vm() { asm volatile("s_waitcnt vmcnt(%0)" ::"i"(N) : "memory"); }
DEV void wait_lgkm() { sfence(); __builtin_amdgcn_s_waitcnt(0xC07F); asm volatile("" ::: "memory"); sfence(); }
DEV void barrier() { sfence(); __builtin_amdgcn_s_waitcnt(0xC07F); asm volatile("s_barrier" ::: "memory"); sfence(); }

DEV u32x4 make_srd(const void* p, u32 bytes) {
  u64 a = (u64)p;
  return (u32x4){(u32)a, (u32)(a >> 32) & 0xffffu, bytes, 0x00020000u};
}
template <int POL> DEV void dma(u32x4 srd, u32 voff, u32 soff, u32 ldsaddr) {
  if (POL == 1)
    asm volatile("s_mov_b32 m0, %3\n\ts_nop 0\n\tbuffer_load_dwordx4 %0, %1, %2 offen nt lds" ::"v"(voff), "s"(srd), "s"(soff), "s"(ldsaddr) : "memory");
  else if (POL == 2)
    asm volatile("s_mov_b32 m0, %3\n\ts_nop 0\n\tbuffer_load_dwordx4 %0, %1, %2 offen sc1 lds" ::"v"(voff), "s"(srd), "s"(soff), "s"(ldsaddr) : "memory");
  else
    asm volatile("s_mov_b32 m0, %3\n\ts_nop 0\n\tbuffer_load_dwordx4 %0, %1, %2 offen lds" ::"v"(voff), "s"(srd), "s"(soff), "s"(ldsaddr) : "memory");
}
template <int POL> DEV void dma4(u32x4 srd, u32x4 v, u32 soff, u32 ldsaddr) {
  if (POL == 1)
    asm volatile("s_mov_b32 m0, %6\n\ts_nop 0\n\tbuffer_load_dwordx4 %0, %4, %5 offen nt lds\n\tbuffer_load_dwordx4 %1, %4, %5 offen offset:1024 nt lds\n\t"
                 "buffer_load_dwordx4 %2, %4, %5 offen offset:2048 nt lds\n\tbuffer_load_dwordx4 %3, %4, %5 offen offset:3072 nt lds"
                 ::"v"(v.x), "v"(v.y), "v"(v.z), "v"(v.w), "s"(srd), "s"(soff), "s"(ldsaddr) : "memory");
  else
    asm volatile("s_mov_b32 m0, %6\n\ts_nop 0\n\tbuffer_load_dwordx4 %0, %4, %5 offen lds\n\tbuffer_load_dwordx4 %1, %4, %5 offen offset:1024 lds\n\t"
                 "buffer_load_dwordx4 %2, %4, %5 offen offset:2048 lds\n\tbuffer_load_dwordx4 %3, %4, %5 offen offset:3072 lds"
                 ::"v"(v.x), "v"(v.y), "v"(v.z), "v"(v.w), "s"(srd), "s"(soff), "s"(ldsaddr) : "memory");
}
DEV void dma2(u32x4 srd, u32 v0, u32 v1, u32 soff, u32 ldsaddr) {
  asm volatile("s_mov_b32 m0, %4\n\ts_nop 0\n\tbuffer_load_dwordx4 %0, %2, %3 offen lds\n\tbuffer_load_dwordx4 %1, %2, %3 offen offset:1024 lds"
               ::"v"(v0), "v"(v1), "s"(srd), "s"(soff), "s"(ldsaddr) : "memory");
}
DEV void touch64(u32x4 srd, u32 voff, u32 soff, u32 ldsaddr) {
  asm volatile("s_mov_b32 m0, %3\n\ts_nop 0\n\tbuffer_load_dword %0, %1, %2 offen lds" ::"v"(voff), "s"(srd), "s"(soff), "s"(ldsaddr) : "memory");
}
DEV u32x4 steps4(u32 v, u32 step) { step -= 1024; return (u32x4){v, v + step, v + 2 * step, v + 3 * step}; }
DEV void store_one(u32x4 v, u32x4 srd, u32 soff) {
  asm volatile("s_mov_b64 exec, 1\n\tbuffer_store_dwordx4 %0, off, %1, %2 sc1\n\ts_mov_b64 exec, -1\n\ts_nop 0" ::"v"(v), "s"(srd), "s"(soff) : "memory");
}
DEV void store_dev(u32x4 v, u32x4 srd, u32 voff, u32 soff) {
  asm volatile("buffer_store_dwordx4 %0, %1, %2, %3 offen sc1\n\ts_nop 1" ::"v"(v), "v"(voff), "s"(srd), "s"(soff) : "memory");
}
#define LDSO(T, a, o) ((LAS T*)(LDSP(char, a) + (o)))
DEV bf16x8 lds_b128(u32 a, int o = 0) { return *LDSO(bf16x8, a, o); }
template <int D> DEV bf16x8 lds_tr(u32 a, int o) {
  s16x4 lo = __builtin_amdgcn_ds_read_tr16_b64_v4i16(LDSO(s16x4, a, o));
  s16x4 hi = __builtin_amdgcn_ds_read_tr16_b64_v4i16(LDSO(s16x4, a, o + D));
  return __builtin_bit_cast(bf16x8, __builtin_shufflevector(lo, hi, 0, 1, 2, 3, 4, 5, 6, 7));
}
DEV f32x16 mfma32(bf16x8 a, bf16x8 b, f32x16 c) { return __builtin_amdgcn_mfma_f32_32x32x16_bf16(a, b, c, 0, 0, 0); }
DEV f32x4 mfma16(bf16x8 a, bf16x8 b, f32x4 c) { return __builtin_amdgcn_mfma_f32_16x16x32_bf16(a, b, c, 0, 0, 0); }
DEV bf16x8 as_bf(u32x4 v) { return __builtin_bit_cast(bf16x8, v); }
DEV u32 pack2(float a, float b) { return __builtin_bit_cast(u32, __builtin_convertvector((f32x2){a, b}, bf16x2)); }
DEV u32 relu2(u32 v) { return __builtin_bit_cast(u32, __builtin_elementwise_max(__builtin_bit_cast(s16x2, v), (s16x2){0, 0})); }
DEV float lo_f(u32 v) { return asf(v << 16); }
DEV float hi_f(u32 v) { return asf(v & 0xffff0000u); }
DEV float rbf(float f) { return lo_f(pack2(f, 0.f)); }
DEV u32 split_hi_lo(float c) { float h = rbf(c); return pack2(h, c - h); }
DEV float mul1(float a, float b) { float r = a * b; asm("" : "+v"(r)); return r; }
template <int CTRL> DEV float dpp_add(float v) {
  return v + asf(__builtin_amdgcn_update_dpp(0u, asu(v), CTRL, 0xf, 0xf, true));
}
DEV float swap16_add(float v) {
  auto r = __builtin_amdgcn_permlane16_swap(asu(v), asu(v), false, false);
  return asf(r[0]) + asf(r[1]);
}
DEV float swap32_add(float v) {
  auto r = __builtin_amdgcn_permlane32_swap(asu(v), asu(v), false, false);
  return asf(r[0]) + asf(r[1]);
}
DEV float sum32(float v) {
  v = dpp_add<0xB1>(v); v = dpp_add<0x4E>(v); v = dpp_add<0x141>(v); v = dpp_add<0x140>(v);
  return swap16_add(v);
}
DEV float max_eps(float t2) { float m; asm("v_max_f32 %0, %1, %2" : "=v"(m) : "s"(1e-6f * LOG2E), "v"(t2)); return m; }
DEV u32x4 phi8(const float (&t2)[8]) {
  float e[8], m[8];
#pragma unroll
  for (int i = 0; i < 8; ++i) e[i] = __builtin_amdgcn_fmed3f(__builtin_amdgcn_exp2f(t2[i]), 0.f, 1.f);
  sfence();
#pragma unroll
  for (int i = 0; i < 8; ++i) asm("v_max_f32 %0, %1, %2" : "=v"(m[i]) : "s"(1e-6f * LOG2E), "v"(t2[i]));
  sfence();
#pragma unroll
  for (int i = 0; i < 8; ++i) { e[i] = __builtin_fmaf(m[i], LN2, e[i]); asm("" : "+v"(e[i])); }
  sfence();
  return (u32x4){pack2(e[0], e[1]), pack2(e[2], e[3]), pack2(e[4], e[5]), pack2(e[6], e[7])};
}
DEV void lds_signal(u32 addr, u32 inc = 1u) {
  asm volatile("s_mov_b64 exec, 1\n\tds_add_u32 %0, %1\n\ts_mov_b64 exec, -1" ::"v"(addr), "v"(inc) : "memory");
}
DEV void lds_wait(u32 addr, u32 waves) {
  u32 v, s, n = SPIN_CAP;
  asm volatile("1:\n\tds_read_b32 %0, %3\n\ts_waitcnt lgkmcnt(0)\n\tv_readfirstlane_b32 %1, %0\n\ts_sub_u32 %2, %2, 1\n\ts_cbranch_scc1 2f\n\t"
               "s_cmp_lt_u32 %1, %4\n\ts_cbranch_scc1 1b\n2:"
               : "=&v"(v), "=&s"(s), "+s"(n) : "v"(addr), "s"(waves) : "memory", "scc");
}
DEV u64 poll4(const char* p, u64 tag, u32 n) {
  u64 f0, f1, f2, f3;
  asm volatile("1:\n\ts_load_dwordx2 %0, %5, 0 glc\n\ts_load_dwordx2 %1, %5, 128 glc\n\ts_load_dwordx2 %2, %5, 256 glc\n\t"
               "s_load_dwordx2 %3, %5, 384 glc\n\ts_waitcnt lgkmcnt(0)\n\t"
               "s_xor_b64 %0, %0, %6\n\ts_xor_b64 %1, %1, %6\n\ts_xor_b64 %2, %2, %6\n\ts_xor_b64 %3, %3, %6\n\t"
               "s_or_b64 %0, %0, %1\n\ts_or_b64 %2, %2, %3\n\ts_or_b64 %0, %0, %2\n\ts_cbranch_scc0 2f\n\t"
               "s_sub_u32 %4, %4, 1\n\ts_cbranch_scc0 1b\n2:"
               : "=&s"(f0), "=&s"(f1), "=&s"(f2), "=&s"(f3), "+s"(n) : "s"(p), "s"(tag) : "memory", "scc");
  return f0;
}
template <int D> DEV void poll2(const char* p, u64 tag, u64 mask) {
  u64 f0, f1; u32 n = SPIN_CAP;
  asm volatile("1:\n\ts_load_dwordx2 %0, %3, 0 glc\n\ts_load_dwordx2 %1, %3, %6 glc\n\ts_waitcnt lgkmcnt(0)\n\t"
               "s_xor_b64 %0, %0, %4\n\ts_xor_b64 %1, %1, %4\n\ts_or_b64 %0, %0, %1\n\ts_and_b64 %0, %0, %5\n\ts_cbranch_scc0 2f\n\t"
               "s_sub_u32 %2, %2, 1\n\ts_cbranch_scc0 1b\n2:"
               : "=&s"(f0), "=&s"(f1), "+s"(n) : "s"(p), "s"(tag), "s"(mask), "i"(D) : "memory", "scc");
}
DEV u32x4 load_dev_wait(u32x4 srd, u32 voff, u32 soff) {
  u32x4 v;
  asm volatile("buffer_load_dwordx4 %0, %1, %2, %3 offen sc1\n\ts_waitcnt vmcnt(0)" : "=&v"(v) : "v"(voff), "s"(srd), "s"(soff) : "memory");
  return v;
}
template <int N, int I = 0, class F> DEV void static_for(F&& f) {
  if constexpr (I < N) { f(std::integral_constant<int, I>{}); static_for<N, I + 1>(f); }
}
template <int T, int K> DEV void need(u32x4& v) {
  asm volatile("s_waitcnt vmcnt(%1)" : "+v"(v) : "i"((T + LEAD < 30 ? T + LEAD : 29) - K));
}
template <class T> DEV void keep(T& v) { asm volatile("" : "+v"(v)); }
template <class T> DEV T opaque_v(T v) { asm volatile("" : "+v"(v)); return v; }
template <class T> DEV T opaque_s(T v) { asm volatile("" : "+s"(v)); return v; }

#define STAMP(i) do { if ((i) == 0 || (i) == PROBE) { sfence(); stamp[(i) ? 1 : 0] = (u32)__builtin_amdgcn_s_memrealtime(); __builtin_amdgcn_s_waitcnt(0xC07F); sfence(); } } while (0)
#define FSTAMP(w, i)

struct Ptrs { const char *x, *wqkv, *w1; char* ws; const char *w2, *wo; char* out; };

DEV void group1(const Ptrs& p, u32 b, u32 lane, u32 w, u32* stamp) {
  const u32 xcd = b & 7, r = b >> 3, j = w & 3;
  const u32 rt = (xcd & 1) * 8 + (r & 7);
  const u32 rot = (r & 7) + (xcd & 6);
  const u32x4 srd_x = make_srd(p.x, 2048 * 1024);
  const u32 vx = (lane >> 3) * 1024 + (((lane & 7) ^ (lane >> 4)) << 4);
  const u32x4 vx4 = {vx, (vx ^ 64) + 7168, vx + 14336, (vx ^ 64) + 21504};
  auto issue_x = [&](int st) { dma4<0>(srd_x, vx4, (rt * 128 + 32 * j) * 1024 + ((st + rot) & 7) * 128, (st & 3) * 32768 + j * 4096); };
  issue_x(0);
  sfence();
  const u32 c4 = (xcd >> 1) * 4 + (r >> 3), g = c4 >> 1;
  const bool xt = (c4 & 1) == 0;
  const u32 hh = lane >> 5, q = (lane & 15) >> 2;
  const u32 wst = xt ? 3072 : 2752;
  const u32x4 srd_w = make_srd(xt ? p.wqkv : p.w1, 512 * wst);
  const u32 ym = xt ? 0u : ~0u, l4 = lane >> 4;
  const u32 blk = ((q ^ l4) + (((q & l4) << 1) & ym)) & 3;
  const u32x4 vwa4 = steps4(l4 * wst + (lane & 3) * 16 + (blk & 1) * 64 + (blk >> 1) * (xt ? 1024 : 128) + (xt ? 1024 + g * 128 : g * 256), 4 * wst);
  auto issue_w = [&](int st) { dma4<0>(srd_w, vwa4, (((st + rot) & 7) * 64 + 16 * j) * wst, (st & 3) * 32768 + 16384 + j * 4096); };
  issue_w(0);
  issue_x(1); issue_w(1);
  sfence();
  const u32 nl = (xt || (g < 6 && !(g & 1))) ? 2 : 1, lstep = xt ? 86 + g * 4 : 64 + (g >> 1) * 6 + ((g & 1) ? (g < 6 ? 4 : 2) : 0);
  const u32x4 vwb4 = steps4((lane >> 3) * wst + ((((lane & 7) ^ ((lane >> 2) & 4)) << 4) & (nl == 2 ? 127 : 48)) + (xt ? g * 128 : lstep * 32), 8 * wst);
  auto issue = [&](int st, int part) {
    if (st < 8) { if (part) issue_w(st); else issue_x(st); }
    else dma4<0>(srd_w, vwb4, ((((st - 8) * 4 + j + rot) & 7) * 64 + 32 * part) * wst, (st - 8) * 32768 + j * 8192 + part * 4096);
  };
  dma2(srd_w, vwb4.x, vwb4.y, ((rot & 7) * 64 + 16 * j) * wst, L_WL + j * 2048);
  const u32x4 srd_pf = j == 3 ? make_srd(p.wo, 512 * 1024) : make_srd(p.w2, 1376 * 1024);
  const u32 vpf = lane < (j == 2 ? 24 : 32) ? lane * 128 : OOB;
  const u32 spf = j < 2 ? (c4 >> 2) * 262144 + r * 8192 + j * 4096 : j == 2 ? (64 + 6 * (c4 >> 2)) * 16384 + r * 3072 : (c4 >> 2) * 131072 + r * 4096;
  const u32 pfst = opaque_s(PFS + r % PFN);
  const u32x4 srd_ws = make_srd(p.ws, WS_END);
  const u32 xa = (j * 32 + (lane & 31)) * 128 + ((hh ^ ((lane >> 1) & 7)) << 4);
  const u32 wb0 = opaque_v(hh * 1024 + q * 128 + (lane & 16) * 2 + (lane & 3) * 8 + ((q & 2) << 5)), wb1 = opaque_v(wb0 ^ 64);
  const u32 kq = c4 >> 2, cb = (c4 & 3) * 256;
  const u32x4 vw2 = steps4(l4 * 1024 + (lane & 3) * 16 + ((q ^ l4) << 6), 4096);
  const u32 csb = WS_CS + (c4 * 2 + (xcd & 1)) * 16384 + j * 512;
  bf16x8 xf[32], cw[12];
  u32x4 gr[6];
  u64 tag;
  u32 mn;
  float rl;
  f32x4 s1[2] = {}, s2[2] = {}, ca = {}, cb2 = {};
#define GRAN_LOADS "buffer_load_dwordx4 %0, %6, %8, %9 offen sc1\n\tbuffer_load_dwordx4 %1, %6, %8, %9 offen offset:2048 sc1\n\t" \
                   "buffer_load_dwordx4 %2, %6, %8, %10 offen sc1\n\tbuffer_load_dwordx4 %3, %6, %8, %10 offen offset:2048 sc1\n\t" \
                   "buffer_load_dwordx4 %4, %7, %8, %11 offen sc1\n\tbuffer_load_dwordx4 %5, %7, %8, %11 offen offset:2048 sc1"
#define GRAN_OPS : "=&v"(gr[0]), "=&v"(gr[1]), "=&v"(gr[2]), "=&v"(gr[3]), "=&v"(gr[4]), "=&v"(gr[5]) \
                 : "v"(hh * 8192 + (lane & 31) * 16), "v"(hh * 4096 + (lane & 31) * 16), "s"(srd_ws), "s"(csb), "s"(csb + 4096), "s"(WS_STATS + rt * RT_B + j * 512) : "memory"
#pragma unroll
  for (int st = 0; st < 8; ++st) {
    const int n = st + 2;
    constexpr int extra[8] = {2, 0, 0, 3, 0, 0, 6, 0};
    if (PF && st > PFS && st <= PFS + PFN)
      asm volatile("s_cmp_eq_u32 %0, %1\n\ts_cbranch_scc1 1f\n\ts_waitcnt vmcnt(%2)\n1:\n\ts_waitcnt vmcnt(%3)" ::"s"(pfst), "i"(st - 1), "i"(8 + extra[st]), "i"(9 + extra[st]) : "memory", "scc");
    else asm volatile("s_waitcnt vmcnt(%0)" ::"i"(8 + extra[st]) : "memory");
    barrier();
    if (st == 0) STAMP(1);
    const u32 slot = (st & 3) * 32768;
    if (st == 5) asm volatile(GRAN_LOADS GRAN_OPS);
    if (PF && st >= PFS && st < PFS + PFN && st == pfst) touch64(srd_pf, vpf, spf, L_JUNK);
    if (st < 7) issue(n, 0);
    sfence();
    u32x2 tg;
    if (st == 0) tg = *LDSP(u32x2, L_SYNC + 128);
    if (st < 2) {
      const u32 xs = (j * 32 + (lane & 15)) * 128 + ((l4 ^ ((lane >> 1) & 7)) << 4);
#pragma unroll
      for (int u = 0; u < 4; ++u) cw[8 + u] = lds_b128(xs ^ ((u & 2) << 5), slot + (u & 1) * 2048);
      const u32 wa = 16384 + (8 * hh + q) * 256 + (lane & 16) * 2 + (lane & 3) * 8 + ((xt ? (j ^ q) : ((j - q) & 3)) << 6), wc = L_WL + (wb0 ^ ((j & 1) << 6));
#pragma unroll
      for (int s = 0; s < 4; ++s) cw[s] = st ? lds_tr<512>(wc, s * 2048) : lds_tr<1024>(wa, s * 4096);
    }
#pragma unroll
    for (int s = 0; s < 4; ++s) xf[st * 4 + s] = lds_b128((xa ^ (s << 5)) + (slot & 65536), slot & 32768);
    sfence();
    if (st == 2) {
      auto pick = [&](const f32x4& v) { const float a = opaque_v(v[0]), c = opaque_v(v[2]); const float lo = (lane & 1) ? v[1] : a, hi = (lane & 1) ? v[3] : c; return (lane & 2) ? hi : lo; };
      __builtin_amdgcn_s_setprio(3);
      store_dev((u32x4){split_hi_lo(ca[0]), (u32)tag, split_hi_lo(cb2[0]), (u32)(tag >> 32)}, srd_ws, hh ? OOB : lane * 16, csb + (r & 7) * 2048);
      const u32 so = j == (c4 & 3) ? WS_STATS + rt * RT_B + kq * 2048 + j * 512 : OOB, vo = l4 == q ? (lane & 15) * 16 : OOB;
      store_dev((u32x4){asu(pick(s1[0])), (u32)tag, asu(pick(s2[0])), (u32)(tag >> 32)}, srd_ws, vo, so);
      store_dev((u32x4){asu(pick(s1[1])), (u32)tag, asu(pick(s2[1])), (u32)(tag >> 32)}, srd_ws, vo, so + 256);
      __builtin_amdgcn_s_setprio(0);
    }
    if (st == 7) {
      asm volatile("" : "+v"(gr[0]), "+v"(gr[1]), "+v"(gr[2]), "+v"(gr[3]), "+v"(gr[4]), "+v"(gr[5]));
      __builtin_amdgcn_s_setprio(3);
#pragma unroll 1
      for (u32 i = 0; i < SPIN_CAP; ++i) {
        u32 bad = opaque_v(((gr[0].y ^ (u32)tag) | (gr[1].y ^ (u32)tag) | (gr[2].y ^ (u32)tag)) | ((gr[3].y ^ (u32)tag) | (gr[4].y ^ (u32)tag) | (gr[5].y ^ (u32)tag)));
        bad |= opaque_v(((gr[0].w ^ (u32)(tag >> 32)) | (gr[1].w ^ (u32)(tag >> 32)) | (gr[2].w ^ (u32)(tag >> 32))) |
                        ((gr[3].w ^ (u32)(tag >> 32)) | (gr[4].w ^ (u32)(tag >> 32)) | (gr[5].w ^ (u32)(tag >> 32))));
        if (all_lanes(bad == 0)) break;
        __builtin_amdgcn_s_sleep(8);
        asm volatile(GRAN_LOADS "\n\ts_waitcnt vmcnt(0)" GRAN_OPS);
      }
      *LDSP(u32x4, L_CORRA + j * 1024 + lane * 16) = (u32x4){gr[0].x, gr[1].x, gr[2].x, gr[3].x};
      *LDSP(u32x4, L_CORRB + (j & 1) * 1024 + lane * 16) = (u32x4){gr[0].z, gr[1].z, gr[2].z, gr[3].z};
      const float mean = swap32_add(asf(gr[4].x) + asf(gr[5].x)) * (1.f / 512.f);
      const float var = __builtin_fmaxf(__builtin_fmaf(-mean, mean, swap32_add(asf(gr[4].z) + asf(gr[5].z)) * (1.f / 512.f)), 0.f);
      const u32 ms = pack2(mean, __builtin_amdgcn_sqrtf(rbf(rbf(var) + 1e-5f)));
      mn = ((ms & 0xffffu) ^ 0x8000u) * 0x10001u;
      rl = LOG2E * __builtin_amdgcn_rcpf(hi_f(ms));
      *LDSP(u32, (hh ? L_RL : L_MN) + (j * 32 + (lane & 31)) * 4) = hh ? asu(rl) : mn;
      __builtin_amdgcn_s_setprio(0);
      sfence();
    }
    issue(n, st < 7);
    sfence();
    if (st == 0) tag = ((u64)(u32)__builtin_amdgcn_readfirstlane(tg.y) << 32) | (u32)__builtin_amdgcn_readfirstlane(tg.x);
    if (st < 2) {
      const u32 selv = (((lane >> 4) ^ (lane >> 2)) & 1) ? 0u : 0x3f803f80u;
      const bf16x8 sel = as_bf((u32x4){selv, selv, selv, selv}), ones = as_bf((u32x4){0x3f803f80u, 0x3f803f80u, 0x3f803f80u, 0x3f803f80u});
#pragma unroll
      for (int u = 0; u < 4; ++u) {
        s1[u & 1] = mfma16(cw[8 + u], ones, s1[u & 1]); s2[u & 1] = mfma16(cw[8 + u], cw[8 + u], s2[u & 1]);
        if (st) cb2 = mfma16(sel, cw[u], cb2); else ca = mfma16(sel, cw[u], ca);
      }
      asm volatile("" : "+v"(s1[0]), "+v"(s1[1]), "+v"(s2[0]), "+v"(s2[1]), "+v"(ca), "+v"(cb2));
      sfence();
    }
  }
  wait_vm<4>();
  __builtin_amdgcn_s_setprio(1);
  barrier();
  STAMP(2);
  {
    f32x16 acc[2];
    bf16x8 wf[8];
    const u32 arow = WS_A + rt * RT_B + (j * NSTEP + lstep) * 1024, l16 = lane * 16;
    auto ldw = [&](int s) { wf[s & 7] = lds_tr<512>(s >= 32 ? wb1 : wb0, (s & 31) * 2048); sfence(); };
    wf[6] = lds_b128(L_CORRB + l16); wf[7] = lds_b128(L_CORRB + l16, 1024);
#pragma unroll
    for (int s = 0; s < 6; ++s) ldw(s);
    issue(9, 1);
    dma4<0>(make_srd(p.w2, 1376 * 1024), vw2, (16 * kq + j) * 16384 + cb, L_WE + j * 4096);
    dma4<0>(make_srd(p.w2, 1376 * 1024), vw2, (16 * kq + 4 + j) * 16384 + cb, L_WE + (4 + j) * 4096);
    STAMP(8);
    const bf16x8 mf = as_bf((u32x4){mn, mn, mn, mn});
    acc[0] = mfma32(wf[6], mf, (f32x16){}); acc[1] = mfma32(wf[7], mf, (f32x16){});
    keep(acc[0]); keep(acc[1]);
    sfence();
#pragma unroll
    for (int s = 0; s < 32; ++s) {
      acc[0] = mfma32(wf[s & 7], xf[s], acc[0]); keep(acc[0]); sfence();
      if (s == BARS) asm volatile("s_waitcnt vmcnt(8)\n\ts_barrier" ::: "memory");
      ldw(s + 6);
    }
    STAMP(12);
    auto relu_store = [&](const f32x16& a, int h) {
      u32x4 o;
#pragma unroll
      for (int i = 0; i < 4; ++i) o[i] = relu2(pack2(a[8 * (h & 1) + 2 * i], a[8 * (h & 1) + 2 * i + 1]));
      store_dev(o, srd_ws, l16, arow + h * 1024);
    };
    if (xt) {
      float t[16], e[16];
      u32x4 o[2];
      static_for<32>([&](auto S_) {
        constexpr int s = S_;
        acc[1] = mfma32(wf[(32 + s) & 7], xf[s], acc[1]); keep(acc[1]); sfence();
        if constexpr (s + 6 < 32) ldw(32 + s + 6);
        static_for<3>([&](auto K_) {
          constexpr int k = (s - 2) * 3 + K_;
          if constexpr (k >= 0 && k < 16) t[k] = mul1(acc[0][k], rl);
          else if constexpr (k >= 16 && k < 32) { e[k - 16] = __builtin_amdgcn_fmed3f(__builtin_amdgcn_exp2f(t[k - 16]), 0.f, 1.f); keep(e[k - 16]); }
          else if constexpr (k >= 32 && k < 48) t[k - 32] = max_eps(t[k - 32]);
          else if constexpr (k >= 48 && k < 64) { e[k - 48] = __builtin_fmaf(t[k - 48], LN2, e[k - 48]); keep(e[k - 48]); }
          else if constexpr (k >= 64 && k < 72) { u32 v = pack2(e[2 * (k - 64)], e[2 * (k - 64) + 1]); keep(v); o[(k - 64) >> 2][(k - 64) & 3] = v; }
        });
        if constexpr (s == 27 || s == 28) store_dev(o[s - 27], srd_ws, l16, arow + (s - 27) * 1024);
        sfence();
      });
      STAMP(3);
      __builtin_amdgcn_s_setprio(3);
#pragma unroll
      for (int h = 2; h < 4; ++h) {
        float t2[8];
#pragma unroll
        for (int i = 0; i < 8; ++i) t2[i] = mul1(acc[1][8 * (h & 1) + i], rl);
        sfence();
        store_dev(phi8(t2), srd_ws, l16, arow + h * 1024);
      }
    } else if (nl == 2) {
#pragma unroll
      for (int s = 0; s < 32; ++s) {
        acc[1] = mfma32(wf[(32 + s) & 7], xf[s], acc[1]); keep(acc[1]); sfence();
        if (s + 6 < 32) ldw(32 + s + 6);
        if (s == 27 || s == 29) relu_store(acc[0], (s - 27) >> 1);
        sfence();
      }
      STAMP(3);
      __builtin_amdgcn_s_setprio(3);
      relu_store(acc[1], 2); relu_store(acc[1], 3);
    } else {
      STAMP(3);
      __builtin_amdgcn_s_setprio(3);
      relu_store(acc[0], 0); relu_store(acc[0], 1);
    }
  }
  STAMP(4);
  lds_signal(L_SYNC + 12);
  lds_wait(L_SYNC + 12, 4);
  dma4<0>(make_srd(p.wo, 512 * 1024), vw2, (8 * kq + j) * 16384 + cb, j * 4096);
  if (WO2) dma4<0>(make_srd(p.wo, 512 * 1024), vw2, (8 * kq + 4 + j) * 16384 + cb, (4 + j) * 4096);
  wait_vm<WO2 ? 8 : 4>();
  store_one((u32x4){(u32)tag, (u32)(tag >> 32), 0u, 0u}, srd_ws, WS_FLAGS + ((rt * 2 + (j >> 1)) * 48 + (xt ? 32 : 16) + g * 2 + (j & 1)) * 128);
  if (G2EARLY) lds_signal(L_SYNC + 8);
  STAMP(5);
  const bool kvjob = ((rt ^ j) & 1) == 0;
  if (!xt && (kvjob || rt == 1)) {
    const u32 hd = g * 4 + j, zb = WS_ZP + (hd >> 1) * 4096;
    const u32 sa = kvjob ? WS_KVP + hd * KVH_B + (rt >> 1) * 4096 : zb, fo = kvjob ? WS_KVF + hd * 1024 + (rt >> 1) * 128 : WS_KVZ + hd * 64;
    if (nl == 1) __builtin_amdgcn_s_sleep(ZDLY);
    u32x4 a[4];
#pragma unroll 1
    for (u32 i = 0; i < SPIN_CAP; ++i) {
      asm volatile("buffer_load_dwordx4 %0, %4, %5, %6 offen sc1\n\tbuffer_load_dwordx4 %1, %4, %5, %6 offen offset:1024 sc1\n\t"
                   "buffer_load_dwordx4 %2, %4, %5, %6 offen offset:2048 sc1\n\tbuffer_load_dwordx4 %3, %4, %5, %6 offen offset:3072 sc1\n\ts_waitcnt vmcnt(0)"
                   : "=&v"(a[0]), "=&v"(a[1]), "=&v"(a[2]), "=&v"(a[3]) : "v"(lane * 16), "s"(srd_ws), "s"(sa) : "memory");
      u32 bad = 0;
#pragma unroll
      for (int k = 0; k < 4; ++k) bad |= (a[k].y ^ (u32)tag) | (a[k].w ^ (u32)(tag >> 32));
      if (all_lanes(bad == 0)) break;
      __builtin_amdgcn_s_sleep(RSLP);
    }
      float v[4] = {(lo_f(a[0].x) + lo_f(a[1].x)) + (lo_f(a[2].x) + lo_f(a[3].x)), (hi_f(a[0].x) + hi_f(a[1].x)) + (hi_f(a[2].x) + hi_f(a[3].x)),
                    (lo_f(a[0].z) + lo_f(a[1].z)) + (lo_f(a[2].z) + lo_f(a[3].z)), (hi_f(a[0].z) + hi_f(a[1].z)) + (hi_f(a[2].z) + hi_f(a[3].z))};
#pragma unroll
      for (int i = 0; i < 4; ++i) v[i] = dpp_add<0x128>(v[i]);
#pragma unroll
      for (int i = 0; i < 4; ++i) v[i] = swap16_add(v[i]);
#pragma unroll
      for (int i = 0; i < 4; ++i) v[i] = swap32_add(v[i]);
      store_dev((u32x4){pack2(v[0], v[1]), (u32)tag, pack2(v[2], v[3]), (u32)(tag >> 32)}, srd_ws, lane < 8 ? lane * 16 : OOB, fo);
  }
  if (!G2EARLY) lds_signal(L_SYNC + 8);
  __builtin_amdgcn_s_setprio(0);
  if (!WO2) dma4<0>(make_srd(p.wo, 512 * 1024), vw2, (8 * kq + 4 + j) * 16384 + cb, (4 + j) * 4096);
  STAMP(6);
  const char* fq = p.ws + WS_FLAGS + ((rt * 2 + (j >> 1)) * 48 + 16 + kq * 4 + (j & 1)) * 128;
  const u32 rdy = L_SYNC + 48 + j * 4;
  u32 now = 0;
#define FLAG_LOADS(A, B, C, D, P) "s_load_dwordx2 " A ", " P ", 0 glc\n\ts_load_dwordx2 " B ", " P ", 256 glc\n\ts_load_dwordx2 " C ", " P ", 2048 glc\n\t" \
                                  "s_load_dwordx2 " D ", " P ", 2304 glc\n\ts_waitcnt lgkmcnt(0)"
  auto tell = [&](u64 f0, u64 f1, u64 f2, u64 f3) {
    const u32 nw = ((f0 ^ tag) | (f1 ^ tag)) ? 0 : (((f2 ^ tag) | (f3 ^ tag)) ? 1 : 2);
    if (nw != now) { now = nw; *LDSP(u32, rdy) = nw; }
  };
#pragma unroll 1
  for (u32 i = 0; i < SPIN_CAP && now < KVAFTER; ++i) {
    u64 f0, f1, f2, f3;
    asm volatile(FLAG_LOADS("%0", "%1", "%2", "%3", "%4") : "=&s"(f0), "=&s"(f1), "=&s"(f2), "=&s"(f3) : "s"(fq) : "memory");
    tell(f0, f1, f2, f3);
  }
  wait_vm<0>();
  lds_signal(L_SYNC + 32);
  __builtin_amdgcn_s_setprio(KPRIO);
  STAMP(7);
  u32x4 k0, k1, z0, z1;
#define KV_LOADS "buffer_load_dwordx4 %0, %4, %6, %7 offen sc1\n\tbuffer_load_dwordx4 %1, %4, %6, %7 offen offset:1024 sc1\n\t" \
                 "buffer_load_dwordx4 %2, %5, %6, %8 offen sc1\n\tbuffer_load_dwordx4 %3, %5, %6, %8 offen offset:64 sc1"
  u32 l16 = lane * 16, lz = l4 * 16, ko = WS_KVF + (kq * 8 + j * 2) * 1024, zo = WS_KVZ + (kq * 8 + j * 2) * 64;
  u32x4 srd2 = srd_ws;
#pragma unroll 1
  for (u32 i = 0; i < SPIN_CAP; ++i) {
    u64 f0, f1, f2, f3;
    asm volatile(KV_LOADS "\n\t" FLAG_LOADS("%9", "%10", "%11", "%12", "%13")
                 : "=&v"(k0), "=&v"(k1), "=&v"(z0), "=&v"(z1), "+v"(l16), "+v"(lz), "+s"(srd2), "+s"(ko), "+s"(zo), "=&s"(f0), "=&s"(f1), "=&s"(f2), "=&s"(f3)
                 : "s"(fq) : "memory");
    tell(f0, f1, f2, f3);
    asm volatile("s_waitcnt vmcnt(0)" : "+v"(k0), "+v"(k1), "+v"(z0), "+v"(z1));
    const u32 bad = ((k0.y ^ (u32)tag) | (k1.y ^ (u32)tag) | (z0.y ^ (u32)tag) | (z1.y ^ (u32)tag)) |
                    ((k0.w ^ (u32)(tag >> 32)) | (k1.w ^ (u32)(tag >> 32)) | (z0.w ^ (u32)(tag >> 32)) | (z1.w ^ (u32)(tag >> 32)));
    if (all_lanes(bad == 0)) break;
  }
  STAMP(9);
  {
    auto a0 = __builtin_amdgcn_permlane16_swap(k0.x, z0.x, false, false), a1 = __builtin_amdgcn_permlane16_swap(k0.z, z0.z, false, false);
    auto b0 = __builtin_amdgcn_permlane16_swap(k1.x, z1.x, false, false), b1 = __builtin_amdgcn_permlane16_swap(k1.z, z1.z, false, false);
    *LDSP(u32x4, L_KVF + j * 2048 + lane * 16) = (u32x4){a0[0], a1[0], a0[1], a1[1]};
    *LDSO(u32x4, L_KVF + j * 2048 + lane * 16, 1024) = (u32x4){b0[0], b1[0], b0[1], b1[1]};
    lds_signal(L_SYNC + 36);
  }
#pragma unroll 1
  for (u32 i = 0; i < SPIN_CAP && now < 2; ++i) {
    u64 f0, f1, f2, f3;
    asm volatile(FLAG_LOADS("%0", "%1", "%2", "%3", "%4") : "=&s"(f0), "=&s"(f1), "=&s"(f2), "=&s"(f3) : "s"(fq) : "memory");
    tell(f0, f1, f2, f3);
  }
  STAMP(13);
}

template <bool XT> DEV void group0_first(const Ptrs& p, u32 b, u32 lane, u32 w, u64& tag, u32* stamp) {
  const u32 xcd = b & 7, r = b >> 3, rt = (xcd & 1) * 8 + (r & 7), c4 = (xcd >> 1) * 4 + (r >> 3), g = c4 >> 1;
  const u32 mh = w >> 1, nh = w & 1, hh = lane >> 5, q = (lane & 15) >> 2;
  f32x16 acc[2][2] = {};
  bf16x8 xf[4][2] = {}, wf[4][2] = {};
  u32 xa = (mh * 64 + (lane & 31)) * 128 + ((hh ^ ((lane >> 1) & 7)) << 4);
  u32 wa = 16384 + (8 * hh + q) * 256 + (lane & 16) * 2 + (lane & 3) * 8 + ((XT ? (nh ^ q) : ((nh - q) & 3)) << 6), wb = wa ^ 128;
  u32 ca = L_CORRA + nh * 1024 + lane * 16;
  const u32x4 srd_ws = make_srd(p.ws, WS_END);
  u32x4 vw2 = steps4((lane >> 4) * 1024 + (lane & 3) * 16 + ((q ^ (lane >> 4)) << 6), 4096);
  auto wlate = [&]() {
    dma4<0>(make_srd(p.w2, 1376 * 1024), vw2, (64 + 6 * (c4 >> 2) + w) * 16384 + (c4 & 3) * 256, L_WL + w * 4096);
    if (w < 2) dma4<0>(make_srd(p.w2, 1376 * 1024), vw2, (68 + 6 * (c4 >> 2) + w) * 16384 + (c4 & 3) * 256, L_WL + (4 + w) * 4096);
  };
  auto wchunk = [&](int i) { dma4<0>(make_srd(p.w2, 1376 * 1024), vw2, (16 * (c4 >> 2) + 4 * i + w) * 16384 + (c4 & 3) * 256, L_WE + (4 * i + w) * 4096); };
  u32 ma0 = L_MN + (mh * 64 + (lane & 31)) * 4;
  *LDSP(u32, L_SYNC + (lane & 31) * 4) = 0u;
  if (w == 0) {
    u64 e;
    asm volatile("s_load_dwordx2 %0, %1, 0\n\ts_waitcnt lgkmcnt(0)" : "=&s"(e) : "s"(p.ws) : "memory");
    e += 0x9E3779B97F4A7C15ull;
    *LDSP(u32x2, L_SYNC + 128) = (u32x2){(u32)e, (u32)(e >> 32)};
  }

  asm volatile("" : "+v"(acc[0][0]), "+v"(acc[0][1]), "+v"(acc[1][0]), "+v"(acc[1][1]), "+v"(vw2));
  asm volatile("" : "+v"(xf[2][0]), "+v"(xf[2][1]), "+v"(wf[2][0]), "+v"(wf[2][1]), "+v"(xf[3][0]), "+v"(xf[3][1]), "+v"(wf[3][0]), "+v"(wf[3][1]),
               "+v"(xa), "+v"(wa), "+v"(wb), "+v"(ca), "+v"(ma0));
  STAMP(1);
  auto ldx = [&](int s, int h) { xf[s][h] = lds_b128(xa ^ (s << 5), h * 4096); sfence(); };
  auto ldw = [&](int s, int h) { wf[s][h] = lds_tr<1024>(h ? wb : wa, s * 4096); sfence(); };
  auto m1 = [&](int s, int i) {
    const int t = (i + 1) >> 1 & 1, u = i >> 1;
    acc[t][u] = XT ? mfma32(xf[s][t], wf[s][u], acc[t][u]) : mfma32(wf[s][u], xf[s][t], acc[t][u]);
    sfence();
  };
  __builtin_amdgcn_s_setprio(2);
  barrier();
#pragma unroll 1
  for (u32 st = 0; st < 8; ++st) {
    m1(2, 0); ldw(0, 0); m1(2, 1); ldx(0, 0); ldx(0, 1); m1(2, 2); ldw(0, 1); m1(2, 3); ldw(1, 0);
    m1(3, 0); ldx(1, 0); ldx(1, 1); m1(3, 1); ldw(1, 1); m1(3, 2); ldw(2, 0); m1(3, 3); ldx(2, 0); ldx(2, 1);
    m1(0, 0); ldw(2, 1); m1(0, 1); ldw(3, 0); m1(0, 2); ldx(3, 0); ldx(3, 1); m1(0, 3); ldw(3, 1);
    m1(1, 0);
    xa = (xa + 32768) & 131071; wa = (wa + 32768) & 131071; wb = (wb + 32768) & 131071;
    asm volatile("" : "+v"(xa), "+v"(wa), "+v"(wb));
    sfence();
    m1(1, 1); m1(1, 2); m1(1, 3);
    barrier();
  }
  f32x4 rl[8];
  m1(2, 0);
  const u32x2 tg = *LDSP(u32x2, L_SYNC + 128);
  const u32 n0 = *LDSP(u32, ma0), n1 = *LDSO(u32, ma0, 128);
  wf[0][0] = lds_b128(ca); wf[0][1] = lds_b128(ca, 2048);
  sfence();
  m1(2, 3);
  if (XT) {
    const u32 ra = opaque_v(L_RL + (mh * 64 + hh * 4) * 4);
#pragma unroll
    for (int i = 0; i < 8; ++i) rl[i] = *LDSO(f32x4, ra, i * 32);
    sfence();
  }
  m1(3, 0);
  tag = ((u64)(u32)__builtin_amdgcn_readfirstlane(tg.y) << 32) | (u32)__builtin_amdgcn_readfirstlane(tg.x);
  xf[0][0] = as_bf((u32x4){n0, n0, n0, n0}); xf[0][1] = as_bf((u32x4){n1, n1, n1, n1});
  keep(xf[0][0]); keep(xf[0][1]);
  sfence();
  m1(3, 3); m1(0, 0); m1(0, 3); m1(2, 1); m1(2, 2); m1(3, 1); m1(3, 2); m1(0, 1); m1(0, 2);
  STAMP(2);
  if constexpr (!XT) {
    __builtin_amdgcn_s_setprio(YPRIO);
#pragma unroll
    for (int k = 0; k < 8; ++k) {
      const int t = k >> 2, u = (k >> 1) & 1, s = k & 1;
      u32x4 o;
#pragma unroll
      for (int i = 0; i < 4; ++i) o[i] = relu2(pack2(acc[t][u][8 * s + 2 * i], acc[t][u][8 * s + 2 * i + 1]));
      store_dev(o, srd_ws, lane * 16, WS_A + rt * RT_B + ((mh * 2 + t) * NSTEP + g * 8 + u * 4 + nh * 2 + s) * 1024);
      if (k == YBAR) asm volatile("s_barrier" ::: "memory");
    }
    STAMP(3);
    wchunk(2);
    if (YW2) { wchunk(3); wait_vm<8>(); } else wait_vm<4>();
    store_one((u32x4){(u32)tag, (u32)(tag >> 32), 0u, 0u}, srd_ws, WS_FLAGS + ((rt * 2 + mh) * 48 + g * 2 + nh) * 128);
    __builtin_amdgcn_s_setprio(0);
    if (!YW2) wchunk(3);
    wlate();
  } else {
    f32x16 kv = {}, zz = {};
    const bf16x8 ones = as_bf((u32x4){0x3f803f80u, 0x3f803f80u, 0x3f803f80u, 0x3f803f80u});
    const bool up = lane & 16;
    const u32 kvb = WS_KVP + (g * 4 + nh * 2) * KVH_B + (rt * 2 + mh) * 128;
    const u32 o0 = opaque_v((up ? KVH_B : 0) + hh * 16384 + (lane & 8) * 512 + (lane & 7) * 16);
    const u32 oz = opaque_v((lane & 14) ? OOB : (up ? 64 : 0) + hh * 32 + (lane & 1) * 16);
#pragma unroll
    for (int k = 0; k < 4; ++k) {
      const int t = k >> 1, s = k & 1;
      if (k == XHOLD) asm volatile("s_barrier" ::: "memory");
      if (XDMA == 0 && (k == 1 || k == 2)) wchunk(k + 1);
      if (XDMA == 0 && k == 3) wlate();
      float t2[8], v2[8];
#pragma unroll
      for (int i = 0; i < 8; ++i) t2[i] = mul1(acc[t][0][8 * s + i], rl[2 * k + (i >> 2)][i & 3]);
#pragma unroll
      for (int i = 0; i < 8; ++i) v2[i] = mul1(acc[t][1][8 * s + i], rl[2 * k + (i >> 2)][i & 3]);
      sfence();
      const u32x4 pv = {pack2(v2[0], v2[1]), pack2(v2[2], v2[3]), pack2(v2[4], v2[5]), pack2(v2[6], v2[7])};
      const u32x4 pk = phi8(t2);
      kv = mfma32(as_bf(pk), as_bf(pv), kv);
      zz = mfma32(as_bf(pk), ones, zz);
      sfence();
    }
    if (XHOLD == 4) asm volatile("s_barrier" ::: "memory");
    u32 ks[4], zs[4];
#pragma unroll
    for (int i = 0; i < 4; ++i) { const u32 lo = pack2(kv[2 * i], kv[2 * i + 1]), hi = pack2(kv[8 + 2 * i], kv[9 + 2 * i]); ks[i] = up ? hi : lo; }
    store_dev((u32x4){ks[0], (u32)tag, ks[1], (u32)(tag >> 32)}, srd_ws, o0, kvb);
    store_dev((u32x4){ks[2], (u32)tag, ks[3], (u32)(tag >> 32)}, srd_ws, o0, kvb + 8192);
#pragma unroll
    for (int i = 0; i < 4; ++i) { const u32 lo = pack2(zz[2 * i], zz[2 * i + 1]), hi = pack2(zz[8 + 2 * i], zz[9 + 2 * i]); zs[i] = up ? hi : lo; }
    const bool od = lane & 1;
    store_dev((u32x4){od ? zs[2] : zs[0], (u32)tag, od ? zs[3] : zs[1], (u32)(tag >> 32)}, srd_ws, oz, WS_ZP + (g * 2 + nh) * 4096 + (rt * 2 + mh) * 128);
    if (XHOLD == 5) asm volatile("s_barrier" ::: "memory");
    STAMP(3);
    __builtin_amdgcn_s_setprio(0);
    if (XDMA == 1) { wchunk(2); wchunk(3); wait_vm<8>(); wlate(); }
  }
}

DEV void group0(const Ptrs& p, u32 b, u32 lane, u32 w, u32* stamp) {
  u64 tag;
  if (b & 64) group0_first<false>(p, b, lane, w, tag, stamp); else group0_first<true>(p, b, lane, w, tag, stamp);
  STAMP(5);
  b = opaque_s(b); lane = opaque_v(lane);
  const u32 rt = (b & 1) * 8 + ((b >> 3) & 7), kq = (b >> 1) & 3, ct = b >> 6, hh = lane >> 5, q = (lane & 15) >> 2;
  const __amdgpu_buffer_rsrc_t rws = __builtin_amdgcn_make_buffer_rsrc(p.ws, 0, WS_END, 0x00020000);
  const u32x4 srd_ws = make_srd(p.ws, WS_END);
  const char* fp = p.ws + WS_FLAGS + ((rt * 2 + (w >> 1)) * 48 + kq * 4) * 128;
  const u32 ab = WS_A + rt * RT_B + w * NSTEP * 1024;
  u32 wl[4], we[4], wm[4];
#pragma unroll
  for (int i = 0; i < 4; ++i) {
    wl[i] = opaque_v((4 * hh + q) * 256 + (lane & 16) * 2 + (lane & 3) * 8 + ((i ^ kq ^ q) << 6));
    we[i] = opaque_v(wl[i] + L_WE);
    wm[i] = opaque_v(wl[i] + L_WL);
  }
  const float rlv = *LDSP(float, L_RL + (w * 32 + (lane & 31)) * 4);
  const u32 tok = rt * 128 + w * 32 + (lane & 31), oo = tok * 1024 + ct * 256 + kq * 64 + hh * 16;
  u32x4 xr0, xr1;
  const u32 rdy = opaque_v(L_SYNC + 48 + w * 4), cl = opaque_v(L_SYNC + 32);
  u32x4 af[30];
  const u32 l16 = opaque_v(lane * 16);
  auto lda = [&](int s) {
    const u32 so = s < 16 ? ab + (16 * kq + s) * 1024 : s < 22 ? ((s < 20 || kq < 3) ? ab + (48 + 6 * kq + s) * 1024 : OOB) : ab + (64 + 8 * kq + s) * 1024;
    asm volatile("buffer_load_dwordx4 %0, %1, %2, %3 offen sc1" : "=v"(af[s]) : "v"(l16), "s"(srd_ws), "s"(so) : "memory");
  };
  bf16x8 wf[3][4], kf[8] = {};
  f32x16 acc[4] = {}, d[2];
  auto ldw = [&](int s, int i) { wf[s % 3][i] = lds_tr<2048>(s < 16 ? we[i] : s < 22 ? wm[i] : wl[i], (s < 16 ? s : s < 22 ? s - 16 : s - 22) * 4096); sfence(); };
  auto peek = [&](u32 a, int o) { return __hip_atomic_load(LDSO(u32, a, o), __ATOMIC_RELAXED, __HIP_MEMORY_SCOPE_WORKGROUP); };
  auto pin = [&](f32x16& v) { asm volatile("" : "+v"(v)); sfence(); };
  const float sc = __builtin_amdgcn_rcpf(rlv);
  f32x16 ceps = {0.f, 0.f, 0.f, 0.f, 0.f, 0.f, 0.f, 0.f, 1e-6f, 1e-6f, 1e-6f, 1e-6f, 1e-6f, 1e-6f, 1e-6f, 1e-6f};
  keep(ceps);
  u32 c0 = 0, c1 = 0;
  wait_vm<0>();
  lds_signal(L_SYNC + 4);
  poll4(fp, tag, SPIN_CAP);
  STAMP(6);
  lds_wait(L_SYNC + 4, 4);
  lds_wait(L_SYNC + 12, 4);
#pragma unroll
  for (int s = 0; s < LEAD; ++s) lda(s);
  lds_wait(L_SYNC + 8, 4);
#pragma unroll
  for (int i = 0; i < 8; ++i) ldw(i >> 2, i & 3);
  __builtin_amdgcn_s_setprio(GPRIO);
  STAMP(7);
  static_for<30>([&](auto S_) {
    constexpr int s = S_;
    if (s == 14 - LEAD || s == 20 - LEAD) { c0 = peek(rdy, 0); sfence(); }
    if (s == 16 - LEAD) { if ((u32)__builtin_amdgcn_readfirstlane(c0) < 1) lds_wait(rdy, 1); STAMP(8); }
    if (s == 22 - LEAD) { if ((u32)__builtin_amdgcn_readfirstlane(c0) < 2) lds_wait(rdy, 2); STAMP(9); }
    if constexpr (s + LEAD < 30) lda(s + LEAD);
    if (s == 17) { c0 = peek(cl, 0); sfence(); }
    if (s == 19 && (u32)__builtin_amdgcn_readfirstlane(c0) < 4) lds_wait(cl, 4);
    if (s == 19) { c1 = peek(cl, 4); sfence(); }
    constexpr int h = s - 22;
    float f, m[8];
    auto scale = [&](const f32x16& dd) { f = mul1(sc, __builtin_amdgcn_rcpf(dd[8])); sfence(); };
    auto mul4 = [&](f32x16& dd, int o) { for (int i = o; i < o + 4; ++i) m[i] = mul1(dd[i], f); if (o) keep(dd); sfence(); };
    auto fin = [&](u32x4& o) { o = (u32x4){pack2(m[0], m[1]), pack2(m[2], m[3]), pack2(m[4], m[5]), pack2(m[6], m[7])}; keep(o); sfence(); };
    if constexpr (s == 21) {
      if ((u32)__builtin_amdgcn_readfirstlane(c1) >= 4) {
#pragma unroll
        for (int i = 0; i < 8; ++i) kf[i] = lds_b128(L_KVF + lane * 16, i * 1024);
      }
      sfence();
    }
    if constexpr (s == 22) {
      if ((u32)__builtin_amdgcn_readfirstlane(c1) < 4) {
        lds_wait(cl + 4, 4);
#pragma unroll
        for (int i = 0; i < 8; ++i) kf[i] = lds_b128(L_KVF + lane * 16, i * 1024);
      }
      STAMP(4);
      need<s, 22>(af[22]); need<s, 23>(af[23]);
      d[0] = mfma32(kf[0], as_bf(af[22]), ceps); pin(d[0]);
      d[1] = mfma32(kf[1], as_bf(af[23]), ceps); pin(d[1]);
      scale(d[0]); mul4(d[0], 0); mul4(d[0], 4); fin(af[22]);
    }
    constexpr bool at = h >= 0 && h < 7;
    if constexpr (s < 22) need<s, s>(af[s]);
    if constexpr (h >= 0 && h < 6) { need<s, s + 2>(af[s + 2]); d[h & 1] = mfma32(kf[h + 2], as_bf(af[s + 2]), ceps); pin(d[h & 1]); }
    if constexpr (at) scale(d[(h + 1) & 1]);
    acc[1] = mfma32(wf[s % 3][1], as_bf(af[s]), acc[1]); pin(acc[1]);
    if (s + 2 < 30) ldw(s + 2, 1);
    if constexpr (at) mul4(d[(h + 1) & 1], 0);
    acc[2] = mfma32(wf[s % 3][2], as_bf(af[s]), acc[2]); pin(acc[2]);
    if (s + 2 < 30) ldw(s + 2, 2);
    if constexpr (at) mul4(d[(h + 1) & 1], 4);
    acc[3] = mfma32(wf[s % 3][3], as_bf(af[s]), acc[3]); pin(acc[3]);
    if (s + 2 < 30) ldw(s + 2, 3);
    if constexpr (s < 22) { acc[0] = mfma32(wf[s % 3][0], as_bf(af[s]), acc[0]); pin(acc[0]); }
    if constexpr (s < 20) ldw(s + 2, 0);
    if constexpr (at) fin(af[s + 1]);
  });
  STAMP(10);
  bf16x8 w0[8];
  const u32 xb = WS_X + rt * RT_B + (ct * 4 + w) * 32768;
#pragma unroll
  for (int i = 1; i < 4; ++i)
#pragma unroll
    for (int k = 0; k < 2; ++k)
      store_dev((u32x4){pack2(acc[i][8 * k], acc[i][8 * k + 1]), pack2(acc[i][8 * k + 2], acc[i][8 * k + 3]), pack2(acc[i][8 * k + 4], acc[i][8 * k + 5]),
                        pack2(acc[i][8 * k + 6], acc[i][8 * k + 7])}, srd_ws, lane * 16, xb + ((kq ^ i) * 4 + kq) * 2048 + k * 1024);
  asm volatile("global_load_dwordx4 %0, %2, %3\n\tglobal_load_dwordx4 %1, %2, %3 offset:32" : "=&v"(xr0), "=&v"(xr1) : "v"(oo), "s"(p.x) : "memory");
#pragma unroll
  for (int s = 0; s < 8; ++s) w0[s] = lds_tr<2048>(wl[0], s * 4096);
  sfence();
#pragma unroll
  for (int s = 0; s < 8; ++s) { acc[0] = mfma32(w0[s], as_bf(af[22 + s]), acc[0]); pin(acc[0]); }
  wait_vm<2>();
  const u32 xf = WS_XFL + ((rt * 4 + ct) * 4 + w) * 512;
  store_one((u32x4){(u32)tag, (u32)(tag >> 32), 0u, 0u}, srd_ws, xf + kq * 128);
  asm volatile("s_waitcnt vmcnt(1)" : "+v"(xr0), "+v"(xr1));
  u32x4 perm0, perm1;
  {
    const u32 row = lane & 15, e = (row & 3) + ((row >> 3) << 2), hit = ((row >> 2) & 1) == hh ? 0x3f80u << (16 * (e & 1)) : 0u;
#pragma unroll
    for (int i = 0; i < 4; ++i) { const u32 t = (e >> 1) == (u32)i ? hit : 0u; perm0[i] = (lane & 16) ? 0u : t; perm1[i] = (lane & 16) ? t : 0u; }
    keep(perm0); keep(perm1);
  }
  float xv[16];
#pragma unroll
  for (int e = 0; e < 4; ++e) { xv[2 * e] = lo_f(xr0[e]); xv[2 * e + 1] = hi_f(xr0[e]); xv[8 + 2 * e] = lo_f(xr1[e]); xv[9 + 2 * e] = hi_f(xr1[e]); }
#pragma unroll
  for (int e = 0; e < 16; ++e) keep(xv[e]);
  STAMP(11);
  poll4(p.ws + xf, tag, SPIN_CAP);
  STAMP(12);
  u32x4 pp[3][2];
#pragma unroll
  for (int i = 0; i < 3; ++i) {
    pp[i][0] = __builtin_amdgcn_raw_buffer_load_b128(rws, lane * 16, xb + (kq * 4 + (kq ^ (i + 1))) * 2048, 16);
    pp[i][1] = __builtin_amdgcn_raw_buffer_load_b128(rws, lane * 16 + 1024, xb + (kq * 4 + (kq ^ (i + 1))) * 2048, 16);
  }
  sfence();
#pragma unroll
  for (int i = 0; i < 3; ++i) { acc[0] = mfma32(as_bf(perm0), as_bf(pp[i][0]), acc[0]); acc[0] = mfma32(as_bf(perm1), as_bf(pp[i][1]), acc[0]); }
  float v[16];
#pragma unroll
  for (int e = 0; e < 16; ++e) v[e] = acc[0][e];
  const float rs = rlv * LN2;
#pragma unroll
  for (int k = 0; k < 2; ++k) {
#pragma unroll
    for (int e = 0; e < 4; ++e) {
      auto sw = __builtin_amdgcn_permlane32_swap(asu(v[8 * k + e]), asu(v[8 * k + 4 + e]), false, false);
      v[8 * k + e] = asf(sw[0]); v[8 * k + 4 + e] = asf(sw[1]);
    }
    u32x4 o;
#pragma unroll
    for (int e = 0; e < 4; ++e) o[e] = pack2(__builtin_fmaf(v[8 * k + 2 * e], rs, xv[8 * k + 2 * e]), __builtin_fmaf(v[8 * k + 2 * e + 1], rs, xv[8 * k + 2 * e + 1]));
    *(GAS u32x4*)(p.out + oo + k * 32) = o;
  }
  if (b == 0 && w == 0) store_one((u32x4){(u32)tag, (u32)(tag >> 32), 0u, 0u}, srd_ws, WS_EPOCH);
  STAMP(13);
}

__global__ __launch_bounds__(512, 2) void fused_block(const char* __restrict__ x, const char* __restrict__ wqkv, const char* __restrict__ w1,
                                                      char* ws, const char* __restrict__ w2, const char* __restrict__ wo, char* __restrict__ out) {
  __shared__ __attribute__((aligned(16))) char lds_all[L_TOTAL];
  u32 stamp[32] = {};
  u64 pc;
  asm volatile("s_getpc_b64 %0" : "=s"(pc) : "s"((u32)(size_t)lds_all));
  STAMP(0);
  const u32 w = __builtin_amdgcn_readfirstlane(__builtin_amdgcn_workitem_id_x() >> 6);
  {
    u32 d0, d1, d2, d3;
    const u64 cp = (pc & ~127ull) + umin((__builtin_amdgcn_workgroup_id_x() >> 3) * 8 + w * 2, CODE_LINES - 2) * 128;
    asm volatile("s_cmp_gt_u32 %5, 3\n\ts_cbranch_scc1 1f\n\ts_load_dword %0, %4, 0\n\ts_load_dword %1, %4, 64\n\ts_load_dword %2, %4, 128\n\t"
                 "s_load_dword %3, %4, 192\n\ts_waitcnt lgkmcnt(0)\n1:"
                 : "=&s"(d0), "=&s"(d1), "=&s"(d2), "=&s"(d3) : "s"(cp), "s"(w) : "memory", "scc");
  }
  asm volatile("" ::"s"(x), "s"(wqkv), "s"(w1), "s"(ws), "s"(w2), "s"(wo), "s"(out));
  const Ptrs p = {x, wqkv, w1, ws, w2, wo, out};
  if (w < 4) group0(p, opaque_s(__builtin_amdgcn_workgroup_id_x()), opaque_v(__builtin_amdgcn_workitem_id_x()) & 63, w, stamp);
  else group1(p, __builtin_amdgcn_workgroup_id_x(), __builtin_amdgcn_workitem_id_x() & 63, w, stamp);
  const u32 b = opaque_s(__builtin_amdgcn_workgroup_id_x()), lane = opaque_v(__builtin_amdgcn_workitem_id_x()) & 63;
  { u32 lim = 4000; if (stamp[1] && (w >> 2) == PGRP && ((b >> 6) & 1) == PTYPE) lim += 10 * (stamp[1] - stamp[0]);
    while ((u32)__builtin_amdgcn_s_memrealtime() - stamp[0] < lim) __builtin_amdgcn_s_sleep(2); }
}

#if defined(__HIP_DEVICE_COMPILE__)
#pragma clang attribute pop
#endif

extern "C" void kernel_launch(void* const* d_in, const int* in_sizes, int n_in, void* d_out, int out_size, void* d_ws, size_t ws_size,
                              hipStream_t stream) {
  fused_block<<<256, 512, 0, stream>>>((const char*)d_in[0], (const char*)d_in[1], (const char*)d_in[4], (char*)d_ws, (const char*)d_in[5],
                                       (const char*)d_in[2], (char*)d_out);
}
```

```cpp
#define WSPOS 2
#include <hip/hip_runtime.h>
#include <hip/hip_bf16.h>
#include <type_traits>

#if defined(__HIP_DEVICE_COMPILE__)
#pragma clang attribute push(__attribute__((target("no-packed-fp32-ops"))), apply_to = function)
#endif
typedef unsigned u32;
typedef unsigned long long u64;
typedef __bf16 bf16x8 __attribute__((ext_vector_type(8)));
typedef __bf16 bf16x2 __attribute__((ext_vector_type(2)));
typedef short s16x4 __attribute__((ext_vector_type(4)));
typedef short s16x2 __attribute__((ext_vector_type(2)));
typedef float f32x16 __attribute__((ext_vector_type(16)));
typedef float f32x4 __attribute__((ext_vector_type(4)));
typedef float f32x2 __attribute__((ext_vector_type(2)));
typedef u32 u32x4 __attribute__((ext_vector_type(4)));
typedef u32 u32x2 __attribute__((ext_vector_type(2)));
#define LAS __attribute__((address_space(3)))
#define GAS __attribute__((address_space(1)))
#define DEV __device__ __forceinline__
#define RLX_AGENT __ATOMIC_RELAXED, __HIP_MEMORY_SCOPE_AGENT
#define LDSP(T, a) ((LAS T*)(size_t)(a))

#define ROT1 1
#define PF 1
#define QWIN 3
#define AWIN 5
#define DLY 0
#define SLP 2
#define PSC "sc1"
#define SL 8
#define SO 12
#define YPRIO 0
#define PFMODE 0
#define GATE 1
#define KVMODE 6
#define STATST 0
#define SPINSLP "s_sleep 1\n\t"
#define LORD 2
#define GST 5
#define YW2 1
#define YBAR 1
#define KVAFTER 2
#define ZDLY 2
#define RSLP 2
#define XHOLD 4
#define KPRIO 0
#define GPRIO 1
#define G2EARLY 0
#define PFS 0
#define PFN 5
#define XDMA 1
#define BARS 10
#define WO2 0
#define LEAD 8
#define WS_OFF(n) (WSPOS == 0 ? (size_t)0 : (WSPOS == 1 ? (n) / 2 : (n) - (48u << 20)) & ~(size_t)0x1fffff)
#define PRIO2 1

constexpr u32 WS_EPOCH = 0;
constexpr u32 WS_FLAGS = 4096;
constexpr u32 WS_XFL = WS_FLAGS + 32 * 48 * 128;
constexpr u32 WS_KVF = WS_XFL + 1024 * 128;
constexpr u32 WS_KVZ = WS_KVF + 32768;
constexpr u32 WS_ZP = WS_KVZ + 2048;
constexpr u32 WS_KVP = WS_ZP + 65536;
constexpr u32 KVH_B = 32768;
constexpr u32 WS_CS = 3u << 19;
constexpr u32 RT_B = 1u << 20;
constexpr u32 WS_A = 2u << 20;
constexpr u32 NSTEP = 118;
constexpr u32 WS_STATS = WS_A + 4 * NSTEP * 1024;
constexpr u32 WS_X = WS_A + 524288;
constexpr u32 WS_END = WS_A + 16 * RT_B;
constexpr u32 WS_STAMP = 32u << 20;
static_assert(WS_KVP + 32 * KVH_B <= WS_CS && WS_CS + 524288 <= WS_A, "ws map");
constexpr u32 OOB = 0x40000000u;

constexpr u32 L_WE = 65536;
constexpr u32 L_SCR = 131072;
constexpr u32 L_CORRA = L_SCR;
constexpr u32 L_CORRB = L_SCR + 4096;
constexpr u32 L_MN = L_SCR + 6144;
constexpr u32 L_RL = L_SCR + 6656;
constexpr u32 L_SYNC = L_SCR + 7168;
constexpr u32 L_JUNK = L_SCR + 7424;
constexpr u32 L_WL = L_SCR + 8192;
constexpr u32 L_KVF = 32768;
constexpr u32 L_FINE = 49152;
constexpr u32 L_TOTAL = 163840;

constexpr u32 CODE_LINES = 180;
constexpr u32 SPIN_CAP = 1u << 18;
constexpr u32 HINT_CAP = 1u << 12;
constexpr float LOG2E = 1.4426950408889634f, LN2 = 0.6931471805599453f;

DEV float asf(u32 v) { return __builtin_bit_cast(float, v); }
DEV u32 asu(float v) { return __builtin_bit_cast(u32, v); }
DEV bool all_lanes(bool v) { return __builtin_amdgcn_ballot_w64(!v) == 0; }
DEV u32 umin(u32 a, u32 b) { return a < b ? a : b; }
DEV void sfence() { __builtin_amdgcn_sched_barrier(0); }
template <int N> DEV void wait_vm() { asm volatile("s_waitcnt vmcnt(%0)" ::"i"(N) : "memory"); }
DEV void wait_lgkm() { sfence(); __builtin_amdgcn_s_waitcnt(0xC07F); asm volatile("" ::: "memory"); sfence(); }
DEV void barrier() { sfence(); __builtin_amdgcn_s_waitcnt(0xC07F); asm volatile("s_barrier" ::: "memory"); sfence(); }

DEV u32x4 make_srd(const void* p, u32 bytes) {
  u64 a = (u64)p;
  return (u32x4){(u32)a, (u32)(a >> 32) & 0xffffu, bytes, 0x00020000u};
}
template <int POL> DEV void dma(u32x4 srd, u32 voff, u32 soff, u32 ldsaddr) {
  if (POL == 1)
    asm volatile("s_mov_b32 m0, %3\n\ts_nop 0\n\tbuffer_load_dwordx4 %0, %1, %2 offen nt lds" ::"v"(voff), "s"(srd), "s"(soff), "s"(ldsaddr) : "memory");
  else if (POL == 2)
    asm volatile("s_mov_b32 m0, %3\n\ts_nop 0\n\tbuffer_load_dwordx4 %0, %1, %2 offen sc1 lds" ::"v"(voff), "s"(srd), "s"(soff), "s"(ldsaddr) : "memory");
  else
    asm volatile("s_mov_b32 m0, %3\n\ts_nop 0\n\tbuffer_load_dwordx4 %0, %1, %2 offen lds" ::"v"(voff), "s"(srd), "s"(soff), "s"(ldsaddr) : "memory");
}
template <int POL> DEV void dma4(u32x4 srd, u32x4 v, u32 soff, u32 ldsaddr) {
  if (POL == 1)
    asm volatile("s_mov_b32 m0, %6\n\ts_nop 0\n\tbuffer_load_dwordx4 %0, %4, %5 offen nt lds\n\tbuffer_load_dwordx4 %1, %4, %5 offen offset:1024 nt lds\n\t"
                 "buffer_load_dwordx4 %2, %4, %5 offen offset:2048 nt lds\n\tbuffer_load_dwordx4 %3, %4, %5 offen offset:3072 nt lds"
                 ::"v"(v.x), "v"(v.y), "v"(v.z), "v"(v.w), "s"(srd), "s"(soff), "s"(ldsaddr) : "memory");
  else
    asm volatile("s_mov_b32 m0, %6\n\ts_nop 0\n\tbuffer_load_dwordx4 %0, %4, %5 offen lds\n\tbuffer_load_dwordx4 %1, %4, %5 offen offset:1024 lds\n\t"
                 "buffer_load_dwordx4 %2, %4, %5 offen offset:2048 lds\n\tbuffer_load_dwordx4 %3, %4, %5 offen offset:3072 lds"
                 ::"v"(v.x), "v"(v.y), "v"(v.z), "v"(v.w), "s"(srd), "s"(soff), "s"(ldsaddr) : "memory");
}
DEV void dma2(u32x4 srd, u32 v0, u32 v1, u32 soff, u32 ldsaddr) {
  asm volatile("s_mov_b32 m0, %4\n\ts_nop 0\n\tbuffer_load_dwordx4 %0, %2, %3 offen lds\n\tbuffer_load_dwordx4 %1, %2, %3 offen offset:1024 lds"
               ::"v"(v0), "v"(v1), "s"(srd), "s"(soff), "s"(ldsaddr) : "memory");
}
DEV void touch64(u32x4 srd, u32 voff, u32 soff, u32 ldsaddr) {
  asm volatile("s_mov_b32 m0, %3\n\ts_nop 0\n\tbuffer_load_dword %0, %1, %2 offen lds" ::"v"(voff), "s"(srd), "s"(soff), "s"(ldsaddr) : "memory");
}
DEV u32x4 steps4(u32 v, u32 step) { step -= 1024; return (u32x4){v, v + step, v + 2 * step, v + 3 * step}; }
DEV void store_one(u32x4 v, u32x4 srd, u32 soff) {
  asm volatile("s_mov_b64 exec, 1\n\tbuffer_store_dwordx4 %0, off, %1, %2 sc1\n\ts_mov_b64 exec, -1\n\ts_nop 0" ::"v"(v), "s"(srd), "s"(soff) : "memory");
}
DEV void store_dev(u32x4 v, u32x4 srd, u32 voff, u32 soff) {
  asm volatile("buffer_store_dwordx4 %0, %1, %2, %3 offen sc1\n\ts_nop 1" ::"v"(v), "v"(voff), "s"(srd), "s"(soff) : "memory");
}
#define LDSO(T, a, o) ((LAS T*)(LDSP(char, a) + (o)))
DEV bf16x8 lds_b128(u32 a, int o = 0) { return *LDSO(bf16x8, a, o); }
template <int D> DEV bf16x8 lds_tr(u32 a, int o) {
  s16x4 lo = __builtin_amdgcn_ds_read_tr16_b64_v4i16(LDSO(s16x4, a, o));
  s16x4 hi = __builtin_amdgcn_ds_read_tr16_b64_v4i16(LDSO(s16x4, a, o + D));
  return __builtin_bit_cast(bf16x8, __builtin_shufflevector(lo, hi, 0, 1, 2, 3, 4, 5, 6, 7));
}
DEV f32x16 mfma32(bf16x8 a, bf16x8 b, f32x16 c) { return __builtin_amdgcn_mfma_f32_32x32x16_bf16(a, b, c, 0, 0, 0); }
DEV f32x4 mfma16(bf16x8 a, bf16x8 b, f32x4 c) { return __builtin_amdgcn_mfma_f32_16x16x32_bf16(a, b, c, 0, 0, 0); }
DEV bf16x8 as_bf(u32x4 v) { return __builtin_bit_cast(bf16x8, v); }
DEV u32 pack2(float a, float b) { return __builtin_bit_cast(u32, __builtin_convertvector((f32x2){a, b}, bf16x2)); }
DEV u32 relu2(u32 v) { return __builtin_bit_cast(u32, __builtin_elementwise_max(__builtin_bit_cast(s16x2, v), (s16x2){0, 0})); }
DEV float lo_f(u32 v) { return asf(v << 16); }
DEV float hi_f(u32 v) { return asf(v & 0xffff0000u); }
DEV float rbf(float f) { return lo_f(pack2(f, 0.f)); }
DEV u32 split_hi_lo(float c) { float h = rbf(c); return pack2(h, c - h); }
DEV float mul1(float a, float b) { float r = a * b; asm("" : "+v"(r)); return r; }
template <int CTRL> DEV float dpp_add(float v) {
  return v + asf(__builtin_amdgcn_update_dpp(0u, asu(v), CTRL, 0xf, 0xf, true));
}
DEV float swap16_add(float v) {
  auto r = __builtin_amdgcn_permlane16_swap(asu(v), asu(v), false, false);
  return asf(r[0]) + asf(r[1]);
}
DEV float swap32_add(float v) {
  auto r = __builtin_amdgcn_permlane32_swap(asu(v), asu(v), false, false);
  return asf(r[0]) + asf(r[1]);
}
DEV float sum32(float v) {
  v = dpp_add<0xB1>(v); v = dpp_add<0x4E>(v); v = dpp_add<0x141>(v); v = dpp_add<0x140>(v);
  return swap16_add(v);
}
DEV float max_eps(float t2) { float m; asm("v_max_f32 %0, %1, %2" : "=v"(m) : "s"(1e-6f * LOG2E), "v"(t2)); return m; }
DEV u32x4 phi8(const float (&t2)[8]) {
  float e[8], m[8];
#pragma unroll
  for (int i = 0; i < 8; ++i) e[i] = __builtin_amdgcn_fmed3f(__builtin_amdgcn_exp2f(t2[i]), 0.f, 1.f);
  sfence();
#pragma unroll
  for (int i = 0; i < 8; ++i) asm("v_max_f32 %0, %1, %2" : "=v"(m[i]) : "s"(1e-6f * LOG2E), "v"(t2[i]));
  sfence();
#pragma unroll
  for (int i = 0; i < 8; ++i) { e[i] = __builtin_fmaf(m[i], LN2, e[i]); asm("" : "+v"(e[i])); }
  sfence();
  return (u32x4){pack2(e[0], e[1]), pack2(e[2], e[3]), pack2(e[4], e[5]), pack2(e[6], e[7])};
}
DEV void lds_signal(u32 addr, u32 inc = 1u) {
  asm volatile("s_mov_b64 exec, 1\n\tds_add_u32 %0, %1\n\ts_mov_b64 exec, -1" ::"v"(addr), "v"(inc) : "memory");
}
DEV void lds_wait(u32 addr, u32 waves) {
  u32 v, s, n = SPIN_CAP;
  asm volatile("1:\n\tds_read_b32 %0, %3\n\ts_waitcnt lgkmcnt(0)\n\tv_readfirstlane_b32 %1, %0\n\ts_sub_u32 %2, %2, 1\n\ts_cbranch_scc1 2f\n\t"
               "s_cmp_lt_u32 %1, %4\n\ts_cbranch_scc1 1b\n2:"
               : "=&v"(v), "=&s"(s), "+s"(n) : "v"(addr), "s"(waves) : "memory", "scc");
}
DEV u64 poll4(const char* p, u64 tag, u32 n) {
  u64 f0, f1, f2, f3;
  asm volatile("1:\n\ts_load_dwordx2 %0, %5, 0 glc\n\ts_load_dwordx2 %1, %5, 128 glc\n\ts_load_dwordx2 %2, %5, 256 glc\n\t"
               "s_load_dwordx2 %3, %5, 384 glc\n\ts_waitcnt lgkmcnt(0)\n\t"
               "s_xor_b64 %0, %0, %6\n\ts_xor_b64 %1, %1, %6\n\ts_xor_b64 %2, %2, %6\n\ts_xor_b64 %3, %3, %6\n\t"
               "s_or_b64 %0, %0, %1\n\ts_or_b64 %2, %2, %3\n\ts_or_b64 %0, %0, %2\n\ts_cbranch_scc0 2f\n\t"
               "s_sub_u32 %4, %4, 1\n\ts_cbranch_scc0 1b\n2:"
               : "=&s"(f0), "=&s"(f1), "=&s"(f2), "=&s"(f3), "+s"(n) : "s"(p), "s"(tag) : "memory", "scc");
  return f0;
}
template <int D> DEV void poll2(const char* p, u64 tag, u64 mask) {
  u64 f0, f1; u32 n = SPIN_CAP;
  asm volatile("1:\n\ts_load_dwordx2 %0, %3, 0 glc\n\ts_load_dwordx2 %1, %3, %6 glc\n\ts_waitcnt lgkmcnt(0)\n\t"
               "s_xor_b64 %0, %0, %4\n\ts_xor_b64 %1, %1, %4\n\ts_or_b64 %0, %0, %1\n\ts_and_b64 %0, %0, %5\n\ts_cbranch_scc0 2f\n\t"
               "s_sub_u32 %2, %2, 1\n\ts_cbranch_scc0 1b\n2:"
               : "=&s"(f0), "=&s"(f1), "+s"(n) : "s"(p), "s"(tag), "s"(mask), "i"(D) : "memory", "scc");
}
DEV u32x4 load_dev_wait(u32x4 srd, u32 voff, u32 soff) {
  u32x4 v;
  asm volatile("buffer_load_dwordx4 %0, %1, %2, %3 offen sc1\n\ts_waitcnt vmcnt(0)" : "=&v"(v) : "v"(voff), "s"(srd), "s"(soff) : "memory");
  return v;
}
template <int N, int I = 0, class F> DEV void static_for(F&& f) {
  if constexpr (I < N) { f(std::integral_constant<int, I>{}); static_for<N, I + 1>(f); }
}
template <int T, int K> DEV void need(u32x4& v) {
  asm volatile("s_waitcnt vmcnt(%1)" : "+v"(v) : "i"((T + LEAD < 30 ? T + LEAD : 29) - K));
}
template <class T> DEV void keep(T& v) { asm volatile("" : "+v"(v)); }
template <class T> DEV T opaque_v(T v) { asm volatile("" : "+v"(v)); return v; }
template <class T> DEV T opaque_s(T v) { asm volatile("" : "+s"(v)); return v; }

#define STAMP(i)
#define FSTAMP(w, i)

struct Ptrs { const char *x, *wqkv, *w1; char* ws; const char *w2, *wo; char* out; };

DEV void group1(const Ptrs& p, u32 b, u32 lane, u32 w, u32* stamp) {
  const u32 xcd = b & 7, r = b >> 3, j = w & 3;
  const u32 rt = (xcd & 1) * 8 + (r & 7);
  const u32 rot = (r & 7) + (xcd & 6);
  const u32x4 srd_x = make_srd(p.x, 2048 * 1024);
  const u32 vx = (lane >> 3) * 1024 + (((lane & 7) ^ (lane >> 4)) << 4);
  const u32x4 vx4 = {vx, (vx ^ 64) + 7168, vx + 14336, (vx ^ 64) + 21504};
  auto issue_x = [&](int st) { dma4<0>(srd_x, vx4, (rt * 128 + 32 * j) * 1024 + ((st + rot) & 7) * 128, (st & 3) * 32768 + j * 4096); };
  issue_x(0);
  sfence();
  const u32 c4 = (xcd >> 1) * 4 + (r >> 3), g = c4 >> 1;
  const bool xt = (c4 & 1) == 0;
  const u32 hh = lane >> 5, q = (lane & 15) >> 2;
  const u32 wst = xt ? 3072 : 2752;
  const u32x4 srd_w = make_srd(xt ? p.wqkv : p.w1, 512 * wst);
  const u32 ym = xt ? 0u : ~0u, l4 = lane >> 4;
  const u32 blk = ((q ^ l4) + (((q & l4) << 1) & ym)) & 3;
  const u32x4 vwa4 = steps4(l4 * wst + (lane & 3) * 16 + (blk & 1) * 64 + (blk >> 1) * (xt ? 1024 : 128) + (xt ? 1024 + g * 128 : g * 256), 4 * wst);
  auto issue_w = [&](int st) { dma4<0>(srd_w, vwa4, (((st + rot) & 7) * 64 + 16 * j) * wst, (st & 3) * 32768 + 16384 + j * 4096); };
  issue_w(0);
  issue_x(1); issue_w(1);
  sfence();
  const u32 nl = (xt || (g < 6 && !(g & 1))) ? 2 : 1, lstep = xt ? 86 + g * 4 : 64 + (g >> 1) * 6 + ((g & 1) ? (g < 6 ? 4 : 2) : 0);
  const u32x4 vwb4 = steps4((lane >> 3) * wst + ((((lane & 7) ^ ((lane >> 2) & 4)) << 4) & (nl == 2 ? 127 : 48)) + (xt ? g * 128 : lstep * 32), 8 * wst);
  auto issue = [&](int st, int part) {
    if (st < 8) { if (part) issue_w(st); else issue_x(st); }
    else dma4<0>(srd_w, vwb4, ((((st - 8) * 4 + j + rot) & 7) * 64 + 32 * part) * wst, (st - 8) * 32768 + j * 8192 + part * 4096);
  };
  dma2(srd_w, vwb4.x, vwb4.y, ((rot & 7) * 64 + 16 * j) * wst, L_WL + j * 2048);
  const u32x4 srd_pf = j == 3 ? make_srd(p.wo, 512 * 1024) : make_srd(p.w2, 1376 * 1024);
  const u32 vpf = lane < (j == 2 ? 24 : 32) ? lane * 128 : OOB;
  const u32 spf = j < 2 ? (c4 >> 2) * 262144 + r * 8192 + j * 4096 : j == 2 ? (64 + 6 * (c4 >> 2)) * 16384 + r * 3072 : (c4 >> 2) * 131072 + r * 4096;
  const u32 pfst = opaque_s(PFS + r % PFN);
  const u32x4 srd_ws = make_srd(p.ws, WS_END);
  const u32 xa = (j * 32 + (lane & 31)) * 128 + ((hh ^ ((lane >> 1) & 7)) << 4);
  const u32 wb0 = opaque_v(hh * 1024 + q * 128 + (lane & 16) * 2 + (lane & 3) * 8 + ((q & 2) << 5)), wb1 = opaque_v(wb0 ^ 64);
  const u32 kq = c4 >> 2, cb = (c4 & 3) * 256;
  const u32x4 vw2 = steps4(l4 * 1024 + (lane & 3) * 16 + ((q ^ l4) << 6), 4096);
  const u32 csb = WS_CS + (c4 * 2 + (xcd & 1)) * 16384 + j * 512;
  bf16x8 xf[32], cw[12];
  u32x4 gr[6];
  u64 tag;
  u32 mn;
  float rl;
  f32x4 s1[2] = {}, s2[2] = {}, ca = {}, cb2 = {};
#define GRAN_LOADS "buffer_load_dwordx4 %0, %6, %8, %9 offen sc1\n\tbuffer_load_dwordx4 %1, %6, %8, %9 offen offset:2048 sc1\n\t" \
                   "buffer_load_dwordx4 %2, %6, %8, %10 offen sc1\n\tbuffer_load_dwordx4 %3, %6, %8, %10 offen offset:2048 sc1\n\t" \
                   "buffer_load_dwordx4 %4, %7, %8, %11 offen sc1\n\tbuffer_load_dwordx4 %5, %7, %8, %11 offen offset:2048 sc1"
#define GRAN_OPS : "=&v"(gr[0]), "=&v"(gr[1]), "=&v"(gr[2]), "=&v"(gr[3]), "=&v"(gr[4]), "=&v"(gr[5]) \
                 : "v"(hh * 8192 + (lane & 31) * 16), "v"(hh * 4096 + (lane & 31) * 16), "s"(srd_ws), "s"(csb), "s"(csb + 4096), "s"(WS_STATS + rt * RT_B + j * 512) : "memory"
#pragma unroll
  for (int st = 0; st < 8; ++st) {
    const int n = st + 2;
    constexpr int extra[8] = {2, 0, 0, 3, 0, 0, 6, 0};
    if (PF && st > PFS && st <= PFS + PFN)
      asm volatile("s_cmp_eq_u32 %0, %1\n\ts_cbranch_scc1 1f\n\ts_waitcnt vmcnt(%2)\n1:\n\ts_waitcnt vmcnt(%3)" ::"s"(pfst), "i"(st - 1), "i"(8 + extra[st]), "i"(9 + extra[st]) : "memory", "scc");
    else asm volatile("s_waitcnt vmcnt(%0)" ::"i"(8 + extra[st]) : "memory");
    barrier();
    if (st == 0) STAMP(1);
    const u32 slot = (st & 3) * 32768;
    if (st == 5) asm volatile(GRAN_LOADS GRAN_OPS);
    if (PF && st >= PFS && st < PFS + PFN && st == pfst) touch64(srd_pf, vpf, spf, L_JUNK);
    if (st < 7) issue(n, 0);
    sfence();
    u32x2 tg;
    if (st == 0) tg = *LDSP(u32x2, L_SYNC + 128);
    if (st < 2) {
      const u32 xs = (j * 32 + (lane & 15)) * 128 + ((l4 ^ ((lane >> 1) & 7)) << 4);
#pragma unroll
      for (int u = 0; u < 4; ++u) cw[8 + u] = lds_b128(xs ^ ((u & 2) << 5), slot + (u & 1) * 2048);
      const u32 wa = 16384 + (8 * hh + q) * 256 + (lane & 16) * 2 + (lane & 3) * 8 + ((xt ? (j ^ q) : ((j - q) & 3)) << 6), wc = L_WL + (wb0 ^ ((j & 1) << 6));
#pragma unroll
      for (int s = 0; s < 4; ++s) cw[s] = st ? lds_tr<512>(wc, s * 2048) : lds_tr<1024>(wa, s * 4096);
    }
#pragma unroll
    for (int s = 0; s < 4; ++s) xf[st * 4 + s] = lds_b128((xa ^ (s << 5)) + (slot & 65536), slot & 32768);
    sfence();
    if (st == 2) {
      auto pick = [&](const f32x4& v) { const float a = opaque_v(v[0]), c = opaque_v(v[2]); const float lo = (lane & 1) ? v[1] : a, hi = (lane & 1) ? v[3] : c; return (lane & 2) ? hi : lo; };
      __builtin_amdgcn_s_setprio(3);
      store_dev((u32x4){split_hi_lo(ca[0]), (u32)tag, split_hi_lo(cb2[0]), (u32)(tag >> 32)}, srd_ws, hh ? OOB : lane * 16, csb + (r & 7) * 2048);
      const u32 so = j == (c4 & 3) ? WS_STATS + rt * RT_B + kq * 2048 + j * 512 : OOB, vo = l4 == q ? (lane & 15) * 16 : OOB;
      store_dev((u32x4){asu(pick(s1[0])), (u32)tag, asu(pick(s2[0])), (u32)(tag >> 32)}, srd_ws, vo, so);
      store_dev((u32x4){asu(pick(s1[1])), (u32)tag, asu(pick(s2[1])), (u32)(tag >> 32)}, srd_ws, vo, so + 256);
      __builtin_amdgcn_s_setprio(0);
    }
    if (st == 7) {
      asm volatile("" : "+v"(gr[0]), "+v"(gr[1]), "+v"(gr[2]), "+v"(gr[3]), "+v"(gr[4]), "+v"(gr[5]));
      __builtin_amdgcn_s_setprio(3);
#pragma unroll 1
      for (u32 i = 0; i < SPIN_CAP; ++i) {
        u32 bad = opaque_v(((gr[0].y ^ (u32)tag) | (gr[1].y ^ (u32)tag) | (gr[2].y ^ (u32)tag)) | ((gr[3].y ^ (u32)tag) | (gr[4].y ^ (u32)tag) | (gr[5].y ^ (u32)tag)));
        bad |= opaque_v(((gr[0].w ^ (u32)(tag >> 32)) | (gr[1].w ^ (u32)(tag >> 32)) | (gr[2].w ^ (u32)(tag >> 32))) |
                        ((gr[3].w ^ (u32)(tag >> 32)) | (gr[4].w ^ (u32)(tag >> 32)) | (gr[5].w ^ (u32)(tag >> 32))));
        if (all_lanes(bad == 0)) break;
        __builtin_amdgcn_s_sleep(8);
        asm volatile(GRAN_LOADS "\n\ts_waitcnt vmcnt(0)" GRAN_OPS);
      }
      *LDSP(u32x4, L_CORRA + j * 1024 + lane * 16) = (u32x4){gr[0].x, gr[1].x, gr[2].x, gr[3].x};
      *LDSP(u32x4, L_CORRB + (j & 1) * 1024 + lane * 16) = (u32x4){gr[0].z, gr[1].z, gr[2].z, gr[3].z};
      const float mean = swap32_add(asf(gr[4].x) + asf(gr[5].x)) * (1.f / 512.f);
      const float var = __builtin_fmaxf(__builtin_fmaf(-mean, mean, swap32_add(asf(gr[4].z) + asf(gr[5].z)) * (1.f / 512.f)), 0.f);
      const u32 ms = pack2(mean, __builtin_amdgcn_sqrtf(rbf(rbf(var) + 1e-5f)));
      mn = ((ms & 0xffffu) ^ 0x8000u) * 0x10001u;
      rl = LOG2E * __builtin_amdgcn_rcpf(hi_f(ms));
      *LDSP(u32, (hh ? L_RL : L_MN) + (j * 32 + (lane & 31)) * 4) = hh ? asu(rl) : mn;
      __builtin_amdgcn_s_setprio(0);
      sfence();
    }
    issue(n, st < 7);
    sfence();
    if (st == 0) tag = ((u64)(u32)__builtin_amdgcn_readfirstlane(tg.y) << 32) | (u32)__builtin_amdgcn_readfirstlane(tg.x);
    if (st < 2) {
      const u32 selv = (((lane >> 4) ^ (lane >> 2)) & 1) ? 0u : 0x3f803f80u;
      const bf16x8 sel = as_bf((u32x4){selv, selv, selv, selv}), ones = as_bf((u32x4){0x3f803f80u, 0x3f803f80u, 0x3f803f80u, 0x3f803f80u});
#pragma unroll
      for (int u = 0; u < 4; ++u) {
        s1[u & 1] = mfma16(cw[8 + u], ones, s1[u & 1]); s2[u & 1] = mfma16(cw[8 + u], cw[8 + u], s2[u & 1]);
        if (st) cb2 = mfma16(sel, cw[u], cb2); else ca = mfma16(sel, cw[u], ca);
      }
      asm volatile("" : "+v"(s1[0]), "+v"(s1[1]), "+v"(s2[0]), "+v"(s2[1]), "+v"(ca), "+v"(cb2));
      sfence();
    }
  }
  wait_vm<4>();
  __builtin_amdgcn_s_setprio(1);
  barrier();
  STAMP(2);
  {
    f32x16 acc[2];
    bf16x8 wf[8];
    const u32 arow = WS_A + rt * RT_B + (j * NSTEP + lstep) * 1024, l16 = lane * 16;
    auto ldw = [&](int s) { wf[s & 7] = lds_tr<512>(s >= 32 ? wb1 : wb0, (s & 31) * 2048); sfence(); };
    wf[6] = lds_b128(L_CORRB + l16); wf[7] = lds_b128(L_CORRB + l16, 1024);
#pragma unroll
    for (int s = 0; s < 6; ++s) ldw(s);
    issue(9, 1);
    dma4<0>(make_srd(p.w2, 1376 * 1024), vw2, (16 * kq + j) * 16384 + cb, L_WE + j * 4096);
    dma4<0>(make_srd(p.w2, 1376 * 1024), vw2, (16 * kq + 4 + j) * 16384 + cb, L_WE + (4 + j) * 4096);
    STAMP(8);
    const bf16x8 mf = as_bf((u32x4){mn, mn, mn, mn});
    acc[0] = mfma32(wf[6], mf, (f32x16){}); acc[1] = mfma32(wf[7], mf, (f32x16){});
    keep(acc[0]); keep(acc[1]);
    sfence();
#pragma unroll
    for (int s = 0; s < 32; ++s) {
      acc[0] = mfma32(wf[s & 7], xf[s], acc[0]); keep(acc[0]); sfence();
      if (s == BARS) asm volatile("s_waitcnt vmcnt(8)\n\ts_barrier" ::: "memory");
      ldw(s + 6);
    }
    STAMP(12);
    auto relu_store = [&](const f32x16& a, int h) {
      u32x4 o;
#pragma unroll
      for (int i = 0; i < 4; ++i) o[i] = relu2(pack2(a[8 * (h & 1) + 2 * i], a[8 * (h & 1) + 2 * i + 1]));
      store_dev(o, srd_ws, l16, arow + h * 1024);
    };
    if (xt) {
      float t[16], e[16];
      u32x4 o[2];
      static_for<32>([&](auto S_) {
        constexpr int s = S_;
        acc[1] = mfma32(wf[(32 + s) & 7], xf[s], acc[1]); keep(acc[1]); sfence();
        if constexpr (s + 6 < 32) ldw(32 + s + 6);
        static_for<3>([&](auto K_) {
          constexpr int k = (s - 2) * 3 + K_;
          if constexpr (k >= 0 && k < 16) t[k] = mul1(acc[0][k], rl);
          else if constexpr (k >= 16 && k < 32) { e[k - 16] = __builtin_amdgcn_fmed3f(__builtin_amdgcn_exp2f(t[k - 16]), 0.f, 1.f); keep(e[k - 16]); }
          else if constexpr (k >= 32 && k < 48) t[k - 32] = max_eps(t[k - 32]);
          else if constexpr (k >= 48 && k < 64) { e[k - 48] = __builtin_fmaf(t[k - 48], LN2, e[k - 48]); keep(e[k - 48]); }
          else if constexpr (k >= 64 && k < 72) { u32 v = pack2(e[2 * (k - 64)], e[2 * (k - 64) + 1]); keep(v); o[(k - 64) >> 2][(k - 64) & 3] = v; }
        });
        if constexpr (s == 27 || s == 28) store_dev(o[s - 27], srd_ws, l16, arow + (s - 27) * 1024);
        sfence();
      });
      STAMP(3);
      __builtin_amdgcn_s_setprio(3);
#pragma unroll
      for (int h = 2; h < 4; ++h) {
        float t2[8];
#pragma unroll
        for (int i = 0; i < 8; ++i) t2[i] = mul1(acc[1][8 * (h & 1) + i], rl);
        sfence();
        store_dev(phi8(t2), srd_ws, l16, arow + h * 1024);
      }
    } else if (nl == 2) {
#pragma unroll
      for (int s = 0; s < 32; ++s) {
        acc[1] = mfma32(wf[(32 + s) & 7], xf[s], acc[1]); keep(acc[1]); sfence();
        if (s + 6 < 32) ldw(32 + s + 6);
        if (s == 27 || s == 29) relu_store(acc[0], (s - 27) >> 1);
        sfence();
      }
      STAMP(3);
      __builtin_amdgcn_s_setprio(3);
      relu_store(acc[1], 2); relu_store(acc[1], 3);
    } else {
      STAMP(3);
      __builtin_amdgcn_s_setprio(3);
      relu_store(acc[0], 0); relu_store(acc[0], 1);
    }
  }
  STAMP(4);
  lds_signal(L_SYNC + 12);
  lds_wait(L_SYNC + 12, 4);
  dma4<0>(make_srd(p.wo, 512 * 1024), vw2, (8 * kq + j) * 16384 + cb, j * 4096);
  if (WO2) dma4<0>(make_srd(p.wo, 512 * 1024), vw2, (8 * kq + 4 + j) * 16384 + cb, (4 + j) * 4096);
  wait_vm<WO2 ? 8 : 4>();
  store_one((u32x4){(u32)tag, (u32)(tag >> 32), 0u, 0u}, srd_ws, WS_FLAGS + ((rt * 2 + (j >> 1)) * 48 + (xt ? 32 : 16) + g * 2 + (j & 1)) * 128);
  if (G2EARLY) lds_signal(L_SYNC + 8);
  STAMP(5);
  const bool kvjob = ((rt ^ j) & 1) == 0;
  if (!xt && (kvjob || rt == 1)) {
    const u32 hd = g * 4 + j, zb = WS_ZP + (hd >> 1) * 4096;
    const u32 sa = kvjob ? WS_KVP + hd * KVH_B + (rt >> 1) * 4096 : zb, fo = kvjob ? WS_KVF + hd * 1024 + (rt >> 1) * 128 : WS_KVZ + hd * 64;
    if (nl == 1) __builtin_amdgcn_s_sleep(ZDLY);
    u32x4 a[4];
#pragma unroll 1
    for (u32 i = 0; i < SPIN_CAP; ++i) {
      asm volatile("buffer_load_dwordx4 %0, %4, %5, %6 offen sc1\n\tbuffer_load_dwordx4 %1, %4, %5, %6 offen offset:1024 sc1\n\t"
                   "buffer_load_dwordx4 %2, %4, %5, %6 offen offset:2048 sc1\n\tbuffer_load_dwordx4 %3, %4, %5, %6 offen offset:3072 sc1\n\ts_waitcnt vmcnt(0)"
                   : "=&v"(a[0]), "=&v"(a[1]), "=&v"(a[2]), "=&v"(a[3]) : "v"(lane * 16), "s"(srd_ws), "s"(sa) : "memory");
      u32 bad = 0;
#pragma unroll
      for (int k = 0; k < 4; ++k) bad |= (a[k].y ^ (u32)tag) | (a[k].w ^ (u32)(tag >> 32));
      if (all_lanes(bad == 0)) break;
      __builtin_amdgcn_s_sleep(RSLP);
    }
      float v[4] = {(lo_f(a[0].x) + lo_f(a[1].x)) + (lo_f(a[2].x) + lo_f(a[3].x)), (hi_f(a[0].x) + hi_f(a[1].x)) + (hi_f(a[2].x) + hi_f(a[3].x)),
                    (lo_f(a[0].z) + lo_f(a[1].z)) + (lo_f(a[2].z) + lo_f(a[3].z)), (hi_f(a[0].z) + hi_f(a[1].z)) + (hi_f(a[2].z) + hi_f(a[3].z))};
#pragma unroll
      for (int i = 0; i < 4; ++i) v[i] = dpp_add<0x128>(v[i]);
#pragma unroll
      for (int i = 0; i < 4; ++i) v[i] = swap16_add(v[i]);
#pragma unroll
      for (int i = 0; i < 4; ++i) v[i] = swap32_add(v[i]);
      store_dev((u32x4){pack2(v[0], v[1]), (u32)tag, pack2(v[2], v[3]), (u32)(tag >> 32)}, srd_ws, lane < 8 ? lane * 16 : OOB, fo);
  }
  if (!G2EARLY) lds_signal(L_SYNC + 8);
  __builtin_amdgcn_s_setprio(0);
  if (!WO2) dma4<0>(make_srd(p.wo, 512 * 1024), vw2, (8 * kq + 4 + j) * 16384 + cb, (4 + j) * 4096);
  STAMP(6);
  const char* fq = p.ws + WS_FLAGS + ((rt * 2 + (j >> 1)) * 48 + 16 + kq * 4 + (j & 1)) * 128;
  const u32 rdy = L_SYNC + 48 + j * 4;
  u32 now = 0;
#define FLAG_LOADS(A, B, C, D, P) "s_load_dwordx2 " A ", " P ", 0 glc\n\ts_load_dwordx2 " B ", " P ", 256 glc\n\ts_load_dwordx2 " C ", " P ", 2048 glc\n\t" \
                                  "s_load_dwordx2 " D ", " P ", 2304 glc\n\ts_waitcnt lgkmcnt(0)"
  auto tell = [&](u64 f0, u64 f1, u64 f2, u64 f3) {
    const u32 nw = ((f0 ^ tag) | (f1 ^ tag)) ? 0 : (((f2 ^ tag) | (f3 ^ tag)) ? 1 : 2);
    if (nw != now) { now = nw; *LDSP(u32, rdy) = nw; }
  };
#pragma unroll 1
  for (u32 i = 0; i < SPIN_CAP && now < KVAFTER; ++i) {
    u64 f0, f1, f2, f3;
    asm volatile(FLAG_LOADS("%0", "%1", "%2", "%3", "%4") : "=&s"(f0), "=&s"(f1), "=&s"(f2), "=&s"(f3) : "s"(fq) : "memory");
    tell(f0, f1, f2, f3);
  }
  wait_vm<0>();
  lds_signal(L_SYNC + 32);
  __builtin_amdgcn_s_setprio(KPRIO);
  STAMP(7);
  u32x4 k0, k1, z0, z1;
#define KV_LOADS "buffer_load_dwordx4 %0, %4, %6, %7 offen sc1\n\tbuffer_load_dwordx4 %1, %4, %6, %7 offen offset:1024 sc1\n\t" \
                 "buffer_load_dwordx4 %2, %5, %6, %8 offen sc1\n\tbuffer_load_dwordx4 %3, %5, %6, %8 offen offset:64 sc1"
  u32 l16 = lane * 16, lz = l4 * 16, ko = WS_KVF + (kq * 8 + j * 2) * 1024, zo = WS_KVZ + (kq * 8 + j * 2) * 64;
  u32x4 srd2 = srd_ws;
#pragma unroll 1
  for (u32 i = 0; i < SPIN_CAP; ++i) {
    u64 f0, f1, f2, f3;
    asm volatile(KV_LOADS "\n\t" FLAG_LOADS("%9", "%10", "%11", "%12", "%13")
                 : "=&v"(k0), "=&v"(k1), "=&v"(z0), "=&v"(z1), "+v"(l16), "+v"(lz), "+s"(srd2), "+s"(ko), "+s"(zo), "=&s"(f0), "=&s"(f1), "=&s"(f2), "=&s"(f3)
                 : "s"(fq) : "memory");
    tell(f0, f1, f2, f3);
    asm volatile("s_waitcnt vmcnt(0)" : "+v"(k0), "+v"(k1), "+v"(z0), "+v"(z1));
    const u32 bad = ((k0.y ^ (u32)tag) | (k1.y ^ (u32)tag) | (z0.y ^ (u32)tag) | (z1.y ^ (u32)tag)) |
                    ((k0.w ^ (u32)(tag >> 32)) | (k1.w ^ (u32)(tag >> 32)) | (z0.w ^ (u32)(tag >> 32)) | (z1.w ^ (u32)(tag >> 32)));
    if (all_lanes(bad == 0)) break;
  }
  STAMP(9);
  {
    auto a0 = __builtin_amdgcn_permlane16_swap(k0.x, z0.x, false, false), a1 = __builtin_amdgcn_permlane16_swap(k0.z, z0.z, false, false);
    auto b0 = __builtin_amdgcn_permlane16_swap(k1.x, z1.x, false, false), b1 = __builtin_amdgcn_permlane16_swap(k1.z, z1.z, false, false);
    *LDSP(u32x4, L_KVF + j * 2048 + lane * 16) = (u32x4){a0[0], a1[0], a0[1], a1[1]};
    *LDSO(u32x4, L_KVF + j * 2048 + lane * 16, 1024) = (u32x4){b0[0], b1[0], b0[1], b1[1]};
    lds_signal(L_SYNC + 36);
  }
#pragma unroll 1
  for (u32 i = 0; i < SPIN_CAP && now < 2; ++i) {
    u64 f0, f1, f2, f3;
    asm volatile(FLAG_LOADS("%0", "%1", "%2", "%3", "%4") : "=&s"(f0), "=&s"(f1), "=&s"(f2), "=&s"(f3) : "s"(fq) : "memory");
    tell(f0, f1, f2, f3);
  }
  STAMP(13);
}

template <bool XT> DEV void group0_first(const Ptrs& p, u32 b, u32 lane, u32 w, u64& tag, u32* stamp) {
  const u32 xcd = b & 7, r = b >> 3, rt = (xcd & 1) * 8 + (r & 7), c4 = (xcd >> 1) * 4 + (r >> 3), g = c4 >> 1;
  const u32 mh = w >> 1, nh = w & 1, hh = lane >> 5, q = (lane & 15) >> 2;
  f32x16 acc[2][2] = {};
  bf16x8 xf[4][2] = {}, wf[4][2] = {};
  u32 xa = (mh * 64 + (lane & 31)) * 128 + ((hh ^ ((lane >> 1) & 7)) << 4);
  u32 wa = 16384 + (8 * hh + q) * 256 + (lane & 16) * 2 + (lane & 3) * 8 + ((XT ? (nh ^ q) : ((nh - q) & 3)) << 6), wb = wa ^ 128;
  u32 ca = L_CORRA + nh * 1024 + lane * 16;
  const u32x4 srd_ws = make_srd(p.ws, WS_END);
  u32x4 vw2 = steps4((lane >> 4) * 1024 + (lane & 3) * 16 + ((q ^ (lane >> 4)) << 6), 4096);
  auto wlate = [&]() {
    dma4<0>(make_srd(p.w2, 1376 * 1024), vw2, (64 + 6 * (c4 >> 2) + w) * 16384 + (c4 & 3) * 256, L_WL + w * 4096);
    if (w < 2) dma4<0>(make_srd(p.w2, 1376 * 1024), vw2, (68 + 6 * (c4 >> 2) + w) * 16384 + (c4 & 3) * 256, L_WL + (4 + w) * 4096);
  };
  auto wchunk = [&](int i) { dma4<0>(make_srd(p.w2, 1376 * 1024), vw2, (16 * (c4 >> 2) + 4 * i + w) * 16384 + (c4 & 3) * 256, L_WE + (4 * i + w) * 4096); };
  u32 ma0 = L_MN + (mh * 64 + (lane & 31)) * 4;
  *LDSP(u32, L_SYNC + (lane & 31) * 4) = 0u;
  if (w == 0) {
    u64 e;
    asm volatile("s_load_dwordx2 %0, %1, 0\n\ts_waitcnt lgkmcnt(0)" : "=&s"(e) : "s"(p.ws) : "memory");
    e += 0x9E3779B97F4A7C15ull;
    *LDSP(u32x2, L_SYNC + 128) = (u32x2){(u32)e, (u32)(e >> 32)};
  }

  asm volatile("" : "+v"(acc[0][0]), "+v"(acc[0][1]), "+v"(acc[1][0]), "+v"(acc[1][1]), "+v"(vw2));
  asm volatile("" : "+v"(xf[2][0]), "+v"(xf[2][1]), "+v"(wf[2][0]), "+v"(wf[2][1]), "+v"(xf[3][0]), "+v"(xf[3][1]), "+v"(wf[3][0]), "+v"(wf[3][1]),
               "+v"(xa), "+v"(wa), "+v"(wb), "+v"(ca), "+v"(ma0));
  STAMP(1);
  auto ldx = [&](int s, int h) { xf[s][h] = lds_b128(xa ^ (s << 5), h * 4096); sfence(); };
  auto ldw = [&](int s, int h) { wf[s][h] = lds_tr<1024>(h ? wb : wa, s * 4096); sfence(); };
  auto m1 = [&](int s, int i) {
    const int t = (i + 1) >> 1 & 1, u = i >> 1;
    acc[t][u] = XT ? mfma32(xf[s][t], wf[s][u], acc[t][u]) : mfma32(wf[s][u], xf[s][t], acc[t][u]);
    sfence();
  };
  __builtin_amdgcn_s_setprio(2);
  barrier();
#pragma unroll 1
  for (u32 st = 0; st < 8; ++st) {
    m1(2, 0); ldw(0, 0); m1(2, 1); ldx(0, 0); ldx(0, 1); m1(2, 2); ldw(0, 1); m1(2, 3); ldw(1, 0);
    m1(3, 0); ldx(1, 0); ldx(1, 1); m1(3, 1); ldw(1, 1); m1(3, 2); ldw(2, 0); m1(3, 3); ldx(2, 0); ldx(2, 1);
    m1(0, 0); ldw(2, 1); m1(0, 1); ldw(3, 0); m1(0, 2); ldx(3, 0); ldx(3, 1); m1(0, 3); ldw(3, 1);
    m1(1, 0);
    xa = (xa + 32768) & 131071; wa = (wa + 32768) & 131071; wb = (wb + 32768) & 131071;
    asm volatile("" : "+v"(xa), "+v"(wa), "+v"(wb));
    sfence();
    m1(1, 1); m1(1, 2); m1(1, 3);
    barrier();
  }
  f32x4 rl[8];
  m1(2, 0);
  const u32x2 tg = *LDSP(u32x2, L_SYNC + 128);
  const u32 n0 = *LDSP(u32, ma0), n1 = *LDSO(u32, ma0, 128);
  wf[0][0] = lds_b128(ca); wf[0][1] = lds_b128(ca, 2048);
  sfence();
  m1(2, 3);
  if (XT) {
    const u32 ra = opaque_v(L_RL + (mh * 64 + hh * 4) * 4);
#pragma unroll
    for (int i = 0; i < 8; ++i) rl[i] = *LDSO(f32x4, ra, i * 32);
    sfence();
  }
  m1(3, 0);
  tag = ((u64)(u32)__builtin_amdgcn_readfirstlane(tg.y) << 32) | (u32)__builtin_amdgcn_readfirstlane(tg.x);
  xf[0][0] = as_bf((u32x4){n0, n0, n0, n0}); xf[0][1] = as_bf((u32x4){n1, n1, n1, n1});
  keep(xf[0][0]); keep(xf[0][1]);
  sfence();
  m1(3, 3); m1(0, 0); m1(0, 3); m1(2, 1); m1(2, 2); m1(3, 1); m1(3, 2); m1(0, 1); m1(0, 2);
  STAMP(2);
  if constexpr (!XT) {
    __builtin_amdgcn_s_setprio(YPRIO);
#pragma unroll
    for (int k = 0; k < 8; ++k) {
      const int t = k >> 2, u = (k >> 1) & 1, s = k & 1;
      u32x4 o;
#pragma unroll
      for (int i = 0; i < 4; ++i) o[i] = relu2(pack2(acc[t][u][8 * s + 2 * i], acc[t][u][8 * s + 2 * i + 1]));
      store_dev(o, srd_ws, lane * 16, WS_A + rt * RT_B + ((mh * 2 + t) * NSTEP + g * 8 + u * 4 + nh * 2 + s) * 1024);
      if (k == YBAR) asm volatile("s_barrier" ::: "memory");
    }
    STAMP(3);
    wchunk(2);
    if (YW2) { wchunk(3); wait_vm<8>(); } else wait_vm<4>();
    store_one((u32x4){(u32)tag, (u32)(tag >> 32), 0u, 0u}, srd_ws, WS_FLAGS + ((rt * 2 + mh) * 48 + g * 2 + nh) * 128);
    __builtin_amdgcn_s_setprio(0);
    if (!YW2) wchunk(3);
    wlate();
  } else {
    f32x16 kv = {}, zz = {};
    const bf16x8 ones = as_bf((u32x4){0x3f803f80u, 0x3f803f80u, 0x3f803f80u, 0x3f803f80u});
    const bool up = lane & 16;
    const u32 kvb = WS_KVP + (g * 4 + nh * 2) * KVH_B + (rt * 2 + mh) * 128;
    const u32 o0 = opaque_v((up ? KVH_B : 0) + hh * 16384 + (lane & 8) * 512 + (lane & 7) * 16);
    const u32 oz = opaque_v((lane & 14) ? OOB : (up ? 64 : 0) + hh * 32 + (lane & 1) * 16);
#pragma unroll
    for (int k = 0; k < 4; ++k) {
      const int t = k >> 1, s = k & 1;
      if (k == XHOLD) asm volatile("s_barrier" ::: "memory");
      if (XDMA == 0 && (k == 1 || k == 2)) wchunk(k + 1);
      if (XDMA == 0 && k == 3) wlate();
      float t2[8], v2[8];
#pragma unroll
      for (int i = 0; i < 8; ++i) t2[i] = mul1(acc[t][0][8 * s + i], rl[2 * k + (i >> 2)][i & 3]);
#pragma unroll
      for (int i = 0; i < 8; ++i) v2[i] = mul1(acc[t][1][8 * s + i], rl[2 * k + (i >> 2)][i & 3]);
      sfence();
      const u32x4 pv = {pack2(v2[0], v2[1]), pack2(v2[2], v2[3]), pack2(v2[4], v2[5]), pack2(v2[6], v2[7])};
      const u32x4 pk = phi8(t2);
      kv = mfma32(as_bf(pk), as_bf(pv), kv);
      zz = mfma32(as_bf(pk), ones, zz);
      sfence();
    }
    if (XHOLD == 4) asm volatile("s_barrier" ::: "memory");
    u32 ks[4], zs[4];
#pragma unroll
    for (int i = 0; i < 4; ++i) { const u32 lo = pack2(kv[2 * i], kv[2 * i + 1]), hi = pack2(kv[8 + 2 * i], kv[9 + 2 * i]); ks[i] = up ? hi : lo; }
    store_dev((u32x4){ks[0], (u32)tag, ks[1], (u32)(tag >> 32)}, srd_ws, o0, kvb);
    store_dev((u32x4){ks[2], (u32)tag, ks[3], (u32)(tag >> 32)}, srd_ws, o0, kvb + 8192);
#pragma unroll
    for (int i = 0; i < 4; ++i) { const u32 lo = pack2(zz[2 * i], zz[2 * i + 1]), hi = pack2(zz[8 + 2 * i], zz[9 + 2 * i]); zs[i] = up ? hi : lo; }
    const bool od = lane & 1;
    store_dev((u32x4){od ? zs[2] : zs[0], (u32)tag, od ? zs[3] : zs[1], (u32)(tag >> 32)}, srd_ws, oz, WS_ZP + (g * 2 + nh) * 4096 + (rt * 2 + mh) * 128);
    if (XHOLD == 5) asm volatile("s_barrier" ::: "memory");
    STAMP(3);
    __builtin_amdgcn_s_setprio(0);
    if (XDMA == 1) { wchunk(2); wchunk(3); wait_vm<8>(); wlate(); }
  }
}

DEV void group0(const Ptrs& p, u32 b, u32 lane, u32 w, u32* stamp) {
  u64 tag;
  if (b & 64) group0_first<false>(p, b, lane, w, tag, stamp); else group0_first<true>(p, b, lane, w, tag, stamp);
  STAMP(5);
  b = opaque_s(b); lane = opaque_v(lane);
  const u32 rt = (b & 1) * 8 + ((b >> 3) & 7), kq = (b >> 1) & 3, ct = b >> 6, hh = lane >> 5, q = (lane & 15) >> 2;
  const __amdgpu_buffer_rsrc_t rws = __builtin_amdgcn_make_buffer_rsrc(p.ws, 0, WS_END, 0x00020000);
  const u32x4 srd_ws = make_srd(p.ws, WS_END);
  const char* fp = p.ws + WS_FLAGS + ((rt * 2 + (w >> 1)) * 48 + kq * 4) * 128;
  const u32 ab = WS_A + rt * RT_B + w * NSTEP * 1024;
  u32 wl[4], we[4], wm[4];
#pragma unroll
  for (int i = 0; i < 4; ++i) {
    wl[i] = opaque_v((4 * hh + q) * 256 + (lane & 16) * 2 + (lane & 3) * 8 + ((i ^ kq ^ q) << 6));
    we[i] = opaque_v(wl[i] + L_WE);
    wm[i] = opaque_v(wl[i] + L_WL);
  }
  const float rlv = *LDSP(float, L_RL + (w * 32 + (lane & 31)) * 4);
  const u32 tok = rt * 128 + w * 32 + (lane & 31), oo = tok * 1024 + ct * 256 + kq * 64 + hh * 16;
  u32x4 xr0, xr1;
  const u32 rdy = opaque_v(L_SYNC + 48 + w * 4), cl = opaque_v(L_SYNC + 32);
  u32x4 af[30];
  const u32 l16 = opaque_v(lane * 16);
  auto lda = [&](int s) {
    const u32 so = s < 16 ? ab + (16 * kq + s) * 1024 : s < 22 ? ((s < 20 || kq < 3) ? ab + (48 + 6 * kq + s) * 1024 : OOB) : ab + (64 + 8 * kq + s) * 1024;
    asm volatile("buffer_load_dwordx4 %0, %1, %2, %3 offen sc1" : "=v"(af[s]) : "v"(l16), "s"(srd_ws), "s"(so) : "memory");
  };
  bf16x8 wf[3][4], kf[8] = {};
  f32x16 acc[4] = {}, d[2];
  auto ldw = [&](int s, int i) { wf[s % 3][i] = lds_tr<2048>(s < 16 ? we[i] : s < 22 ? wm[i] : wl[i], (s < 16 ? s : s < 22 ? s - 16 : s - 22) * 4096); sfence(); };
  auto peek = [&](u32 a, int o) { return __hip_atomic_load(LDSO(u32, a, o), __ATOMIC_RELAXED, __HIP_MEMORY_SCOPE_WORKGROUP); };
  auto pin = [&](f32x16& v) { asm volatile("" : "+v"(v)); sfence(); };
  const float sc = __builtin_amdgcn_rcpf(rlv);
  f32x16 ceps = {0.f, 0.f, 0.f, 0.f, 0.f, 0.f, 0.f, 0.f, 1e-6f, 1e-6f, 1e-6f, 1e-6f, 1e-6f, 1e-6f, 1e-6f, 1e-6f};
  keep(ceps);
  u32 c0 = 0, c1 = 0;
  wait_vm<0>();
  lds_signal(L_SYNC + 4);
  poll4(fp, tag, SPIN_CAP);
  STAMP(6);
  lds_wait(L_SYNC + 4, 4);
  lds_wait(L_SYNC + 12, 4);
#pragma unroll
  for (int s = 0; s < LEAD; ++s) lda(s);
  lds_wait(L_SYNC + 8, 4);
#pragma unroll
  for (int i = 0; i < 8; ++i) ldw(i >> 2, i & 3);
  __builtin_amdgcn_s_setprio(GPRIO);
  STAMP(7);
  static_for<30>([&](auto S_) {
    constexpr int s = S_;
    if (s == 14 - LEAD || s == 20 - LEAD) { c0 = peek(rdy, 0); sfence(); }
    if (s == 16 - LEAD) { if ((u32)__builtin_amdgcn_readfirstlane(c0) < 1) lds_wait(rdy, 1); STAMP(8); }
    if (s == 22 - LEAD) { if ((u32)__builtin_amdgcn_readfirstlane(c0) < 2) lds_wait(rdy, 2); STAMP(9); }
    if constexpr (s + LEAD < 30) lda(s + LEAD);
    if (s == 17) { c0 = peek(cl, 0); sfence(); }
    if (s == 19 && (u32)__builtin_amdgcn_readfirstlane(c0) < 4) lds_wait(cl, 4);
    if (s == 19) { c1 = peek(cl, 4); sfence(); }
    constexpr int h = s - 22;
    float f, m[8];
    auto scale = [&](const f32x16& dd) { f = mul1(sc, __builtin_amdgcn_rcpf(dd[8])); sfence(); };
    auto mul4 = [&](f32x16& dd, int o) { for (int i = o; i < o + 4; ++i) m[i] = mul1(dd[i], f); if (o) keep(dd); sfence(); };
    auto fin = [&](u32x4& o) { o = (u32x4){pack2(m[0], m[1]), pack2(m[2], m[3]), pack2(m[4], m[5]), pack2(m[6], m[7])}; keep(o); sfence(); };
    if constexpr (s == 21) {
      if ((u32)__builtin_amdgcn_readfirstlane(c1) >= 4) {
#pragma unroll
        for (int i = 0; i < 8; ++i) kf[i] = lds_b128(L_KVF + lane * 16, i * 1024);
      }
      sfence();
    }
    if constexpr (s == 22) {
      if ((u32)__builtin_amdgcn_readfirstlane(c1) < 4) {
        lds_wait(cl + 4, 4);
#pragma unroll
        for (int i = 0; i < 8; ++i) kf[i] = lds_b128(L_KVF + lane * 16, i * 1024);
      }
      STAMP(4);
      need<s, 22>(af[22]); need<s, 23>(af[23]);
      d[0] = mfma32(kf[0], as_bf(af[22]), ceps); pin(d[0]);
      d[1] = mfma32(kf[1], as_bf(af[23]), ceps); pin(d[1]);
      scale(d[0]); mul4(d[0], 0); mul4(d[0], 4); fin(af[22]);
    }
    constexpr bool at = h >= 0 && h < 7;
    if constexpr (s < 22) need<s, s>(af[s]);
    if constexpr (h >= 0 && h < 6) { need<s, s + 2>(af[s + 2]); d[h & 1] = mfma32(kf[h + 2], as_bf(af[s + 2]), ceps); pin(d[h & 1]); }
    if constexpr (at) scale(d[(h + 1) & 1]);
    acc[1] = mfma32(wf[s % 3][1], as_bf(af[s]), acc[1]); pin(acc[1]);
    if (s + 2 < 30) ldw(s + 2, 1);
    if constexpr (at) mul4(d[(h + 1) & 1], 0);
    acc[2] = mfma32(wf[s % 3][2], as_bf(af[s]), acc[2]); pin(acc[2]);
    if (s + 2 < 30) ldw(s + 2, 2);
    if constexpr (at) mul4(d[(h + 1) & 1], 4);
    acc[3] = mfma32(wf[s % 3][3], as_bf(af[s]), acc[3]); pin(acc[3]);
    if (s + 2 < 30) ldw(s + 2, 3);
    if constexpr (s < 22) { acc[0] = mfma32(wf[s % 3][0], as_bf(af[s]), acc[0]); pin(acc[0]); }
    if constexpr (s < 20) ldw(s + 2, 0);
    if constexpr (at) fin(af[s + 1]);
  });
  STAMP(10);
  bf16x8 w0[8];
  const u32 xb = WS_X + rt * RT_B + (ct * 4 + w) * 32768;
#pragma unroll
  for (int i = 1; i < 4; ++i)
#pragma unroll
    for (int k = 0; k < 2; ++k)
      store_dev((u32x4){pack2(acc[i][8 * k], acc[i][8 * k + 1]), pack2(acc[i][8 * k + 2], acc[i][8 * k + 3]), pack2(acc[i][8 * k + 4], acc[i][8 * k + 5]),
                        pack2(acc[i][8 * k + 6], acc[i][8 * k + 7])}, srd_ws, lane * 16, xb + ((kq ^ i) * 4 + kq) * 2048 + k * 1024);
  asm volatile("global_load_dwordx4 %0, %2, %3\n\tglobal_load_dwordx4 %1, %2, %3 offset:32" : "=&v"(xr0), "=&v"(xr1) : "v"(oo), "s"(p.x) : "memory");
#pragma unroll
  for (int s = 0; s < 8; ++s) w0[s] = lds_tr<2048>(wl[0], s * 4096);
  sfence();
#pragma unroll
  for (int s = 0; s < 8; ++s) { acc[0] = mfma32(w0[s], as_bf(af[22 + s]), acc[0]); pin(acc[0]); }
  wait_vm<2>();
  const u32 xf = WS_XFL + ((rt * 4 + ct) * 4 + w) * 512;
  store_one((u32x4){(u32)tag, (u32)(tag >> 32), 0u, 0u}, srd_ws, xf + kq * 128);
  asm volatile("s_waitcnt vmcnt(1)" : "+v"(xr0), "+v"(xr1));
  u32x4 perm0, perm1;
  {
    const u32 row = lane & 15, e = (row & 3) + ((row >> 3) << 2), hit = ((row >> 2) & 1) == hh ? 0x3f80u << (16 * (e & 1)) : 0u;
#pragma unroll
    for (int i = 0; i < 4; ++i) { const u32 t = (e >> 1) == (u32)i ? hit : 0u; perm0[i] = (lane & 16) ? 0u : t; perm1[i] = (lane & 16) ? t : 0u; }
    keep(perm0); keep(perm1);
  }
  float xv[16];
#pragma unroll
  for (int e = 0; e < 4; ++e) { xv[2 * e] = lo_f(xr0[e]); xv[2 * e + 1] = hi_f(xr0[e]); xv[8 + 2 * e] = lo_f(xr1[e]); xv[9 + 2 * e] = hi_f(xr1[e]); }
#pragma unroll
  for (int e = 0; e < 16; ++e) keep(xv[e]);
  STAMP(11);
  poll4(p.ws + xf, tag, SPIN_CAP);
  STAMP(12);
  u32x4 pp[3][2];
#pragma unroll
  for (int i = 0; i < 3; ++i) {
    pp[i][0] = __builtin_amdgcn_raw_buffer_load_b128(rws, lane * 16, xb + (kq * 4 + (kq ^ (i + 1))) * 2048, 16);
    pp[i][1] = __builtin_amdgcn_raw_buffer_load_b128(rws, lane * 16 + 1024, xb + (kq * 4 + (kq ^ (i + 1))) * 2048, 16);
  }
  sfence();
#pragma unroll
  for (int i = 0; i < 3; ++i) { acc[0] = mfma32(as_bf(perm0), as_bf(pp[i][0]), acc[0]); acc[0] = mfma32(as_bf(perm1), as_bf(pp[i][1]), acc[0]); }
  float v[16];
#pragma unroll
  for (int e = 0; e < 16; ++e) v[e] = acc[0][e];
  const float rs = rlv * LN2;
#pragma unroll
  for (int k = 0; k < 2; ++k) {
#pragma unroll
    for (int e = 0; e < 4; ++e) {
      auto sw = __builtin_amdgcn_permlane32_swap(asu(v[8 * k + e]), asu(v[8 * k + 4 + e]), false, false);
      v[8 * k + e] = asf(sw[0]); v[8 * k + 4 + e] = asf(sw[1]);
    }
    u32x4 o;
#pragma unroll
    for (int e = 0; e < 4; ++e) o[e] = pack2(__builtin_fmaf(v[8 * k + 2 * e], rs, xv[8 * k + 2 * e]), __builtin_fmaf(v[8 * k + 2 * e + 1], rs, xv[8 * k + 2 * e + 1]));
    *(GAS u32x4*)(p.out + oo + k * 32) = o;
  }
  if (b == 0 && w == 0) store_one((u32x4){(u32)tag, (u32)(tag >> 32), 0u, 0u}, srd_ws, WS_EPOCH);
  STAMP(13);
}

__global__ __launch_bounds__(512, 2) void fused_block(const char* __restrict__ x, const char* __restrict__ wqkv, const char* __restrict__ w1,
                                                      char* ws, const char* __restrict__ w2, const char* __restrict__ wo, char* __restrict__ out) {
  __shared__ __attribute__((aligned(16))) char lds_all[L_TOTAL];
  u32 stamp[32] = {};
  u64 pc;
  asm volatile("s_getpc_b64 %0" : "=s"(pc) : "s"((u32)(size_t)lds_all));
  STAMP(0);
  const u32 w = __builtin_amdgcn_readfirstlane(__builtin_amdgcn_workitem_id_x() >> 6);
  {
    u32 d0, d1, d2, d3;
    const u64 cp = (pc & ~127ull) + umin((__builtin_amdgcn_workgroup_id_x() >> 3) * 8 + w * 2, CODE_LINES - 2) * 128;
    asm volatile("s_cmp_gt_u32 %5, 3\n\ts_cbranch_scc1 1f\n\ts_load_dword %0, %4, 0\n\ts_load_dword %1, %4, 64\n\ts_load_dword %2, %4, 128\n\t"
                 "s_load_dword %3, %4, 192\n\ts_waitcnt lgkmcnt(0)\n1:"
                 : "=&s"(d0), "=&s"(d1), "=&s"(d2), "=&s"(d3) : "s"(cp), "s"(w) : "memory", "scc");
  }
  asm volatile("" ::"s"(x), "s"(wqkv), "s"(w1), "s"(ws), "s"(w2), "s"(wo), "s"(out));
  const Ptrs p = {x, wqkv, w1, ws, w2, wo, out};
  if (w < 4) group0(p, opaque_s(__builtin_amdgcn_workgroup_id_x()), opaque_v(__builtin_amdgcn_workitem_id_x()) & 63, w, stamp);
  else group1(p, __builtin_amdgcn_workgroup_id_x(), __builtin_amdgcn_workitem_id_x() & 63, w, stamp);
}

#if defined(__HIP_DEVICE_COMPILE__)
#pragma clang attribute pop
#endif

extern "C" void kernel_launch(void* const* d_in, const int* in_sizes, int n_in, void* d_out, int out_size, void* d_ws, size_t ws_size,
                              hipStream_t stream) {
  fused_block<<<256, 512, 0, stream>>>((const char*)d_in[0], (const char*)d_in[1], (const char*)d_in[4], (char*)d_ws + WS_OFF(ws_size), (const char*)d_in[5],
                                       (const char*)d_in[2], (char*)d_out);
}
```

```cpp
#include <hip/hip_runtime.h>
#include <hip/hip_bf16.h>
#include <type_traits>

#if defined(__HIP_DEVICE_COMPILE__)
#pragma clang attribute push(__attribute__((target("no-packed-fp32-ops"))), apply_to = function)
#endif
typedef unsigned u32;
typedef unsigned long long u64;
typedef __bf16 bf16x8 __attribute__((ext_vector_type(8)));
typedef __bf16 bf16x2 __attribute__((ext_vector_type(2)));
typedef short s16x4 __attribute__((ext_vector_type(4)));
typedef short s16x2 __attribute__((ext_vector_type(2)));
typedef float f32x16 __attribute__((ext_vector_type(16)));
typedef float f32x4 __attribute__((ext_vector_type(4)));
typedef float f32x2 __attribute__((ext_vector_type(2)));
typedef u32 u32x4 __attribute__((ext_vector_type(4)));
typedef u32 u32x2 __attribute__((ext_vector_type(2)));
#define LAS __attribute__((address_space(3)))
#define GAS __attribute__((address_space(1)))
#define DEV __device__ __forceinline__
#define RLX_AGENT __ATOMIC_RELAXED, __HIP_MEMORY_SCOPE_AGENT
#define LDSP(T, a) ((LAS T*)(size_t)(a))

#define ROT1 1
#define PF 1
#define QWIN 3
#define AWIN 5
#define DLY 0
#define SLP 2
#define PSC "sc1"
#define SL 8
#define SO 12
#define YPRIO 0
#define PFMODE 0
#define GATE 1
#define KVMODE 6
#define STATST 0
#define SPINSLP "s_sleep 1\n\t"
#define LORD 2
#define GST 5
#define YW2 1
#define YBAR 1
#define KVAFTER 2
#define ZDLY 2
#define RSLP 2
#define XHOLD 3
#define KPRIO 0
#define GPRIO 1
#define G2EARLY 0
#define PFS 0
#define PFN 3
#define XDMA 1
#define BARS 10
#define WO2 0
#define LEAD 8
#define WSPOS 0
#define WS_OFF(n) (WSPOS == 0 ? (size_t)0 : (WSPOS == 1 ? (n) / 2 : (n) - (48u << 20)) & ~(size_t)0x1fffff)
#define OUTP 0
#define PRE 1
#define WG0 0
#define DEF0 8
#define FLAGAT 16
#define KD 0
#define G2SKIP 1
#define PFH 0
#define STAG 1
#define ST7 0
#define WGRP 2
#define PHIG 27
#define PRIO2 1

constexpr u32 WS_EPOCH = 0;
constexpr u32 WS_FLAGS = 4096;
constexpr u32 WS_XFL = WS_FLAGS + 32 * 48 * 128;
constexpr u32 WS_KVF = WS_XFL + 1024 * 128;
constexpr u32 WS_KVZ = WS_KVF + 32768;
constexpr u32 WS_ZP = WS_KVZ + 2048;
constexpr u32 WS_KVP = WS_ZP + 65536;
constexpr u32 KVH_B = 32768;
constexpr u32 WS_CS = 3u << 19;
constexpr u32 RT_B = 1u << 20;
constexpr u32 WS_A = 2u << 20;
constexpr u32 NSTEP = 118;
constexpr u32 WS_STATS = WS_A + 4 * NSTEP * 1024;
constexpr u32 WS_X = WS_A + 524288;
constexpr u32 WS_END = WS_A + 16 * RT_B;
constexpr u32 WS_STAMP = 32u << 20;
static_assert(WS_KVP + 32 * KVH_B <= WS_CS && WS_CS + 524288 <= WS_A, "ws map");
constexpr u32 OOB = 0x40000000u;

constexpr u32 L_WE = 65536;
constexpr u32 L_SCR = 131072;
constexpr u32 L_CORRA = L_SCR;
constexpr u32 L_CORRB = L_SCR + 4096;
constexpr u32 L_MN = L_SCR + 6144;
constexpr u32 L_RL = L_SCR + 6656;
constexpr u32 L_SYNC = L_SCR + 7168;
constexpr u32 L_JUNK = L_SCR + 7424;
constexpr u32 L_WL = L_SCR + 8192;
constexpr u32 L_KVF = 32768;
constexpr u32 L_FINE = 49152;
constexpr u32 L_TOTAL = 163840;

constexpr u32 CODE_LINES = 180;
constexpr u32 SPIN_CAP = 1u << 18;
constexpr u32 HINT_CAP = 1u << 12;
constexpr float LOG2E = 1.4426950408889634f, LN2 = 0.6931471805599453f;

DEV float asf(u32 v) { return __builtin_bit_cast(float, v); }
DEV u32 asu(float v) { return __builtin_bit_cast(u32, v); }
DEV bool all_lanes(bool v) { return __builtin_amdgcn_ballot_w64(!v) == 0; }
DEV u32 umin(u32 a, u32 b) { return a < b ? a : b; }
DEV void sfence() { __builtin_amdgcn_sched_barrier(0); }
template <int N> DEV void wait_vm() { asm volatile("s_waitcnt vmcnt(%0)" ::"i"(N) : "memory"); }
DEV void wait_lgkm() { sfence(); __builtin_amdgcn_s_waitcnt(0xC07F); asm volatile("" ::: "memory"); sfence(); }
DEV void barrier() { sfence(); __builtin_amdgcn_s_waitcnt(0xC07F); asm volatile("s_barrier" ::: "memory"); sfence(); }

DEV u32x4 make_srd(const void* p, u32 bytes) {
  u64 a = (u64)p;
  return (u32x4){(u32)a, (u32)(a >> 32) & 0xffffu, bytes, 0x00020000u};
}
template <int POL> DEV void dma(u32x4 srd, u32 voff, u32 soff, u32 ldsaddr) {
  if (POL == 1)
    asm volatile("s_mov_b32 m0, %3\n\ts_nop 0\n\tbuffer_load_dwordx4 %0, %1, %2 offen nt lds" ::"v"(voff), "s"(srd), "s"(soff), "s"(ldsaddr) : "memory");
  else if (POL == 2)
    asm volatile("s_mov_b32 m0, %3\n\ts_nop 0\n\tbuffer_load_dwordx4 %0, %1, %2 offen sc1 lds" ::"v"(voff), "s"(srd), "s"(soff), "s"(ldsaddr) : "memory");
  else
    asm volatile("s_mov_b32 m0, %3\n\ts_nop 0\n\tbuffer_load_dwordx4 %0, %1, %2 offen lds" ::"v"(voff), "s"(srd), "s"(soff), "s"(ldsaddr) : "memory");
}
template <int POL> DEV void dma4(u32x4 srd, u32x4 v, u32 soff, u32 ldsaddr) {
  if (POL == 1)
    asm volatile("s_mov_b32 m0, %6\n\ts_nop 0\n\tbuffer_load_dwordx4 %0, %4, %5 offen nt lds\n\tbuffer_load_dwordx4 %1, %4, %5 offen offset:1024 nt lds\n\t"
                 "buffer_load_dwordx4 %2, %4, %5 offen offset:2048 nt lds\n\tbuffer_load_dwordx4 %3, %4, %5 offen offset:3072 nt lds"
                 ::"v"(v.x), "v"(v.y), "v"(v.z), "v"(v.w), "s"(srd), "s"(soff), "s"(ldsaddr) : "memory");
  else
    asm volatile("s_mov_b32 m0, %6\n\ts_nop 0\n\tbuffer_load_dwordx4 %0, %4, %5 offen lds\n\tbuffer_load_dwordx4 %1, %4, %5 offen offset:1024 lds\n\t"
                 "buffer_load_dwordx4 %2, %4, %5 offen offset:2048 lds\n\tbuffer_load_dwordx4 %3, %4, %5 offen offset:3072 lds"
                 ::"v"(v.x), "v"(v.y), "v"(v.z), "v"(v.w), "s"(srd), "s"(soff), "s"(ldsaddr) : "memory");
}
DEV void dma2(u32x4 srd, u32 v0, u32 v1, u32 soff, u32 ldsaddr) {
  asm volatile("s_mov_b32 m0, %4\n\ts_nop 0\n\tbuffer_load_dwordx4 %0, %2, %3 offen lds\n\tbuffer_load_dwordx4 %1, %2, %3 offen offset:1024 lds"
               ::"v"(v0), "v"(v1), "s"(srd), "s"(soff), "s"(ldsaddr) : "memory");
}
DEV void dma8_if(u32 cond, int want, u32x4 srd0, u32x4 v0, u32 so0, u32 la0, u32x4 srd1, u32x4 v1, u32 so1, u32 la1) {
  asm volatile("s_cmp_lg_u32 %14, %15\n\ts_cbranch_scc1 1f\n\ts_mov_b32 m0, %10\n\ts_nop 0\n\t"
               "buffer_load_dwordx4 %0, %8, %9 offen lds\n\tbuffer_load_dwordx4 %1, %8, %9 offen offset:1024 lds\n\t"
               "buffer_load_dwordx4 %2, %8, %9 offen offset:2048 lds\n\tbuffer_load_dwordx4 %3, %8, %9 offen offset:3072 lds\n\ts_mov_b32 m0, %13\n\ts_nop 0\n\t"
               "buffer_load_dwordx4 %4, %11, %12 offen lds\n\tbuffer_load_dwordx4 %5, %11, %12 offen offset:1024 lds\n\t"
               "buffer_load_dwordx4 %6, %11, %12 offen offset:2048 lds\n\tbuffer_load_dwordx4 %7, %11, %12 offen offset:3072 lds\n1:"
               ::"v"(v0.x), "v"(v0.y), "v"(v0.z), "v"(v0.w), "v"(v1.x), "v"(v1.y), "v"(v1.z), "v"(v1.w), "s"(srd0), "s"(so0), "s"(la0), "s"(srd1), "s"(so1), "s"(la1),
                 "s"(cond), "i"(want) : "memory", "scc");
}
DEV void touch64_if(u32 cond, int want, u32x4 srd, u32 voff, u32 soff, u32 ldsaddr) {
  asm volatile("s_cmp_lg_u32 %4, %5\n\ts_cbranch_scc1 1f\n\ts_mov_b32 m0, %3\n\ts_nop 0\n\tbuffer_load_dword %0, %1, %2 offen lds\n1:"
               ::"v"(voff), "s"(srd), "s"(soff), "s"(ldsaddr), "s"(cond), "i"(want) : "memory", "scc");
}
DEV float sel_mask(u64 m, float no, float yes) { float r; asm("v_cndmask_b32 %0, %1, %2, %3" : "=v"(r) : "v"(no), "v"(yes), "s"(m)); return r; }
DEV void touch64(u32x4 srd, u32 voff, u32 soff, u32 ldsaddr) {
  asm volatile("s_mov_b32 m0, %3\n\ts_nop 0\n\tbuffer_load_dword %0, %1, %2 offen lds" ::"v"(voff), "s"(srd), "s"(soff), "s"(ldsaddr) : "memory");
}
DEV u32x4 steps4(u32 v, u32 step) { step -= 1024; return (u32x4){v, v + step, v + 2 * step, v + 3 * step}; }
DEV void store_one(u32x4 v, u32x4 srd, u32 soff) {
  asm volatile("s_mov_b64 exec, 1\n\tbuffer_store_dwordx4 %0, off, %1, %2 sc1\n\ts_mov_b64 exec, -1\n\ts_nop 0" ::"v"(v), "s"(srd), "s"(soff) : "memory");
}
DEV void store_dev(u32x4 v, u32x4 srd, u32 voff, u32 soff) {
  asm volatile("buffer_store_dwordx4 %0, %1, %2, %3 offen sc1\n\ts_nop 1" ::"v"(v), "v"(voff), "s"(srd), "s"(soff) : "memory");
}
#define LDSO(T, a, o) ((LAS T*)(LDSP(char, a) + (o)))
DEV bf16x8 lds_b128(u32 a, int o = 0) { return *LDSO(bf16x8, a, o); }
template <int D> DEV bf16x8 lds_tr(u32 a, int o) {
  s16x4 lo = __builtin_amdgcn_ds_read_tr16_b64_v4i16(LDSO(s16x4, a, o));
  s16x4 hi = __builtin_amdgcn_ds_read_tr16_b64_v4i16(LDSO(s16x4, a, o + D));
  return __builtin_bit_cast(bf16x8, __builtin_shufflevector(lo, hi, 0, 1, 2, 3, 4, 5, 6, 7));
}
DEV f32x16 mfma32(bf16x8 a, bf16x8 b, f32x16 c) { return __builtin_amdgcn_mfma_f32_32x32x16_bf16(a, b, c, 0, 0, 0); }
DEV f32x4 mfma16(bf16x8 a, bf16x8 b, f32x4 c) { return __builtin_amdgcn_mfma_f32_16x16x32_bf16(a, b, c, 0, 0, 0); }
DEV bf16x8 as_bf(u32x4 v) { return __builtin_bit_cast(bf16x8, v); }
DEV u32 pack2(float a, float b) { return __builtin_bit_cast(u32, __builtin_convertvector((f32x2){a, b}, bf16x2)); }
DEV u32 relu2(u32 v) { return __builtin_bit_cast(u32, __builtin_elementwise_max(__builtin_bit_cast(s16x2, v), (s16x2){0, 0})); }
DEV float lo_f(u32 v) { return asf(v << 16); }
DEV float hi_f(u32 v) { return asf(v & 0xffff0000u); }
DEV float rbf(float f) { return lo_f(pack2(f, 0.f)); }
DEV u32 split_hi_lo(float c) { float h = rbf(c); return pack2(h, c - h); }
DEV float mul1(float a, float b) { float r = a * b; asm("" : "+v"(r)); return r; }
template <int CTRL> DEV float dpp_add(float v) {
  return v + asf(__builtin_amdgcn_update_dpp(0u, asu(v), CTRL, 0xf, 0xf, true));
}
DEV float swap16_add(float v) {
  auto r = __builtin_amdgcn_permlane16_swap(asu(v), asu(v), false, false);
  return asf(r[0]) + asf(r[1]);
}
DEV float swap32_add(float v) {
  auto r = __builtin_amdgcn_permlane32_swap(asu(v), asu(v), false, false);
  return asf(r[0]) + asf(r[1]);
}
DEV float sum32(float v) {
  v = dpp_add<0xB1>(v); v = dpp_add<0x4E>(v); v = dpp_add<0x141>(v); v = dpp_add<0x140>(v);
  return swap16_add(v);
}
DEV float max_eps(float t2) { float m; asm("v_max_f32 %0, %1, %2" : "=v"(m) : "s"(1e-6f * LOG2E), "v"(t2)); return m; }
DEV u32x4 phi8(const float (&t2)[8]) {
  float e[8], m[8];
#pragma unroll
  for (int i = 0; i < 8; ++i) e[i] = __builtin_amdgcn_fmed3f(__builtin_amdgcn_exp2f(t2[i]), 0.f, 1.f);
  sfence();
#pragma unroll
  for (int i = 0; i < 8; ++i) asm("v_max_f32 %0, %1, %2" : "=v"(m[i]) : "s"(1e-6f * LOG2E), "v"(t2[i]));
  sfence();
#pragma unroll
  for (int i = 0; i < 8; ++i) { e[i] = __builtin_fmaf(m[i], LN2, e[i]); asm("" : "+v"(e[i])); }
  sfence();
  return (u32x4){pack2(e[0], e[1]), pack2(e[2], e[3]), pack2(e[4], e[5]), pack2(e[6], e[7])};
}
DEV void lds_signal(u32 addr, u32 inc = 1u) {
  asm volatile("s_mov_b64 exec, 1\n\tds_add_u32 %0, %1\n\ts_mov_b64 exec, -1" ::"v"(addr), "v"(inc) : "memory");
}
DEV void lds_wait(u32 addr, u32 waves) {
  u32 v, s, n = SPIN_CAP;
  asm volatile("1:\n\tds_read_b32 %0, %3\n\ts_waitcnt lgkmcnt(0)\n\tv_readfirstlane_b32 %1, %0\n\ts_sub_u32 %2, %2, 1\n\ts_cbranch_scc1 2f\n\t"
               "s_cmp_lt_u32 %1, %4\n\ts_cbranch_scc1 1b\n2:"
               : "=&v"(v), "=&s"(s), "+s"(n) : "v"(addr), "s"(waves) : "memory", "scc");
}
DEV u64 poll4(const char* p, u64 tag, u32 n) {
  u64 f0, f1, f2, f3;
  asm volatile("1:\n\ts_load_dwordx2 %0, %5, 0 glc\n\ts_load_dwordx2 %1, %5, 128 glc\n\ts_load_dwordx2 %2, %5, 256 glc\n\t"
               "s_load_dwordx2 %3, %5, 384 glc\n\ts_waitcnt lgkmcnt(0)\n\t"
               "s_xor_b64 %0, %0, %6\n\ts_xor_b64 %1, %1, %6\n\ts_xor_b64 %2, %2, %6\n\ts_xor_b64 %3, %3, %6\n\t"
               "s_or_b64 %0, %0, %1\n\ts_or_b64 %2, %2, %3\n\ts_or_b64 %0, %0, %2\n\ts_cbranch_scc0 2f\n\t"
               "s_sub_u32 %4, %4, 1\n\ts_cbranch_scc0 1b\n2:"
               : "=&s"(f0), "=&s"(f1), "=&s"(f2), "=&s"(f3), "+s"(n) : "s"(p), "s"(tag) : "memory", "scc");
  return f0;
}
template <int D> DEV void poll2(const char* p, u64 tag, u64 mask) {
  u64 f0, f1; u32 n = SPIN_CAP;
  asm volatile("1:\n\ts_load_dwordx2 %0, %3, 0 glc\n\ts_load_dwordx2 %1, %3, %6 glc\n\ts_waitcnt lgkmcnt(0)\n\t"
               "s_xor_b64 %0, %0, %4\n\ts_xor_b64 %1, %1, %4\n\ts_or_b64 %0, %0, %1\n\ts_and_b64 %0, %0, %5\n\ts_cbranch_scc0 2f\n\t"
               "s_sub_u32 %2, %2, 1\n\ts_cbranch_scc0 1b\n2:"
               : "=&s"(f0), "=&s"(f1), "+s"(n) : "s"(p), "s"(tag), "s"(mask), "i"(D) : "memory", "scc");
}
DEV u32x4 load_dev_wait(u32x4 srd, u32 voff, u32 soff) {
  u32x4 v;
  asm volatile("buffer_load_dwordx4 %0, %1, %2, %3 offen sc1\n\ts_waitcnt vmcnt(0)" : "=&v"(v) : "v"(voff), "s"(srd), "s"(soff) : "memory");
  return v;
}
template <int N, int I = 0, class F> DEV void static_for(F&& f) {
  if constexpr (I < N) { f(std::integral_constant<int, I>{}); static_for<N, I + 1>(f); }
}
template <int T, int K> DEV void need(u32x4& v) {
  asm volatile("s_waitcnt vmcnt(%1)" : "+v"(v) : "i"((T + LEAD < 30 ? T + LEAD : 29) - K));
}
constexpr int phi_cost(int k) {
  const int i = k / 5, op = k % 5;
  return op == 0 ? (i < 16 ? 1 : 0) : op == 1 ? (i >= 1 && i < 17 ? 2 : 0) : op == 2 ? (i >= 1 && i < 17 ? 1 : 0) : op == 3 ? (i >= 2 && i < 18 ? 1 : 0) : (i >= 4 && i < 20 && !(i & 1) ? 1 : 0);
}
constexpr int phi_gap(int k) {
  if (!phi_cost(k)) return -1;
  int c = 0;
  for (int i = 0; i < k; ++i) c += phi_cost(i);
  return 1 + c * PHIG / 88;
}
template <class T> DEV void keep(T& v) { asm volatile("" : "+v"(v)); }
template <class T> DEV T opaque_v(T v) { asm volatile("" : "+v"(v)); return v; }
template <class T> DEV T opaque_s(T v) { asm volatile("" : "+s"(v)); return v; }

#define STAMP(i)
#define FSTAMP(w, i)

struct Ptrs { const char *x, *wqkv, *w1; char* ws; const char *w2, *wo; char* out; };
typedef u64 u64x4 __attribute__((ext_vector_type(4)));
typedef u64 u64x2 __attribute__((ext_vector_type(2)));
struct Args {
  u64x4 a; u64x2 c; u64 d;
  DEV void request(int path) {
    asm volatile("s_load_dwordx8 %0, %3, 0 ; %4\n\ts_load_dwordx4 %1, %3, 32\n\ts_load_dwordx2 %2, %3, 48"
                 : "=&s"(a), "=&s"(c), "=&s"(d) : "s"(__builtin_amdgcn_kernarg_segment_ptr()), "i"(path));
  }
  DEV Ptrs get() {
    asm volatile("s_waitcnt lgkmcnt(0)" : "+s"(a), "+s"(c), "+s"(d));
    return {(const char*)a[0], (const char*)a[1], (const char*)a[2], (char*)a[3], (const char*)c[0], (const char*)c[1], (char*)d};
  }
};

DEV void group1(u32 b, u32 lane, u32 w, u32* stamp) {
  Args args;
  args.request(1);
  b = opaque_s(b); w = opaque_s(w);
  sfence();
  const u32 xcd = b & 7, r = b >> 3, j = w & 3;
  const u32 rt = (xcd & 1) * 8 + (r & 7);
  const u32 rot = (r & 7) + (xcd & 6);
  const u32 vx = (lane >> 3) * 1024 + (((lane & 7) ^ (lane >> 4)) << 4);
  u32x4 vx4 = {vx, (vx ^ 64) + 7168, vx + 14336, (vx ^ 64) + 21504};
  const u32 c4 = (xcd >> 1) * 4 + (r >> 3), g = c4 >> 1;
  const bool xt = (c4 & 1) == 0;
  const u32 hh = lane >> 5, q = (lane & 15) >> 2;
  const u32 wst = xt ? 3072 : 2752;
  const u32 ym = xt ? 0u : ~0u, l4 = lane >> 4;
  const u32 blk = ((q ^ l4) + (((q & l4) << 1) & ym)) & 3;
  u32x4 vwa4 = steps4(l4 * wst + (lane & 3) * 16 + (blk & 1) * 64 + (blk >> 1) * (xt ? 1024 : 128) + (xt ? 1024 + g * 128 : g * 256), 4 * wst);
  u32 sx0 = (rt * 128 + 32 * j) * 1024 + (rot & 7) * 128, sw0 = ((rot & 7) * 64 + 16 * j) * wst;
  asm volatile("" : "+v"(vx4), "+v"(vwa4), "+s"(sx0), "+s"(sw0));
  sfence();
  const Ptrs p = args.get();
  const u32x4 srd_x = make_srd(p.x, 2048 * 1024);
  dma4<0>(srd_x, vx4, sx0, j * 4096);
  const u32x4 srd_w = make_srd(xt ? p.wqkv : p.w1, 512 * wst);
  dma4<0>(srd_w, vwa4, sw0, 16384 + j * 4096);
  sfence();
  auto issue_x = [&](int st) { dma4<0>(srd_x, vx4, (rt * 128 + 32 * j) * 1024 + ((st + rot) & 7) * 128, (st & 3) * 32768 + j * 4096); };
  auto issue_w = [&](int st) { dma4<0>(srd_w, vwa4, (((st + rot) & 7) * 64 + 16 * j) * wst, (st & 3) * 32768 + 16384 + j * 4096); };
  const u32 nl = (xt || (g < 6 && !(g & 1))) ? 2 : 1, lstep = xt ? 86 + g * 4 : 64 + (g >> 1) * 6 + ((g & 1) ? (g < 6 ? 4 : 2) : 0);
  const u32x4 vwb4 = steps4((lane >> 3) * wst + ((((lane & 7) ^ ((lane >> 2) & 4)) << 4) & (nl == 2 ? 127 : 48)) + (xt ? g * 128 : lstep * 32), 8 * wst);
  auto issue = [&](int st, int part) {
    if (st < 8) { if (part) issue_w(st); else issue_x(st); }
    else dma4<0>(srd_w, vwb4, ((((st - 8) * 4 + j + rot) & 7) * 64 + 32 * part) * wst, (st - 8) * 32768 + j * 8192 + part * 4096);
  };
  const u32x4 srd_pf = j == 3 ? make_srd(p.wo, 512 * 1024) : make_srd(p.w2, 1376 * 1024);
  const u32 vpf = (lane < (j == 2 ? 24 : 32) && (!PFH || ((lane >> 2) & 1) == (xcd & 1))) ? lane * 128 : OOB;
  const u32 spf = j < 2 ? (c4 >> 2) * 262144 + r * 8192 + j * 4096 : j == 2 ? (64 + 6 * (c4 >> 2)) * 16384 + r * 3072 : (c4 >> 2) * 131072 + r * 4096;
  const u32 pfst = opaque_s(PFS + r % PFN);
  const u32x4 srd_ws = make_srd(p.ws, WS_END);
  const u32 xa = (j * 32 + (lane & 31)) * 128 + ((hh ^ ((lane >> 1) & 7)) << 4);
  const u32 wb0 = opaque_v(hh * 1024 + q * 128 + (lane & 16) * 2 + (lane & 3) * 8 + ((q & 2) << 5)), wb1 = opaque_v(wb0 ^ 64);
  const u32 kq = c4 >> 2, cb = (c4 & 3) * 256;
  const u32x4 vw2 = steps4(l4 * 1024 + (lane & 3) * 16 + ((q ^ l4) << 6), 4096);
  const u32 csb = WS_CS + (c4 * 2 + (xcd & 1)) * 16384 + j * 512;
  bf16x8 xf[32], cw[8];
  u32x4 gr[6];
  u64 tag;
  u32 mn;
  float rl;
  f32x4 s1 = {}, s2 = {}, ca = {};
  const u32 late = opaque_s(STAG ? j >> 1 : 0);
  const u32 g_v0 = opaque_v(hh * 8192 + (lane & 31) * 16), g_v1 = opaque_v(hh * 4096 + (lane & 31) * 16);
  const u32 g_s0 = opaque_s(csb), g_s1 = opaque_s(csb + 4096), g_s2 = opaque_s(WS_STATS + rt * RT_B + j * 512);
  const u32 p_v0 = opaque_v(hh ? OOB : lane * 16), p_v1 = opaque_v(l4 == q ? (lane & 15) * 16 : OOB);
  const u32 p_s0 = opaque_s(csb + (r & 7) * 2048), p_s1 = opaque_s(j >= 2 ? WS_STATS + rt * RT_B + kq * 2048 + (c4 & 3) * 512 + (j & 1) * 256 : OOB);
  const u32 w_a = opaque_v(L_CORRA + j * 1024 + lane * 16), w_b = opaque_v((j < 2 ? L_CORRB + j * 1024 : L_WL) + lane * 16), w_s = opaque_v((hh ? L_RL : L_MN) + (j * 32 + (lane & 31)) * 4);
  const u32 r_x = opaque_v(((c4 & 3) * 32 + (j & 1) * 16 + (lane & 15)) * 128 + ((l4 ^ ((lane >> 1) & 7)) << 4)), r_x2 = opaque_v(r_x ^ 64);
  const u32 r_a = opaque_v(16384 + (8 * hh + q) * 256 + (lane & 16) * 2 + (lane & 3) * 8 + ((xt ? (j ^ q) : ((j - q) & 3)) << 6)), r_b = opaque_v(L_WL + (wb0 ^ ((j & 1) << 6)));
  const u32 selv = (((lane >> 4) ^ (lane >> 2)) & 1) ? 0u : 0x3f803f80u;
  const bf16x8 sel = opaque_v(as_bf((u32x4){selv, selv, selv, selv})), ones = opaque_v(as_bf((u32x4){0x3f803f80u, 0x3f803f80u, 0x3f803f80u, 0x3f803f80u}));
  u32 xal[4], xah[4];
#pragma unroll
  for (int s = 0; s < 4; ++s) { xal[s] = opaque_v(xa ^ (s << 5)); xah[s] = opaque_v(xal[s] + 65536); }
  const u32x4 srd_o = opaque_s(srd_ws), srd_p = opaque_s(srd_pf);
  const u32 vpf_ = opaque_v(vpf), spf_ = opaque_s(spf);
  asm volatile("" : "+v"(s1), "+v"(s2), "+v"(ca));
  sfence();
  issue_x(1); issue_w(1);
  dma2(srd_w, vwb4.x, vwb4.y, ((rot & 7) * 64 + 16 * j) * wst, L_WL + j * 2048);
  sfence();
#define GRAN_LOADS "buffer_load_dwordx4 %0, %6, %8, %9 offen sc1\n\tbuffer_load_dwordx4 %1, %6, %8, %9 offen offset:2048 sc1\n\t" \
                   "buffer_load_dwordx4 %2, %6, %8, %10 offen sc1\n\tbuffer_load_dwordx4 %3, %6, %8, %10 offen offset:2048 sc1\n\t" \
                   "buffer_load_dwordx4 %4, %7, %8, %11 offen sc1\n\tbuffer_load_dwordx4 %5, %7, %8, %11 offen offset:2048 sc1"
#define GRAN_OPS : "=&v"(gr[0]), "=&v"(gr[1]), "=&v"(gr[2]), "=&v"(gr[3]), "=&v"(gr[4]), "=&v"(gr[5]) \
                 : "v"(g_v0), "v"(g_v1), "s"(srd_o), "s"(g_s0), "s"(g_s1), "s"(g_s2) : "memory"
#pragma unroll
  for (int st = 0; st < 8; ++st) {
    const int n = st + 2;
    const u32 so_x = (rt * 128 + 32 * j) * 1024 + ((n + rot) & 7) * 128, so_w = (((n + rot) & 7) * 64 + 16 * j) * wst, la = (n & 3) * 32768 + j * 4096;
    constexpr int extra[8] = {2, 0, 0, 2, 0, 0, 6, 0};
    if (PF && st > PFS && st <= PFS + PFN)
      asm volatile("s_cmp_eq_u32 %0, %1\n\ts_cbranch_scc1 1f\n\ts_waitcnt vmcnt(%2)\n1:\n\ts_waitcnt vmcnt(%3)" ::"s"(pfst), "i"(st - 1), "i"(8 + extra[st]), "i"(9 + extra[st]) : "memory", "scc");
    else asm volatile("s_waitcnt vmcnt(%0)" ::"i"(8 + extra[st]) : "memory");
    barrier();
    if (st == 0) STAMP(1);
    auto capture = [&]() {
#pragma unroll
      for (int s = 0; s < 4; ++s) xf[st * 4 + s] = lds_b128((st & 2) ? xah[s] : xal[s], (st & 1) * 32768);
      sfence();
    };
    if (st == 5) asm volatile(GRAN_LOADS GRAN_OPS);
    if (PF && st >= PFS && st < PFS + PFN) touch64_if(pfst, st, srd_p, vpf_, spf_, L_JUNK);
    if (st < 3) {
      u32x2 tg;
      if (st == 0) tg = *LDSP(u32x2, L_SYNC + 128);
      if (st < 2) {
        cw[4] = lds_b128(r_x, st * 32768); cw[5] = lds_b128(r_x2, st * 32768);
#pragma unroll
        for (int s = 0; s < 4; ++s) cw[s] = st ? lds_tr<512>(r_b, s * 2048) : lds_tr<1024>(r_a, s * 4096);
      }
      sfence();
      dma8_if(late, 0, srd_x, vx4, so_x, la, srd_w, vwa4, so_w, la + 16384);
      sfence();
      __builtin_amdgcn_s_setprio(3);
      if (st < 2) {
        wait_lgkm();
        if (st == 0) {
#pragma unroll
          for (int u = 0; u < 4; ++u) ca = mfma16(sel, cw[u], ca);
          keep(ca);
          asm volatile("s_cmp_lt_u32 %5, 2\n\ts_cbranch_scc1 1f\n\tv_mfma_f32_16x16x32_bf16 %0, %2, %4, %0\n\tv_mfma_f32_16x16x32_bf16 %1, %2, %2, %1\n\t"
                       "v_mfma_f32_16x16x32_bf16 %0, %3, %4, %0\n\tv_mfma_f32_16x16x32_bf16 %1, %3, %3, %1\n1:"
                       : "+v"(s1), "+v"(s2) : "v"(cw[4]), "v"(cw[5]), "v"(ones), "s"(j) : "scc");
          tag = ((u64)(u32)__builtin_amdgcn_readfirstlane(tg.y) << 32) | (u32)__builtin_amdgcn_readfirstlane(tg.x);
        } else {
          asm volatile("s_cmp_lt_u32 %10, 2\n\ts_cbranch_scc1 1f\n\tv_mfma_f32_16x16x32_bf16 %0, %2, %4, %0\n\tv_mfma_f32_16x16x32_bf16 %1, %2, %2, %1\n\t"
                       "v_mfma_f32_16x16x32_bf16 %0, %3, %4, %0\n\tv_mfma_f32_16x16x32_bf16 %1, %3, %3, %1\n\ts_branch 2f\n1:\n\t"
                       "v_mfma_f32_16x16x32_bf16 %0, %5, %6, %0\n\tv_mfma_f32_16x16x32_bf16 %0, %5, %7, %0\n\t"
                       "v_mfma_f32_16x16x32_bf16 %0, %5, %8, %0\n\tv_mfma_f32_16x16x32_bf16 %0, %5, %9, %0\n2:"
                       : "+v"(s1), "+v"(s2) : "v"(cw[4]), "v"(cw[5]), "v"(ones), "v"(sel), "v"(cw[0]), "v"(cw[1]), "v"(cw[2]), "v"(cw[3]), "s"(j) : "scc");
        }
      } else {
        auto pick = [&](const f32x4& v) {
          return sel_mask(0xccccccccccccccccull, sel_mask(0xaaaaaaaaaaaaaaaaull, v[0], v[1]), sel_mask(0xaaaaaaaaaaaaaaaaull, v[2], v[3]));
        };
        const u32x4 o0 = {split_hi_lo(ca[0]), (u32)tag, split_hi_lo(s1[0]), (u32)(tag >> 32)};
        const u32x4 o1 = {asu(pick(s1)), (u32)tag, asu(pick(s2)), (u32)(tag >> 32)};
        __builtin_amdgcn_s_setprio(0);
        store_dev(o0, srd_o, p_v0, p_s0); store_dev(o1, srd_o, p_v1, p_s1);
      }
      __builtin_amdgcn_s_setprio(0);
      sfence();
      capture();
      if (STAG) dma8_if(late, 1, srd_x, vx4, so_x, la, srd_w, vwa4, so_w, la + 16384);
    } else if (st < 7) {
      issue(n, 0);
      sfence();
      capture();
      issue(n, 1);
    } else {
      capture();
      asm volatile("" : "+v"(gr[0]), "+v"(gr[1]), "+v"(gr[2]), "+v"(gr[3]), "+v"(gr[4]), "+v"(gr[5]));
      __builtin_amdgcn_s_setprio(3);
      auto bad6 = [&]() {
        u32 bad = opaque_v(((gr[0].y ^ (u32)tag) | (gr[1].y ^ (u32)tag) | (gr[2].y ^ (u32)tag)) | ((gr[3].y ^ (u32)tag) | (gr[4].y ^ (u32)tag) | (gr[5].y ^ (u32)tag)));
        bad |= opaque_v(((gr[0].w ^ (u32)(tag >> 32)) | (gr[1].w ^ (u32)(tag >> 32)) | (gr[2].w ^ (u32)(tag >> 32))) |
                        ((gr[3].w ^ (u32)(tag >> 32)) | (gr[4].w ^ (u32)(tag >> 32)) | (gr[5].w ^ (u32)(tag >> 32))));
        return !all_lanes(bad == 0);
      };
      if (__builtin_expect(bad6(), 0)) {
#pragma unroll 1
        for (u32 i = 0; i < SPIN_CAP; ++i) {
          __builtin_amdgcn_s_sleep(8);
          asm volatile(GRAN_LOADS "\n\ts_waitcnt vmcnt(0)" GRAN_OPS);
          if (!bad6()) break;
        }
      }
      *LDSP(u32x4, w_a) = (u32x4){gr[0].x, gr[1].x, gr[2].x, gr[3].x};
      *LDSP(u32x4, w_b) = (u32x4){gr[0].z, gr[1].z, gr[2].z, gr[3].z};
      sfence();
      if (ST7 == 1) issue(n, 0);
      sfence();
      const float mean = swap32_add(asf(gr[4].x) + asf(gr[5].x)) * (1.f / 512.f);
      const float var = __builtin_fmaxf(__builtin_fmaf(-mean, mean, swap32_add(asf(gr[4].z) + asf(gr[5].z)) * (1.f / 512.f)), 0.f);
      const u32 ms = pack2(mean, __builtin_amdgcn_sqrtf(rbf(rbf(var) + 1e-5f)));
      mn = ((ms & 0xffffu) ^ 0x8000u) * 0x10001u;
      rl = LOG2E * __builtin_amdgcn_rcpf(hi_f(ms));
      *LDSP(u32, w_s) = asu(sel_mask(0xffffffff00000000ull, asf(mn), rl));
      __builtin_amdgcn_s_setprio(0);
      sfence();
      if (ST7 == 0) issue(n, 0);
    }
    sfence();
  }
  wait_vm<4>();
  __builtin_amdgcn_s_setprio(1);
  barrier();
  STAMP(2);
  {
    f32x16 acc[2];
    bf16x8 wf[8];
    const u32 arow = WS_A + rt * RT_B + (j * NSTEP + lstep) * 1024, l16 = lane * 16;
    auto ldw = [&](int s) { wf[s & 7] = lds_tr<512>(s >= 32 ? wb1 : wb0, (s & 31) * 2048); sfence(); };
    issue(9, 1);
    wf[6] = lds_b128(L_CORRB + l16); wf[7] = lds_b128(L_CORRB + l16, 1024);
#pragma unroll
    for (int s = 0; s < 6; ++s) ldw(s);
    if (!WG0) {
      dma4<0>(make_srd(p.w2, 1376 * 1024), vw2, (16 * kq + j) * 16384 + cb, L_WE + j * 4096);
      dma4<0>(make_srd(p.w2, 1376 * 1024), vw2, (16 * kq + 4 + j) * 16384 + cb, L_WE + (4 + j) * 4096);
    }
    STAMP(8);
    const bf16x8 mf = as_bf((u32x4){mn, mn, mn, mn});
    acc[0] = mfma32(wf[6], mf, (f32x16){}); acc[1] = mfma32(wf[7], mf, (f32x16){});
    keep(acc[0]); keep(acc[1]);
    sfence();
#pragma unroll
    for (int s = 0; s < 32; ++s) {
      acc[0] = mfma32(wf[s & 7], xf[s], acc[0]); keep(acc[0]); sfence();
      if (s == BARS) asm volatile("s_waitcnt vmcnt(%0)\n\ts_barrier" ::"i"(WG0 ? 0 : 8) : "memory");
      ldw(s + 6);
    }
    STAMP(12);
    auto relu_store = [&](const f32x16& a, int h) {
      u32x4 o;
#pragma unroll
      for (int i = 0; i < 4; ++i) o[i] = relu2(pack2(a[8 * (h & 1) + 2 * i], a[8 * (h & 1) + 2 * i + 1]));
      store_dev(o, srd_ws, l16, arow + h * 1024);
    };
    if (G2SKIP && !WG0) wait_vm<0>();
    if (xt) {
      float t[16], e[16];
      u32x4 o[2];
      static_for<32>([&](auto S_) {
        constexpr int s = S_;
        if constexpr (s % WGRP == 0 && s + 6 <= 32) { __builtin_amdgcn_s_waitcnt(0xC07F | ((12 - 2 * WGRP) << 8)); sfence(); }
        acc[1] = mfma32(wf[(32 + s) & 7], xf[s], acc[1]); keep(acc[1]); sfence();
        if constexpr (s + 6 < 32) ldw(32 + s + 6);
        static_for<100>([&](auto K_) {
          constexpr int k = K_, i = k / 5, op = k % 5;
          if constexpr (phi_gap(k) == s) {
            if constexpr (op == 0) t[i] = mul1(acc[0][i], rl);
            if constexpr (op == 1) { e[i - 1] = __builtin_amdgcn_fmed3f(__builtin_amdgcn_exp2f(t[i - 1]), 0.f, 1.f); keep(e[i - 1]); }
            if constexpr (op == 2) { t[i - 1] = max_eps(t[i - 1]); keep(t[i - 1]); }
            if constexpr (op == 3) { e[i - 2] = __builtin_fmaf(t[i - 2], LN2, e[i - 2]); keep(e[i - 2]); }
            if constexpr (op == 4) { u32 v = pack2(e[i - 4], e[i - 3]); keep(v); o[(i - 4) >> 3][((i - 4) >> 1) & 3] = v; }
          }
        });
        if constexpr (s == 29 || s == 30) store_dev(o[s - 29], srd_ws, l16, arow + (s - 29) * 1024);
        sfence();
      });
      STAMP(3);
      __builtin_amdgcn_s_setprio(3);
#pragma unroll
      for (int h = 2; h < 4; ++h) {
        float t2[8];
#pragma unroll
        for (int i = 0; i < 8; ++i) t2[i] = mul1(acc[1][8 * (h & 1) + i], rl);
        sfence();
        store_dev(phi8(t2), srd_ws, l16, arow + h * 1024);
      }
    } else if (nl == 2) {
#pragma unroll
      for (int s = 0; s < 32; ++s) {
        acc[1] = mfma32(wf[(32 + s) & 7], xf[s], acc[1]); keep(acc[1]); sfence();
        if (s + 6 < 32) ldw(32 + s + 6);
        if (s == 27 || s == 29) relu_store(acc[0], (s - 27) >> 1);
        sfence();
      }
      STAMP(3);
      __builtin_amdgcn_s_setprio(3);
      relu_store(acc[1], 2); relu_store(acc[1], 3);
    } else {
      STAMP(3);
      __builtin_amdgcn_s_setprio(3);
      relu_store(acc[0], 0); relu_store(acc[0], 1);
    }
  }
  STAMP(4);
  lds_signal(L_SYNC + 12);
  lds_wait(L_SYNC + 12, 4);
  dma4<0>(make_srd(p.wo, 512 * 1024), vw2, (8 * kq + j) * 16384 + cb, j * 4096);
  if (WO2) dma4<0>(make_srd(p.wo, 512 * 1024), vw2, (8 * kq + 4 + j) * 16384 + cb, (4 + j) * 4096);
  wait_vm<WO2 ? 8 : 4>();
  store_one((u32x4){(u32)tag, (u32)(tag >> 32), 0u, 0u}, srd_ws, WS_FLAGS + ((rt * 2 + (j >> 1)) * 48 + (xt ? 32 : 16) + g * 2 + (j & 1)) * 128);
  if (G2EARLY) lds_signal(L_SYNC + 8);
  STAMP(5);
  const u32 job = ((g < 6 ? g >> 1 : g - 3) * 4 + j) * 16 + rt;
  if (nl == 1 && job < 272) {
    const u32 sa = job < 256 ? WS_KVP + job * 4096 : WS_ZP + rt * 4096, fo = job < 256 ? WS_KVF + job * 128 : WS_KVZ + rt * 128;
    __builtin_amdgcn_s_sleep(ZDLY);
    u32x4 a[4];
#pragma unroll 1
    for (u32 i = 0; i < SPIN_CAP; ++i) {
      asm volatile("buffer_load_dwordx4 %0, %4, %5, %6 offen sc1\n\tbuffer_load_dwordx4 %1, %4, %5, %6 offen offset:1024 sc1\n\t"
                   "buffer_load_dwordx4 %2, %4, %5, %6 offen offset:2048 sc1\n\tbuffer_load_dwordx4 %3, %4, %5, %6 offen offset:3072 sc1\n\ts_waitcnt vmcnt(0)"
                   : "=&v"(a[0]), "=&v"(a[1]), "=&v"(a[2]), "=&v"(a[3]) : "v"(lane * 16), "s"(srd_ws), "s"(sa) : "memory");
      u32 bad = 0;
#pragma unroll
      for (int k = 0; k < 4; ++k) bad |= (a[k].y ^ (u32)tag) | (a[k].w ^ (u32)(tag >> 32));
      if (all_lanes(bad == 0)) break;
      __builtin_amdgcn_s_sleep(RSLP);
    }
      float v[4] = {(lo_f(a[0].x) + lo_f(a[1].x)) + (lo_f(a[2].x) + lo_f(a[3].x)), (hi_f(a[0].x) + hi_f(a[1].x)) + (hi_f(a[2].x) + hi_f(a[3].x)),
                    (lo_f(a[0].z) + lo_f(a[1].z)) + (lo_f(a[2].z) + lo_f(a[3].z)), (hi_f(a[0].z) + hi_f(a[1].z)) + (hi_f(a[2].z) + hi_f(a[3].z))};
#pragma unroll
      for (int i = 0; i < 4; ++i) v[i] = dpp_add<0x128>(v[i]);
#pragma unroll
      for (int i = 0; i < 4; ++i) v[i] = swap16_add(v[i]);
#pragma unroll
      for (int i = 0; i < 4; ++i) v[i] = swap32_add(v[i]);
      store_dev((u32x4){pack2(v[0], v[1]), (u32)tag, pack2(v[2], v[3]), (u32)(tag >> 32)}, srd_ws, lane < 8 ? lane * 16 : OOB, fo);
  }
  if (!G2EARLY) lds_signal(L_SYNC + 8);
  __builtin_amdgcn_s_setprio(0);
  if (!WO2) dma4<0>(make_srd(p.wo, 512 * 1024), vw2, (8 * kq + 4 + j) * 16384 + cb, (4 + j) * 4096);
  STAMP(6);
  const char* fq = p.ws + WS_FLAGS + ((rt * 2 + (j >> 1)) * 48 + 16 + kq * 4 + (j & 1)) * 128;
  const u32 rdy = L_SYNC + 48 + j * 4;
  u32 now = 0;
#define FLAG_LOADS(A, B, C, D, P) "s_load_dwordx2 " A ", " P ", 0 glc\n\ts_load_dwordx2 " B ", " P ", 256 glc\n\ts_load_dwordx2 " C ", " P ", 2048 glc\n\t" \
                                  "s_load_dwordx2 " D ", " P ", 2304 glc\n\ts_waitcnt lgkmcnt(0)"
  auto tell = [&](u64 f0, u64 f1, u64 f2, u64 f3) {
    const u32 nw = ((f0 ^ tag) | (f1 ^ tag)) ? 0 : (((f2 ^ tag) | (f3 ^ tag)) ? 1 : 2);
    if (nw != now) { now = nw; *LDSP(u32, rdy) = nw; }
  };
#pragma unroll 1
  for (u32 i = 0; i < SPIN_CAP && now < (KD ? (xt ? 0 : 1) : KVAFTER); ++i) {
    u64 f0, f1, f2, f3;
    asm volatile(FLAG_LOADS("%0", "%1", "%2", "%3", "%4") : "=&s"(f0), "=&s"(f1), "=&s"(f2), "=&s"(f3) : "s"(fq) : "memory");
    tell(f0, f1, f2, f3);
  }
  if (KD && !xt) __builtin_amdgcn_s_sleep(KD);
  wait_vm<0>();
  lds_signal(L_SYNC + 32);
  __builtin_amdgcn_s_setprio(KPRIO);
  STAMP(7);
  u32x4 k0, k1, z0, z1;
#define KV_LOADS "buffer_load_dwordx4 %0, %4, %6, %7 offen sc1\n\tbuffer_load_dwordx4 %1, %4, %6, %7 offen offset:1024 sc1\n\t" \
                 "buffer_load_dwordx4 %2, %5, %6, %8 offen sc1\n\tbuffer_load_dwordx4 %3, %5, %6, %8 offen offset:64 sc1"
  u32 l16 = lane * 16, lz = l4 * 16, ko = WS_KVF + (kq * 8 + j * 2) * 1024, zo = WS_KVZ + (kq * 8 + j * 2) * 64;
  u32x4 srd2 = srd_ws;
#pragma unroll 1
  for (u32 i = 0; i < SPIN_CAP; ++i) {
    u64 f0, f1, f2, f3;
    asm volatile(KV_LOADS "\n\t" FLAG_LOADS("%9", "%10", "%11", "%12", "%13")
                 : "=&v"(k0), "=&v"(k1), "=&v"(z0), "=&v"(z1), "+v"(l16), "+v"(lz), "+s"(srd2), "+s"(ko), "+s"(zo), "=&s"(f0), "=&s"(f1), "=&s"(f2), "=&s"(f3)
                 : "s"(fq) : "memory");
    tell(f0, f1, f2, f3);
    asm volatile("s_waitcnt vmcnt(0)" : "+v"(k0), "+v"(k1), "+v"(z0), "+v"(z1));
    const u32 bad = ((k0.y ^ (u32)tag) | (k1.y ^ (u32)tag) | (z0.y ^ (u32)tag) | (z1.y ^ (u32)tag)) |
                    ((k0.w ^ (u32)(tag >> 32)) | (k1.w ^ (u32)(tag >> 32)) | (z0.w ^ (u32)(tag >> 32)) | (z1.w ^ (u32)(tag >> 32)));
    if (all_lanes(bad == 0)) break;
  }
  STAMP(9);
  {
    auto a0 = __builtin_amdgcn_permlane16_swap(k0.x, z0.x, false, false), a1 = __builtin_amdgcn_permlane16_swap(k0.z, z0.z, false, false);
    auto b0 = __builtin_amdgcn_permlane16_swap(k1.x, z1.x, false, false), b1 = __builtin_amdgcn_permlane16_swap(k1.z, z1.z, false, false);
    *LDSP(u32x4, L_KVF + j * 2048 + lane * 16) = (u32x4){a0[0], a1[0], a0[1], a1[1]};
    *LDSO(u32x4, L_KVF + j * 2048 + lane * 16, 1024) = (u32x4){b0[0], b1[0], b0[1], b1[1]};
    lds_signal(L_SYNC + 36);
  }
#pragma unroll 1
  for (u32 i = 0; i < SPIN_CAP && now < 2; ++i) {
    u64 f0, f1, f2, f3;
    asm volatile(FLAG_LOADS("%0", "%1", "%2", "%3", "%4") : "=&s"(f0), "=&s"(f1), "=&s"(f2), "=&s"(f3) : "s"(fq) : "memory");
    tell(f0, f1, f2, f3);
  }
  STAMP(13);
}

template <bool XT> DEV void group0_first(const Ptrs& p, u32 b, u32 lane, u32 w, u64& tag, u32* stamp) {
  const u32 xcd = b & 7, r = b >> 3, rt = (xcd & 1) * 8 + (r & 7), c4 = (xcd >> 1) * 4 + (r >> 3), g = c4 >> 1;
  const u32 mh = w >> 1, nh = w & 1, hh = lane >> 5, q = (lane & 15) >> 2;
  f32x16 acc[2][2] = {};
  bf16x8 xf[4][2] = {}, wf[4][2] = {};
  u32 xa = (mh * 64 + (lane & 31)) * 128 + ((hh ^ ((lane >> 1) & 7)) << 4);
  u32 wa = 16384 + (8 * hh + q) * 256 + (lane & 16) * 2 + (lane & 3) * 8 + ((XT ? (nh ^ q) : ((nh - q) & 3)) << 6), wb = wa ^ 128;
  u32 ca = L_CORRA + nh * 1024 + lane * 16;
  const u32x4 srd_ws = make_srd(p.ws, WS_END);
  u32x4 vw2 = steps4((lane >> 4) * 1024 + (lane & 3) * 16 + ((q ^ (lane >> 4)) << 6), 4096);
  auto wlate = [&]() {
    dma4<0>(make_srd(p.w2, 1376 * 1024), vw2, (64 + 6 * (c4 >> 2) + w) * 16384 + (c4 & 3) * 256, L_WL + w * 4096);
    if (w < 2) dma4<0>(make_srd(p.w2, 1376 * 1024), vw2, (68 + 6 * (c4 >> 2) + w) * 16384 + (c4 & 3) * 256, L_WL + (4 + w) * 4096);
  };
  auto wchunk = [&](int i) { dma4<0>(make_srd(p.w2, 1376 * 1024), vw2, (16 * (c4 >> 2) + 4 * i + w) * 16384 + (c4 & 3) * 256, L_WE + (4 * i + w) * 4096); };
  u32 ma0 = L_MN + (mh * 64 + (lane & 31)) * 4;
  *LDSP(u32, L_SYNC + (lane & 31) * 4) = 0u;
  if (w == 0) {
    u64 e;
    asm volatile("s_load_dwordx2 %0, %1, 0\n\ts_waitcnt lgkmcnt(0)" : "=&s"(e) : "s"(p.ws) : "memory");
    e += 0x9E3779B97F4A7C15ull;
    *LDSP(u32x2, L_SYNC + 128) = (u32x2){(u32)e, (u32)(e >> 32)};
  }

  asm volatile("" : "+v"(acc[0][0]), "+v"(acc[0][1]), "+v"(acc[1][0]), "+v"(acc[1][1]), "+v"(vw2));
  asm volatile("" : "+v"(xf[2][0]), "+v"(xf[2][1]), "+v"(wf[2][0]), "+v"(wf[2][1]), "+v"(xf[3][0]), "+v"(xf[3][1]), "+v"(wf[3][0]), "+v"(wf[3][1]),
               "+v"(xa), "+v"(wa), "+v"(wb), "+v"(ca), "+v"(ma0));
  STAMP(1);
  auto ldx = [&](int s, int h) { xf[s][h] = lds_b128(xa ^ (s << 5), h * 4096); sfence(); };
  auto ldw = [&](int s, int h) { wf[s][h] = lds_tr<1024>(h ? wb : wa, s * 4096); sfence(); };
  auto m1 = [&](int s, int i) {
    const int t = (i + 1) >> 1 & 1, u = i >> 1;
    acc[t][u] = XT ? mfma32(xf[s][t], wf[s][u], acc[t][u]) : mfma32(wf[s][u], xf[s][t], acc[t][u]);
    sfence();
  };
  __builtin_amdgcn_s_setprio(2);
  barrier();
#pragma unroll 1
  for (u32 st = 0; st < 8; ++st) {
    m1(2, 0); ldw(0, 0); m1(2, 1); ldx(0, 0); ldx(0, 1); m1(2, 2); ldw(0, 1); m1(2, 3); ldw(1, 0);
    m1(3, 0); ldx(1, 0); ldx(1, 1); m1(3, 1); ldw(1, 1); m1(3, 2); ldw(2, 0); m1(3, 3); ldx(2, 0); ldx(2, 1);
    m1(0, 0); ldw(2, 1); m1(0, 1); ldw(3, 0); m1(0, 2); ldx(3, 0); ldx(3, 1); m1(0, 3); ldw(3, 1);
    m1(1, 0);
    xa = (xa + 32768) & 131071; wa = (wa + 32768) & 131071; wb = (wb + 32768) & 131071;
    asm volatile("" : "+v"(xa), "+v"(wa), "+v"(wb));
    sfence();
    m1(1, 1); m1(1, 2); m1(1, 3);
    barrier();
  }
  f32x4 rl[8];
  m1(2, 0);
  const u32x2 tg = *LDSP(u32x2, L_SYNC + 128);
  const u32 n0 = *LDSP(u32, ma0), n1 = *LDSO(u32, ma0, 128);
  wf[0][0] = lds_b128(ca); wf[0][1] = lds_b128(ca, 2048);
  sfence();
  m1(2, 3);
  if (XT) {
    const u32 ra = opaque_v(L_RL + (mh * 64 + hh * 4) * 4);
#pragma unroll
    for (int i = 0; i < 8; ++i) rl[i] = *LDSO(f32x4, ra, i * 32);
    sfence();
  }
  m1(3, 0);
  tag = ((u64)(u32)__builtin_amdgcn_readfirstlane(tg.y) << 32) | (u32)__builtin_amdgcn_readfirstlane(tg.x);
  xf[0][0] = as_bf((u32x4){n0, n0, n0, n0}); xf[0][1] = as_bf((u32x4){n1, n1, n1, n1});
  keep(xf[0][0]); keep(xf[0][1]);
  sfence();
  m1(3, 3); m1(0, 0); m1(0, 3); m1(2, 1); m1(2, 2); m1(3, 1); m1(3, 2); m1(0, 1); m1(0, 2);
  STAMP(2);
  if constexpr (!XT) {
    __builtin_amdgcn_s_setprio(YPRIO);
#pragma unroll
    for (int k = 0; k < 8; ++k) {
      const int t = k >> 2, u = (k >> 1) & 1, s = k & 1;
      u32x4 o;
#pragma unroll
      for (int i = 0; i < 4; ++i) o[i] = relu2(pack2(acc[t][u][8 * s + 2 * i], acc[t][u][8 * s + 2 * i + 1]));
      store_dev(o, srd_ws, lane * 16, WS_A + rt * RT_B + ((mh * 2 + t) * NSTEP + g * 8 + u * 4 + nh * 2 + s) * 1024);
      if (k == YBAR) asm volatile("s_barrier" ::: "memory");
    }
    STAMP(3);
    wchunk(2);
    if (YW2) { wchunk(3); wait_vm<8>(); } else wait_vm<4>();
    store_one((u32x4){(u32)tag, (u32)(tag >> 32), 0u, 0u}, srd_ws, WS_FLAGS + ((rt * 2 + mh) * 48 + g * 2 + nh) * 128);
    __builtin_amdgcn_s_setprio(0);
    if (!YW2) wchunk(3);
    if (WG0) { wchunk(0); wchunk(1); }
    wlate();
  } else {
    f32x16 kv = {}, zz = {};
    const bf16x8 ones = as_bf((u32x4){0x3f803f80u, 0x3f803f80u, 0x3f803f80u, 0x3f803f80u});
    const bool up = lane & 16;
    const u32 kvb = WS_KVP + (g * 4 + nh * 2) * KVH_B + (rt * 2 + mh) * 128;
    const u32 o0 = opaque_v((up ? KVH_B : 0) + hh * 16384 + (lane & 8) * 512 + (lane & 7) * 16);
    const u32 oz = opaque_v((lane & 14) ? OOB : (up ? 64 : 0) + hh * 32 + (lane & 1) * 16);
#pragma unroll
    for (int k = 0; k < 4; ++k) {
      const int t = k >> 1, s = k & 1;
      if (k == XHOLD) asm volatile("s_barrier" ::: "memory");
      if (XDMA == 0 && (k == 1 || k == 2)) wchunk(k + 1);
      if (XDMA == 0 && k == 3) wlate();
      float t2[8], v2[8];
#pragma unroll
      for (int i = 0; i < 8; ++i) t2[i] = mul1(acc[t][0][8 * s + i], rl[2 * k + (i >> 2)][i & 3]);
#pragma unroll
      for (int i = 0; i < 8; ++i) v2[i] = mul1(acc[t][1][8 * s + i], rl[2 * k + (i >> 2)][i & 3]);
      sfence();
      const u32x4 pv = {pack2(v2[0], v2[1]), pack2(v2[2], v2[3]), pack2(v2[4], v2[5]), pack2(v2[6], v2[7])};
      const u32x4 pk = phi8(t2);
      kv = mfma32(as_bf(pk), as_bf(pv), kv);
      zz = mfma32(as_bf(pk), ones, zz);
      sfence();
    }
    if (XHOLD == 4) asm volatile("s_barrier" ::: "memory");
    u32 ks[4], zs[4];
#pragma unroll
    for (int i = 0; i < 4; ++i) { const u32 lo = pack2(kv[2 * i], kv[2 * i + 1]), hi = pack2(kv[8 + 2 * i], kv[9 + 2 * i]); ks[i] = up ? hi : lo; }
    store_dev((u32x4){ks[0], (u32)tag, ks[1], (u32)(tag >> 32)}, srd_ws, o0, kvb);
    store_dev((u32x4){ks[2], (u32)tag, ks[3], (u32)(tag >> 32)}, srd_ws, o0, kvb + 8192);
#pragma unroll
    for (int i = 0; i < 4; ++i) { const u32 lo = pack2(zz[2 * i], zz[2 * i + 1]), hi = pack2(zz[8 + 2 * i], zz[9 + 2 * i]); zs[i] = up ? hi : lo; }
    const bool od = lane & 1;
    store_dev((u32x4){od ? zs[2] : zs[0], (u32)tag, od ? zs[3] : zs[1], (u32)(tag >> 32)}, srd_ws, oz, WS_ZP + (g * 2 + nh) * 4096 + (rt * 2 + mh) * 128);
    if (XHOLD == 5) asm volatile("s_barrier" ::: "memory");
    STAMP(3);
    __builtin_amdgcn_s_setprio(0);
    if (XDMA == 1) { wchunk(2); wchunk(3); wait_vm<8>(); if (WG0) { wchunk(0); wchunk(1); } wlate(); }
  }
}

DEV void group0(u32 b, u32 lane, u32 w, u32* stamp) {
  Args args;
  args.request(0);
  const Ptrs p = args.get();
  u64 tag;
  if (b & 64) group0_first<false>(p, b, lane, w, tag, stamp); else group0_first<true>(p, b, lane, w, tag, stamp);
  STAMP(5);
  b = opaque_s(b); lane = opaque_v(lane);
  const u32 rt = (b & 1) * 8 + ((b >> 3) & 7), kq = (b >> 1) & 3, ct = b >> 6, hh = lane >> 5, q = (lane & 15) >> 2;
  const __amdgpu_buffer_rsrc_t rws = __builtin_amdgcn_make_buffer_rsrc(p.ws, 0, WS_END, 0x00020000);
  const u32x4 srd_ws = make_srd(p.ws, WS_END);
  const char* fp = p.ws + WS_FLAGS + ((rt * 2 + (w >> 1)) * 48 + kq * 4) * 128;
  const u32 ab = WS_A + rt * RT_B + w * NSTEP * 1024;
  u32 wl[4], we[4], wm[4];
#pragma unroll
  for (int i = 0; i < 4; ++i) {
    wl[i] = opaque_v((4 * hh + q) * 256 + (lane & 16) * 2 + (lane & 3) * 8 + ((i ^ kq ^ q) << 6));
    we[i] = opaque_v(wl[i] + L_WE);
    wm[i] = opaque_v(wl[i] + L_WL);
  }
  const float rlv = *LDSP(float, L_RL + (w * 32 + (lane & 31)) * 4);
  const u32 tok = rt * 128 + w * 32 + (lane & 31), oo = tok * 1024 + ct * 256 + kq * 64 + hh * 16;
  u32x4 xr0, xr1;
  const u32 rdy = opaque_v(L_SYNC + 48 + w * 4), cl = opaque_v(L_SYNC + 32);
  u32x4 af[30];
  const u32 l16 = opaque_v(lane * 16);
  auto lda = [&](int s) {
    const u32 so = s < 16 ? ab + (16 * kq + s) * 1024 : s < 22 ? ((s < 20 || kq < 3) ? ab + (48 + 6 * kq + s) * 1024 : OOB) : ab + (64 + 8 * kq + s) * 1024;
    asm volatile("buffer_load_dwordx4 %0, %1, %2, %3 offen sc1" : "=v"(af[s]) : "v"(l16), "s"(srd_ws), "s"(so) : "memory");
  };
  bf16x8 wf[3][4], kf[4];
  f32x16 acc[4] = {}, d[2];
  auto ldw = [&](int s, int i) { wf[s % 3][i] = lds_tr<2048>(s < 16 ? we[i] : s < 22 ? wm[i] : wl[i], (s < 16 ? s : s < 22 ? s - 16 : s - 22) * 4096); sfence(); };
  auto peek = [&](u32 a, int o) { return __hip_atomic_load(LDSO(u32, a, o), __ATOMIC_RELAXED, __HIP_MEMORY_SCOPE_WORKGROUP); };
  auto pin = [&](f32x16& v) { asm volatile("" : "+v"(v)); sfence(); };
  const float sc = __builtin_amdgcn_rcpf(rlv);
  f32x16 ceps = {0.f, 0.f, 0.f, 0.f, 0.f, 0.f, 0.f, 0.f, 1e-6f, 1e-6f, 1e-6f, 1e-6f, 1e-6f, 1e-6f, 1e-6f, 1e-6f};
  keep(ceps);
  u32 c0 = 0, c1 = 0;
  poll4(fp, tag, SPIN_CAP);
  wait_vm<0>();
  lds_signal(L_SYNC + 4);
  STAMP(6);
  lds_wait(L_SYNC + 12, 4);
#pragma unroll
  for (int s = 0; s < LEAD; ++s) lda(s);
  lds_wait(L_SYNC + 4, 4);
  if (G2SKIP == 0 || (G2SKIP == 1 && (b & 64))) lds_wait(L_SYNC + 8, 4);
#pragma unroll
  for (int i = 0; i < 8; ++i) if ((i & 3) || (i >> 2) < DEF0) ldw(i >> 2, i & 3);
  __builtin_amdgcn_s_setprio(GPRIO);
  STAMP(7);
  static_for<30>([&](auto S_) {
    constexpr int s = S_;
    if (s == 14 - LEAD || s == 20 - LEAD) { c0 = peek(rdy, 0); sfence(); }
    if (s == 16 - LEAD) { if ((u32)__builtin_amdgcn_readfirstlane(c0) < 1) lds_wait(rdy, 1); STAMP(8); }
    if (s == 22 - LEAD) { if ((u32)__builtin_amdgcn_readfirstlane(c0) < 2) lds_wait(rdy, 2); STAMP(9); }
    if constexpr (s + LEAD < 30) lda(s + LEAD);
    if (s == 17) { c0 = peek(cl, 0); sfence(); }
    if (s == 19 && (u32)__builtin_amdgcn_readfirstlane(c0) < 4) lds_wait(cl, 4);
    if (s == 19) { c1 = peek(cl, 4); sfence(); }
    constexpr int h = s - 22;
    float f, m[8];
    auto scale = [&](const f32x16& dd) { f = mul1(sc, __builtin_amdgcn_rcpf(dd[8])); sfence(); };
    auto mul4 = [&](f32x16& dd, int o) { for (int i = o; i < o + 4; ++i) m[i] = mul1(dd[i], f); if (o) keep(dd); sfence(); };
    auto fin = [&](u32x4& o) { o = (u32x4){pack2(m[0], m[1]), pack2(m[2], m[3]), pack2(m[4], m[5]), pack2(m[6], m[7])}; keep(o); sfence(); };
    if constexpr (s == 21) {
      if ((u32)__builtin_amdgcn_readfirstlane(c1) < 4) lds_wait(cl + 4, 4);
#pragma unroll
      for (int i = 0; i < 4; ++i) kf[i] = lds_b128(L_KVF + lane * 16, i * 1024);
      sfence();
    }
    if constexpr (s == 22) {
      STAMP(4);
      need<s, 22>(af[22]); need<s, 23>(af[23]);
      d[0] = mfma32(kf[0], as_bf(af[22]), ceps); pin(d[0]);
      d[1] = mfma32(kf[1], as_bf(af[23]), ceps); pin(d[1]);
      scale(d[0]); mul4(d[0], 0); mul4(d[0], 4); fin(af[22]);
    }
    constexpr bool at = h >= 0 && h < 7;
    if constexpr (s < 22) need<s, s>(af[s]);
    if constexpr (h >= 0 && h < 6) { need<s, s + 2>(af[s + 2]); d[h & 1] = mfma32(kf[(h + 2) & 3], as_bf(af[s + 2]), ceps); pin(d[h & 1]); }
    if constexpr (h >= 0 && h < 4) { kf[h] = lds_b128(L_KVF + lane * 16, (h + 4) * 1024); sfence(); }
    if constexpr (at) scale(d[(h + 1) & 1]);
    acc[1] = mfma32(wf[s % 3][1], as_bf(af[s]), acc[1]); pin(acc[1]);
    if (s + 2 < 30) ldw(s + 2, 1);
    if constexpr (at) mul4(d[(h + 1) & 1], 0);
    acc[2] = mfma32(wf[s % 3][2], as_bf(af[s]), acc[2]); pin(acc[2]);
    if (s + 2 < 30) ldw(s + 2, 2);
    if constexpr (at) mul4(d[(h + 1) & 1], 4);
    acc[3] = mfma32(wf[s % 3][3], as_bf(af[s]), acc[3]); pin(acc[3]);
    if (s + 2 < 30) ldw(s + 2, 3);
    if constexpr (s < DEF0) { acc[0] = mfma32(wf[s % 3][0], as_bf(af[s]), acc[0]); pin(acc[0]); }
    if constexpr (s + 2 < DEF0) ldw(s + 2, 0);
    if constexpr (at) fin(af[s + 1]);
  });
  STAMP(10);
  bf16x8 w0[8];
  auto ld0 = [&](int s) { w0[s & 7] = lds_tr<2048>(s < 16 ? we[0] : s < 22 ? wm[0] : wl[0], (s < 16 ? s : s < 22 ? s - 16 : s - 22) * 4096); sfence(); };
#pragma unroll
  for (int s = DEF0; s < DEF0 + 6; ++s) ld0(s);
  const u32 xb = WS_X + rt * RT_B + (ct * 4 + w) * 32768;
#pragma unroll
  for (int i = 1; i < 4; ++i)
#pragma unroll
    for (int k = 0; k < 2; ++k)
      store_dev((u32x4){pack2(acc[i][8 * k], acc[i][8 * k + 1]), pack2(acc[i][8 * k + 2], acc[i][8 * k + 3]), pack2(acc[i][8 * k + 4], acc[i][8 * k + 5]),
                        pack2(acc[i][8 * k + 6], acc[i][8 * k + 7])}, srd_ws, lane * 16, xb + ((kq ^ i) * 4 + kq) * 2048 + k * 1024);
  asm volatile("global_load_dwordx4 %0, %2, %3\n\tglobal_load_dwordx4 %1, %2, %3 offset:32" : "=&v"(xr0), "=&v"(xr1) : "v"(oo), "s"(p.x) : "memory");
  const u32 xf = WS_XFL + ((rt * 4 + ct) * 4 + w) * 512;
#pragma unroll
  for (int s = DEF0; s < 30; ++s) {
    acc[0] = mfma32(w0[s & 7], as_bf(af[s]), acc[0]); pin(acc[0]);
    if (s + 6 < 30) ld0(s + 6);
    if (s == (DEF0 + FLAGAT < 29 ? DEF0 + FLAGAT : 29)) { wait_vm<2>(); store_one((u32x4){(u32)tag, (u32)(tag >> 32), 0u, 0u}, srd_ws, xf + kq * 128); }
  }
  asm volatile("s_waitcnt vmcnt(1)" : "+v"(xr0), "+v"(xr1));
  u32x4 perm0, perm1;
  {
    const u32 row = lane & 15, e = (row & 3) + ((row >> 3) << 2), hit = ((row >> 2) & 1) == hh ? 0x3f80u << (16 * (e & 1)) : 0u;
#pragma unroll
    for (int i = 0; i < 4; ++i) { const u32 t = (e >> 1) == (u32)i ? hit : 0u; perm0[i] = (lane & 16) ? 0u : t; perm1[i] = (lane & 16) ? t : 0u; }
    keep(perm0); keep(perm1);
  }
  float xv[16];
#pragma unroll
  for (int e = 0; e < 4; ++e) { xv[2 * e] = lo_f(xr0[e]); xv[2 * e + 1] = hi_f(xr0[e]); xv[8 + 2 * e] = lo_f(xr1[e]); xv[9 + 2 * e] = hi_f(xr1[e]); }
#pragma unroll
  for (int e = 0; e < 16; ++e) keep(xv[e]);
  STAMP(11);
  poll4(p.ws + xf, tag, SPIN_CAP);
  STAMP(12);
  u32x4 pp[3][2];
#pragma unroll
  for (int i = 0; i < 3; ++i) {
    pp[i][0] = __builtin_amdgcn_raw_buffer_load_b128(rws, lane * 16, xb + (kq * 4 + (kq ^ (i + 1))) * 2048, 16);
    pp[i][1] = __builtin_amdgcn_raw_buffer_load_b128(rws, lane * 16 + 1024, xb + (kq * 4 + (kq ^ (i + 1))) * 2048, 16);
  }
  sfence();
#pragma unroll
  for (int i = 0; i < 3; ++i) { acc[0] = mfma32(as_bf(perm0), as_bf(pp[i][0]), acc[0]); acc[0] = mfma32(as_bf(perm1), as_bf(pp[i][1]), acc[0]); }
  float v[16];
#pragma unroll
  for (int e = 0; e < 16; ++e) v[e] = acc[0][e];
  const float rs = rlv * LN2;
#pragma unroll
  for (int k = 0; k < 2; ++k) {
#pragma unroll
    for (int e = 0; e < 4; ++e) {
      auto sw = __builtin_amdgcn_permlane32_swap(asu(v[8 * k + e]), asu(v[8 * k + 4 + e]), false, false);
      v[8 * k + e] = asf(sw[0]); v[8 * k + 4 + e] = asf(sw[1]);
    }
    u32x4 o;
#pragma unroll
    for (int e = 0; e < 4; ++e) o[e] = pack2(__builtin_fmaf(v[8 * k + 2 * e], rs, xv[8 * k + 2 * e]), __builtin_fmaf(v[8 * k + 2 * e + 1], rs, xv[8 * k + 2 * e + 1]));
    *(GAS u32x4*)(p.out + oo + k * 32) = o;
  }
  if (b == 0 && w == 0) store_one((u32x4){(u32)tag, (u32)(tag >> 32), 0u, 0u}, srd_ws, WS_EPOCH);
  STAMP(13);
}

__global__ __launch_bounds__(512, 2) void fused_block(const char* __restrict__ x, const char* __restrict__ wqkv, const char* __restrict__ w1,
                                                      char* ws, const char* __restrict__ w2, const char* __restrict__ wo, char* __restrict__ out) {
  __shared__ __attribute__((aligned(16))) char lds_all[L_TOTAL];
  u32 stamp[32] = {};
  u64 pc;
  asm volatile("s_getpc_b64 %0" : "=s"(pc) : "s"((u32)(size_t)lds_all));
  STAMP(0);
  const u32 w = __builtin_amdgcn_readfirstlane(__builtin_amdgcn_workitem_id_x() >> 6);
  {
    u32 d0, d1, d2, d3;
    asm volatile("s_cmp_gt_u32 %6, 3\n\ts_cbranch_scc1 1f\n\ts_and_b32 %0, %5, -8\n\ts_lshl_b32 %1, %6, 1\n\ts_add_u32 %0, %0, %1\n\ts_min_u32 %0, %0, %7\n\t"
                 "s_lshl_b32 %0, %0, 7\n\ts_add_u32 %1, %0, 64\n\ts_add_u32 %2, %0, 128\n\ts_add_u32 %3, %0, 192\n\t"
                 "s_load_dword %0, %4, %0\n\ts_load_dword %1, %4, %1\n\ts_load_dword %2, %4, %2\n\ts_load_dword %3, %4, %3\n\ts_waitcnt lgkmcnt(0)\n1:"
                 : "=&s"(d0), "=&s"(d1), "=&s"(d2), "=&s"(d3) : "s"(pc), "s"(__builtin_amdgcn_workgroup_id_x()), "s"(w), "i"(CODE_LINES - 3) : "memory", "scc");
  }
  if (w < 4) group0(opaque_s(__builtin_amdgcn_workgroup_id_x()), opaque_v(__builtin_amdgcn_workitem_id_x()) & 63, w, stamp);
  else group1(__builtin_amdgcn_workgroup_id_x(), __builtin_amdgcn_workitem_id_x() & 63, w, stamp);
}

#if defined(__HIP_DEVICE_COMPILE__)
#pragma clang attribute pop
#endif

extern "C" void kernel_launch(void* const* d_in, const int* in_sizes, int n_in, void* d_out, int out_size, void* d_ws, size_t ws_size,
                              hipStream_t stream) {
  fused_block<<<256, 512, 0, stream>>>((const char*)d_in[0], (const char*)d_in[1], (const char*)d_in[4], (char*)d_ws + WS_OFF(ws_size), (const char*)d_in[5],
                                       (const char*)d_in[2], (char*)d_out);
}
```
